```python
import torch

def _softmax(x, axis=-1):
    return torch.softmax(x, dim=axis)
def _gelu(x):
    return torch.nn.functional.gelu(x, approximate="tanh")
def _silu(x):
    return torch.nn.functional.silu(x)
def _sigmoid(x):
    return torch.sigmoid(x)
def _layernorm(x, axis=-1, eps=1e-5):
    m = x.mean(dim=axis, keepdim=True)
    v = x.var(dim=axis, keepdim=True, unbiased=False)
    return (x - m) / torch.sqrt(v + eps)
def _scatter_add(tgt, idx, src, axis=0):
    return tgt.clone().index_add_(axis, idx, src)
def _segment_sum(src, seg, n):
    out = torch.zeros((n,) + src.shape[1:], dtype=src.dtype)
    return out.index_add_(0, seg, src)
def _pad(x, pw):
    flat = tuple(v for pair in reversed(pw) for v in pair)
    return torch.nn.functional.pad(x, flat)

kernel_name = 'concat_heads__mha_block_cached__mha_block_cached__near_ridge'

def setup_inputs():

    _w = (lambda a: (lambda t: t.to(torch.bfloat16) if t.is_floating_point() else t)(torch.as_tensor(a)))
    def _A_setup():
        B, Sq, D = 16, 192, 576
        H, Sk, Dh = 32, 512, 128
        return {
            "x": _w((lambda *s: torch.randn(*s, dtype=torch.bfloat16) * 0.8)(B, Sq, D)),
            "k_cache": _w((lambda *s: torch.randn(*s, dtype=torch.bfloat16) * 0.8)(B, H, Sk, Dh)),
            "v_cache": _w((lambda *s: torch.randn(*s, dtype=torch.bfloat16) * 0.05)(B, H, Sk, Dh)),
            "wq": _w((lambda *s: torch.randn(*s, dtype=torch.bfloat16) * 0.8)(D, H * Dh) * (D ** -0.5)),
            "wo": _w((lambda *s: torch.randn(*s, dtype=torch.bfloat16) * 0.05)(H * Dh, D) * ((H * Dh) ** -0.5)),
        }
    _A_in = _A_setup()

    def _B_setup():
        B, Sq, D = 16, 192, 576
        H, Sk, Dh = 32, 512, 128
        return {
            "x": _w((lambda *s: torch.randn(*s, dtype=torch.bfloat16) * 0.8)(B, Sq, D)),
            "k_cache": _w((lambda *s: torch.randn(*s, dtype=torch.bfloat16) * 0.8)(B, H, Sk, Dh)),
            "v_cache": _w((lambda *s: torch.randn(*s, dtype=torch.bfloat16) * 0.05)(B, H, Sk, Dh)),
            "wq": _w((lambda *s: torch.randn(*s, dtype=torch.bfloat16) * 0.8)(D, H * Dh) * (D ** -0.5)),
            "wo": _w((lambda *s: torch.randn(*s, dtype=torch.bfloat16) * 0.05)(H * Dh, D) * ((H * Dh) ** -0.5)),
        }
    _B_in = _B_setup()

    return {
        "A_x": _A_in["x"],
        "A_k_cache": _A_in["k_cache"],
        "A_v_cache": _A_in["v_cache"],
        "A_wq": _A_in["wq"],
        "A_wo": _A_in["wo"],
        "B_x": _B_in["x"],
        "B_k_cache": _B_in["k_cache"],
        "B_v_cache": _B_in["v_cache"],
        "B_wq": _B_in["wq"],
        "B_wo": _B_in["wo"],
    }

def reference(A_x, A_k_cache, A_v_cache, A_wq, A_wo, B_x, B_k_cache, B_v_cache, B_wq, B_wo):
    def _A_ref(x, k_cache, v_cache, wq, wo):
        B, Sq, D = x.shape
        H, Dh = 32, 128
        q = (lambda x, *d: x.permute(*d))((x @ wq).reshape(B, Sq, H, Dh), 0, 2, 1, 3)
        scores = torch.einsum("bhqd,bhkd->bhqk", q, k_cache) * (Dh ** -0.5)
        p = _softmax(scores, axis=-1)
        o = torch.einsum("bhqk,bhkd->bhqd", p, v_cache)
        return (lambda x, *d: x.permute(*d))(o, 0, 2, 1, 3).reshape(B, Sq, H * Dh) @ wo

    def _B_ref(x, k_cache, v_cache, wq, wo):
        B, Sq, D = x.shape
        H, Dh = 32, 128
        q = (lambda x, *d: x.permute(*d))((x @ wq).reshape(B, Sq, H, Dh), 0, 2, 1, 3)
        scores = torch.einsum("bhqd,bhkd->bhqk", q, k_cache) * (Dh ** -0.5)
        p = _softmax(scores, axis=-1)
        o = torch.einsum("bhqk,bhkd->bhqd", p, v_cache)
        return (lambda x, *d: x.permute(*d))(o, 0, 2, 1, 3).reshape(B, Sq, H * Dh) @ wo

    a = _A_ref(A_x, A_k_cache, A_v_cache, A_wq, A_wo)
    b = _B_ref(B_x, B_k_cache, B_v_cache, B_wq, B_wo)
    return torch.concatenate([a, b], axis=-1)
```

```cpp
#include <hip/hip_runtime.h>
#include <hip/hip_bf16.h>
#include <stdint.h>

constexpr int NB = 16, SQ = 192, DM = 576, NH = 32, SK = 512, DH = 128, HD = NH * DH;
constexpr float QSCALE = 0.088388347648318440f * 1.4426950408889634f;
constexpr float THR = 8.0f;

typedef __bf16   bf16x8 __attribute__((ext_vector_type(8)));
typedef __bf16   bf16x2 __attribute__((ext_vector_type(2)));
typedef short    s16x4  __attribute__((ext_vector_type(4)));
typedef short    s16x8  __attribute__((ext_vector_type(8)));
typedef float    f32x16 __attribute__((ext_vector_type(16)));
typedef float    f32x4  __attribute__((ext_vector_type(4)));
typedef float    f32x2  __attribute__((ext_vector_type(2)));
typedef unsigned u32x4  __attribute__((ext_vector_type(4)));
typedef unsigned u32x2  __attribute__((ext_vector_type(2)));
#define DI __device__ __forceinline__
#define LAS __attribute__((address_space(3)))
typedef LAS const char* lds_cptr;

DI unsigned rfl(unsigned x) { return __builtin_amdgcn_readfirstlane(x); }
DI unsigned cvtpk(float lo, float hi) { f32x2 v = {lo, hi}; bf16x2 b = __builtin_convertvector(v, bf16x2); return __builtin_bit_cast(unsigned, b); }
DI bf16x8 ldsb128(lds_cptr p) { return *(const LAS bf16x8*)p; }
DI s16x4 ldstr(lds_cptr p) { return __builtin_amdgcn_ds_read_tr16_b64_v4i16((LAS s16x4*)p); }
DI bf16x8 cat8(s16x4 lo, s16x4 hi) { s16x8 v = __builtin_shufflevector(lo, hi, 0, 1, 2, 3, 4, 5, 6, 7); return __builtin_bit_cast(bf16x8, v); }
#define MFMA32(a, b, c) __builtin_amdgcn_mfma_f32_32x32x16_bf16((a), (b), (c), 0, 0, 0)
#define MFMA16(a, b, c) __builtin_amdgcn_mfma_f32_16x16x32_bf16((a), (b), (c), 0, 0, 0)

DI void dma16(const char* sbase, unsigned voff, unsigned lds_dst) {
  asm volatile("s_mov_b32 m0, %0\n\ts_nop 0\n\tglobal_load_lds_dwordx4 %1, %2" :: "s"(lds_dst), "v"(voff), "s"(sbase) : "memory");
}
DI void dma16nt(const char* sbase, unsigned voff, unsigned lds_dst) {
  asm volatile("s_mov_b32 m0, %0\n\ts_nop 0\n\tglobal_load_lds_dwordx4 %1, %2 nt" :: "s"(lds_dst), "v"(voff), "s"(sbase) : "memory");
}
DI const char* sptr(const void* p) {
  uint64_t u = (uint64_t)p; unsigned lo = rfl((unsigned)u), hi = rfl((unsigned)(u >> 32));
  return (const char*)(((uint64_t)hi << 32) | lo);
}

struct Ptrs { const void* x[2]; const void* k[2]; const void* v[2]; const void* wq[2]; const void* wo[2]; };

__global__ __launch_bounds__(256) void k0_wot(const unsigned short* __restrict__ woA, const unsigned short* __restrict__ woB, unsigned short* __restrict__ wot) {
  const int gid = blockIdx.x * 256 + threadIdx.x;
  const int lane = gid & 63, frag = gid >> 6;
  const int ntile = frag % 36, kstep = (frag / 36) & 127, br = frag / (36 * 128);
  const unsigned short* wo = br ? woB : woA;
  const int n = 16 * ntile + (lane & 15), g = lane >> 4;
  unsigned short e[8];
#pragma unroll
  for (int j = 0; j < 8; ++j) e[j] = wo[(size_t)(32 * kstep + 16 * (g >> 1) + 8 * (j >> 2) + 4 * (g & 1) + (j & 3)) * DM + n];
  u32x4 w = {e[0] | ((unsigned)e[1] << 16), e[2] | ((unsigned)e[3] << 16), e[4] | ((unsigned)e[5] << 16), e[6] | ((unsigned)e[7] << 16)};
  *(u32x4*)(wot + (size_t)gid * 8) = w;
}

constexpr int K1_RING = 0, K1_SLOT = 16384, K1_XST = 3 * K1_SLOT, K1_LDS = K1_XST + 6 * 4096;

__global__ __launch_bounds__(384, 3) void k1_attn(Ptrs P, char* __restrict__ ows) {
  extern __shared__ __attribute__((aligned(16))) char lds[];
  const int tid = threadIdx.x, lane = tid & 63, r32 = lane & 31, hi = lane >> 5;
  const int wid = rfl(tid >> 6);
  const int bid = blockIdx.x, xcd = bid & 7, idx = bid >> 3, br = idx >> 6, rr = idx & 63;
  const int h = 8 * (xcd & 3) + (rr & 7), b = 8 * (xcd >> 2) + (rr >> 3);
  const char* xg = (const char*)(br ? P.x[1] : P.x[0]) + (size_t)(b * SQ + 32 * wid) * (DM * 2);
  const char* wg = (const char*)(br ? P.wq[1] : P.wq[0]) + h * (DH * 2);
  const char* kg = (const char*)(br ? P.k[1] : P.k[0]) + (size_t)(b * NH + h) * (SK * DH * 2);
  const char* vg = (const char*)(br ? P.v[1] : P.v[0]) + (size_t)(b * NH + h) * (SK * DH * 2);
  const unsigned lds0 = (unsigned)(uintptr_t)lds;
  const int prow = (lane >> 2) & 7, psub = lane >> 5, pslot = lane & 3, pbit = (lane >> 4) & 1;
  const bool three = wid < 4;

  f32x16 acc[4];
#pragma unroll
  for (int i = 0; i < 4; ++i) acc[i] = f32x16{};
  {
    const unsigned vx0 = prow * (DM * 2) + 64 * psub + 16 * (pslot ^ pbit), vx1 = vx0 ^ 32;
    const unsigned vw = prow * (HD * 2) + 64 * psub + 16 * pslot;
    const unsigned xdst = lds0 + K1_XST + 4096 * wid;
    auto dma_x = [&](int i) {
      const char* s = xg + 128 * i;
      dma16(s, vx0, xdst); dma16(s + 8 * DM * 2, vx1, xdst + 1024); dma16(s + 16 * DM * 2, vx0, xdst + 2048); dma16(s + 24 * DM * 2, vx1, xdst + 3072);
    };
    auto dma_w = [&](int i, unsigned slot) {
#pragma unroll
      for (int j = 0; j < 3; ++j) { const int p = wid + 6 * j;
        if (j < 2 || three) dma16(wg + (size_t)(64 * i + 8 * (p >> 1)) * (HD * 2) + 128 * (p & 1), vw, lds0 + slot + 1024 * p); }
    };
    const unsigned vk0 = prow * 256 + 64 * psub + 16 * (pslot ^ pbit), vv = prow * 256 + 64 * psub + 16 * pslot;
    auto dma_kv = [&](int t, unsigned slot) {
#pragma unroll
      for (int j = 0; j < 3; ++j) { const int p = wid + 6 * j, pp = p & 7;
        if (j < 2 || three) dma16nt((p < 8 ? kg : vg) + (32 * t + 8 * (pp >> 1)) * 256 + 128 * (pp & 1),
                                    p < 8 ? (vk0 ^ (((pp >> 1) & 1) << 5)) : vv, lds0 + slot + 1024 * p); }
    };
    const lds_cptr xrd0 = (lds_cptr)lds + K1_XST + 4096 * wid + 1024 * (r32 >> 3) + 64 * (r32 & 7) + 16 * (hi ^ ((r32 >> 2) & 3));
    const lds_cptr xrd1 = (lds_cptr)lds + K1_XST + 4096 * wid + 1024 * (r32 >> 3) + 64 * (r32 & 7) + 16 * ((2 + hi) ^ ((r32 >> 2) & 3));
    const int tq = (lane >> 2) & 3, tp = lane & 3, tblk = (lane >> 4) & 1, tsw = ((tp & 1) << 1) | (tp >> 1);
    const lds_cptr wrd = (lds_cptr)lds + 2048 * hi + 64 * tq + 32 * tblk + 8 * tsw;
    dma_x(0); dma_w(0, 0); dma_w(1, K1_SLOT);
    unsigned slot = 0, slot2 = 2 * K1_SLOT;
    for (int i = 0; i < 9; ++i) {
      if (three) asm volatile("s_waitcnt vmcnt(3) lgkmcnt(0)\n\ts_barrier" ::: "memory");
      else       asm volatile("s_waitcnt vmcnt(2) lgkmcnt(0)\n\ts_barrier" ::: "memory");
      bf16x8 xf[4];
      xf[0] = ldsb128(xrd0); xf[1] = ldsb128(xrd1); xf[2] = ldsb128(xrd0 + 512); xf[3] = ldsb128(xrd1 + 512);
      asm volatile("s_waitcnt lgkmcnt(0)" : "+v"(xf[0]), "+v"(xf[1]), "+v"(xf[2]), "+v"(xf[3]) :: "memory");
      if (i < 8) dma_x(i + 1);
      if (i < 7) dma_w(i + 2, slot2); else dma_kv(i - 7, slot2);
      const lds_cptr wp = wrd + slot;
#pragma unroll
      for (int ks = 0; ks < 4; ++ks)
#pragma unroll
        for (int db = 0; db < 4; ++db) {
          const bf16x8 a = cat8(ldstr(wp + 4096 * ks + 512 * db), ldstr(wp + 4096 * ks + 512 * db + 256));
          acc[db] = MFMA32(a, xf[ks], acc[db]);
        }
      slot = slot == 2 * K1_SLOT ? 0 : slot + K1_SLOT; slot2 = slot2 == 2 * K1_SLOT ? 0 : slot2 + K1_SLOT;
    }
    bf16x8 qf[8];
#pragma unroll
    for (int db = 0; db < 4; ++db)
#pragma unroll
      for (int s = 0; s < 2; ++s) {
        u32x4 w;
#pragma unroll
        for (int e = 0; e < 4; ++e) w[e] = cvtpk(acc[db][8 * s + 2 * e] * QSCALE, acc[db][8 * s + 2 * e + 1] * QSCALE);
        qf[2 * db + s] = __builtin_bit_cast(bf16x8, w);
      }
#pragma unroll
    for (int i = 0; i < 4; ++i) acc[i] = f32x16{};
    const lds_cptr krd0 = (lds_cptr)lds + 2048 * (r32 >> 3) + 64 * (r32 & 7) + 16 * (hi ^ ((r32 >> 2) & 3));
    const lds_cptr krd1 = (lds_cptr)lds + 2048 * (r32 >> 3) + 64 * (r32 & 7) + 16 * ((2 + hi) ^ ((r32 >> 2) & 3));
    const lds_cptr vrd = (lds_cptr)lds + 8192 + 256 * hi + 64 * tq + 32 * tblk + 8 * tp;
    f32x16 negm = f32x16{};
    float lsum = 0.f;
    for (int t = 0; t < 16; ++t) {
      if (t < 15) { if (three) asm volatile("s_waitcnt vmcnt(3) lgkmcnt(0)\n\ts_barrier" ::: "memory");
                    else       asm volatile("s_waitcnt vmcnt(2) lgkmcnt(0)\n\ts_barrier" ::: "memory"); }
      else asm volatile("s_waitcnt vmcnt(0) lgkmcnt(0)\n\ts_barrier" ::: "memory");
      if (t < 14) dma_kv(t + 2, slot2);
      f32x16 s = negm;
#pragma unroll
      for (int ks = 0; ks < 8; ++ks) s = MFMA32(ldsb128(((ks & 1) ? krd1 : krd0) + slot + 512 * (ks >> 1)), qf[ks], s);
      float mx = fmaxf(fmaxf(s[0], s[1]), s[2]);
#pragma unroll
      for (int r = 3; r < 15; r += 2) mx = fmaxf(fmaxf(mx, s[r]), s[r + 1]);
      mx = fmaxf(mx, s[15]);
      if (t == 0 || __builtin_expect(__any(!(mx <= THR)), 0)) {
        auto sw = __builtin_amdgcn_permlane32_swap(__float_as_uint(mx), __float_as_uint(mx), false, false);
        float dl = fmaxf(__uint_as_float(sw[0]), __uint_as_float(sw[1]));
        if (t != 0) {
          dl = fmaxf(dl, 0.f);
          const float f = __builtin_amdgcn_exp2f(-dl);
          lsum *= f;
#pragma unroll
          for (int db = 0; db < 4; ++db) acc[db] *= f;
        }
#pragma unroll
        for (int r = 0; r < 16; ++r) { s[r] -= dl; negm[r] -= dl; }
      }
#pragma unroll
      for (int r = 0; r < 16; ++r) { s[r] = __builtin_amdgcn_exp2f(s[r]); lsum += s[r]; }
      bf16x8 pf[2];
#pragma unroll
      for (int ss = 0; ss < 2; ++ss) {
        u32x4 w;
#pragma unroll
        for (int e = 0; e < 4; ++e) w[e] = cvtpk(s[8 * ss + 2 * e], s[8 * ss + 2 * e + 1]);
        pf[ss] = __builtin_bit_cast(bf16x8, w);
      }
      const lds_cptr vp = vrd + slot;
#pragma unroll
      for (int ss = 0; ss < 2; ++ss)
#pragma unroll
        for (int db = 0; db < 4; ++db) {
          const bf16x8 a = cat8(ldstr(vp + 4096 * ss + 512 * db), ldstr(vp + 4096 * ss + 2048 + 512 * db));
          acc[db] = MFMA32(a, pf[ss], acc[db]);
        }
      slot = slot == 2 * K1_SLOT ? 0 : slot + K1_SLOT; slot2 = slot2 == 2 * K1_SLOT ? 0 : slot2 + K1_SLOT;
    }
    auto sw = __builtin_amdgcn_permlane32_swap(__float_as_uint(lsum), __float_as_uint(lsum), false, false);
    const float inv = 1.0f / (__uint_as_float(sw[0]) + __uint_as_float(sw[1]));
    const int mtile = (br * NB + b) * 12 + 2 * wid + (r32 >> 4);
    char* op = ows + ((size_t)mtile * 128 + 4 * h) * 1024 + 16 * (r32 & 15) + 256 * hi;
#pragma unroll
    for (int db = 0; db < 4; ++db)
#pragma unroll
      for (int ss = 0; ss < 2; ++ss) {
        u32x4 w;
#pragma unroll
        for (int e = 0; e < 4; ++e) w[e] = cvtpk(acc[db][8 * ss + 2 * e] * inv, acc[db][8 * ss + 2 * e + 1] * inv);
        *(u32x4*)(op + 1024 * db + 512 * ss) = w;
      }
  }
}

constexpr int K2_LDS = 4 * 27 * 1024;
__global__ __launch_bounds__(256, 1) void k2_oproj(const char* __restrict__ ows, const char* __restrict__ wot, __hip_bfloat16* __restrict__ out) {
  extern __shared__ __attribute__((aligned(16))) char lds[];
  const int tid = threadIdx.x, lane = tid & 63; const int wid = rfl(tid >> 6);
  const int bid = blockIdx.x, xcd = bid & 7, idx = bid >> 3, br = xcd >> 2, mg = (xcd & 3) * 8 + (idx >> 2), ng = idx & 3;
  const char* ap = ows + ((size_t)(br * 192 + mg * 6) * 128) * 1024 + lane * 16;
  const char* bp = wot + ((size_t)br * 128 * 36 + 9 * ng) * 1024 + lane * 16;
  f32x4 acc[6][9];
#pragma unroll
  for (int m = 0; m < 6; ++m)
#pragma unroll
    for (int n = 0; n < 9; ++n) acc[m][n] = f32x4{};
  constexpr int PD = 3;
  bf16x8 af[PD][6], bf[PD][9];
  auto ld = [&](int buf, int it) {
    const int kstep = 4 * it + wid;
#pragma unroll
    for (int m = 0; m < 6; ++m) af[buf][m] = *(const bf16x8*)(ap + (size_t)(m * 128 + kstep) * 1024);
#pragma unroll
    for (int n = 0; n < 9; ++n) bf[buf][n] = *(const bf16x8*)(bp + (size_t)(kstep * 36 + n) * 1024);
  };
  auto mm = [&](int buf) {
#pragma unroll
    for (int m = 0; m < 6; ++m)
#pragma unroll
      for (int n = 0; n < 9; ++n) acc[m][n] = MFMA16(bf[buf][n], af[buf][m], acc[m][n]);
  };
  ld(0, 0); ld(1, 1);
  for (int it = 0; it < 30; it += 3) {
    ld(2, it + 2); mm(0);
    ld(0, it + 3); mm(1);
    ld(1, it + 4); mm(2);
  }
  mm(0); mm(1);
#pragma unroll
  for (int rd = 0; rd < 2; ++rd) {
    if (rd) __syncthreads();
#pragma unroll
    for (int m = 0; m < 3; ++m)
#pragma unroll
      for (int n = 0; n < 9; ++n) *(f32x4*)(lds + ((wid * 27 + m * 9 + n) * 64 + lane) * 16) = acc[3 * rd + m][n];
    __syncthreads();
    for (int u = tid; u < 864; u += 256) {
      const int row = u / 18, c8 = u % 18, m = row >> 4, mi = row & 15, n = c8 >> 1, g = (c8 & 1) * 2;
      f32x4 s0 = f32x4{}, s1 = f32x4{};
#pragma unroll
      for (int w = 0; w < 4; ++w) {
        const char* p = lds + ((w * 27 + m * 9 + n) * 64 + mi + 16 * g) * 16;
        s0 += *(const f32x4*)p; s1 += *(const f32x4*)(p + 256);
      }
      u32x4 o = {cvtpk(s0[0], s0[1]), cvtpk(s0[2], s0[3]), cvtpk(s1[0], s1[1]), cvtpk(s1[2], s1[3])};
      *(u32x4*)(out + (size_t)(mg * 96 + 48 * rd + row) * (2 * DM) + br * DM + 144 * ng + 8 * c8) = o;
    }
  }
}

extern "C" void kernel_launch(void* const* d_in, const int* in_sizes, int n_in, void* d_out, int out_size, void* d_ws, size_t ws_size, hipStream_t stream) {
  Ptrs P;
  for (int i = 0; i < 2; ++i) { P.x[i] = d_in[5 * i + 0]; P.k[i] = d_in[5 * i + 1]; P.v[i] = d_in[5 * i + 2]; P.wq[i] = d_in[5 * i + 3]; P.wo[i] = d_in[5 * i + 4]; }
  char* ows = (char*)d_ws;
  char* wot = ows + (size_t)2 * NB * SQ * HD * 2;
  (void)hipFuncSetAttribute((const void*)k1_attn, hipFuncAttributeMaxDynamicSharedMemorySize, K1_LDS);
  (void)hipFuncSetAttribute((const void*)k2_oproj, hipFuncAttributeMaxDynamicSharedMemorySize, K2_LDS);
  hipLaunchKernelGGL(k0_wot, dim3(2 * 128 * 36 * 64 / 256), dim3(256), 0, stream, (const unsigned short*)P.wo[0], (const unsigned short*)P.wo[1], (unsigned short*)wot);
  hipLaunchKernelGGL(k1_attn, dim3(1024), dim3(384), K1_LDS, stream, P, ows);
  hipLaunchKernelGGL(k2_oproj, dim3(256), dim3(256), K2_LDS, stream, ows, wot, (__hip_bfloat16*)d_out);
}
```

```cpp
#include <hip/hip_runtime.h>
#include <hip/hip_bf16.h>
#include <stdint.h>

constexpr int NB = 16, SQ = 192, DM = 576, NH = 32, SK = 512, DH = 128, HD = NH * DH;
constexpr float QSCALE = 0.088388347648318440f * 1.4426950408889634f;

typedef __bf16   bf16x8 __attribute__((ext_vector_type(8)));
typedef __bf16   bf16x2 __attribute__((ext_vector_type(2)));
typedef float    f32x4  __attribute__((ext_vector_type(4)));
typedef float    f32x2  __attribute__((ext_vector_type(2)));
typedef unsigned u32x4  __attribute__((ext_vector_type(4)));
typedef unsigned u32x2  __attribute__((ext_vector_type(2)));
typedef unsigned short u16;
typedef unsigned long long u64;
#define DI __device__ __forceinline__

DI unsigned rfl(unsigned x) { return __builtin_amdgcn_readfirstlane(x); }
DI unsigned cvtpk(float lo, float hi) { f32x2 v = {lo, hi}; bf16x2 b = __builtin_convertvector(v, bf16x2); return __builtin_bit_cast(unsigned, b); }
#define MFMA16(a, b, c) __builtin_amdgcn_mfma_f32_16x16x32_bf16((a), (b), (c), 0, 0, 0)

struct Ptrs { const void* x[2]; const void* k[2]; const void* v[2]; const void* wq[2]; const void* wo[2]; };

DI bf16x8 gather8(const u16* p, int lo_stride, int hi_off) {
  u32x4 w;
#pragma unroll
  for (int e = 0; e < 4; ++e) {
    const int j0 = 2 * e, j1 = 2 * e + 1;
    w[e] = p[(j0 & 3) * lo_stride + (j0 >> 2) * hi_off] | ((unsigned)p[(j1 & 3) * lo_stride + (j1 >> 2) * hi_off] << 16);
  }
  return __builtin_bit_cast(bf16x8, w);
}
DI void slow_rows(const u16* x, const u16* wq, const u16* k, const u16* v, char* o, unsigned lane) {
  const int l16 = lane & 15, g = lane >> 4;
  f32x4 acc[8];
#pragma unroll
  for (int i = 0; i < 8; ++i) acc[i] = f32x4{};
  for (int c = 0; c < DM; c += 32) {
    const bf16x8 xb = *(const bf16x8*)(x + l16 * DM + c + 8 * g);
#pragma unroll
    for (int db = 0; db < 8; ++db) acc[db] = MFMA16(gather8(wq + (size_t)(c + 8 * g) * HD + 16 * db + l16, HD, 4 * HD), xb, acc[db]);
  }
  bf16x8 qf[4];
#pragma unroll
  for (int ks = 0; ks < 4; ++ks) {
    u32x4 w = {cvtpk(acc[2 * ks][0] * QSCALE, acc[2 * ks][1] * QSCALE), cvtpk(acc[2 * ks][2] * QSCALE, acc[2 * ks][3] * QSCALE),
               cvtpk(acc[2 * ks + 1][0] * QSCALE, acc[2 * ks + 1][1] * QSCALE), cvtpk(acc[2 * ks + 1][2] * QSCALE, acc[2 * ks + 1][3] * QSCALE)};
    qf[ks] = __builtin_bit_cast(bf16x8, w);
  }
#pragma unroll
  for (int i = 0; i < 8; ++i) acc[i] = f32x4{};
  float m = -INFINITY, l = 0.f;
  for (int t = 0; t < SK; t += 32) {
    f32x4 s[2];
#pragma unroll
    for (int kb = 0; kb < 2; ++kb) {
      s[kb] = f32x4{};
#pragma unroll
      for (int ks = 0; ks < 4; ++ks) s[kb] = MFMA16(gather8(k + (t + 16 * kb + l16) * DH + 32 * ks + 4 * g, 1, 16), qf[ks], s[kb]);
    }
    float mx = fmaxf(fmaxf(fmaxf(s[0][0], s[0][1]), fmaxf(s[0][2], s[0][3])), fmaxf(fmaxf(s[1][0], s[1][1]), fmaxf(s[1][2], s[1][3])));
    mx = fmaxf(mx, __shfl_xor(mx, 16)); mx = fmaxf(mx, __shfl_xor(mx, 32));
    const float mn = fmaxf(m, mx), f = __builtin_amdgcn_exp2f(m - mn);
    m = mn; l *= f;
#pragma unroll
    for (int i = 0; i < 8; ++i) acc[i] *= f;
#pragma unroll
    for (int kb = 0; kb < 2; ++kb)
#pragma unroll
      for (int r = 0; r < 4; ++r) { s[kb][r] = __builtin_amdgcn_exp2f(s[kb][r] - m); l += s[kb][r]; }
    u32x4 w = {cvtpk(s[0][0], s[0][1]), cvtpk(s[0][2], s[0][3]), cvtpk(s[1][0], s[1][1]), cvtpk(s[1][2], s[1][3])};
    const bf16x8 pf = __builtin_bit_cast(bf16x8, w);
#pragma unroll
    for (int db = 0; db < 8; ++db) acc[db] = MFMA16(gather8(v + (t + 4 * g) * DH + 16 * db + l16, DH, 16 * DH), pf, acc[db]);
  }
  l += __shfl_xor(l, 16); l += __shfl_xor(l, 32);
  const float inv = 1.0f / l;
#pragma unroll
  for (int kk = 0; kk < 4; ++kk) {
    u32x4 w = {cvtpk(acc[2 * kk][0] * inv, acc[2 * kk][1] * inv), cvtpk(acc[2 * kk][2] * inv, acc[2 * kk][3] * inv),
               cvtpk(acc[2 * kk + 1][0] * inv, acc[2 * kk + 1][1] * inv), cvtpk(acc[2 * kk + 1][2] * inv, acc[2 * kk + 1][3] * inv)};
    *(u32x4*)(o + 6144 * kk + 16 * lane) = w;
  }
}

#define K_STAMP 0
constexpr int K_LDS = 163840;
__global__ __launch_bounds__(256, 1) void k1_fused(Ptrs P, char* __restrict__ ows, char* __restrict__ wot, char* __restrict__ cnt, char* __restrict__ out) {
  const unsigned w = rfl(threadIdx.x >> 6), it = w >> 1, qh = w & 1;
  const unsigned bid = blockIdx.x, xcd = bid & 7, idx = bid >> 3;
  const unsigned br = xcd >> 2, grp = 4 * (xcd & 1) + (idx & 3), b = 8 * ((xcd >> 1) & 1) + (idx >> 2), h0 = 4 * grp + 2 * it;
  const char* pk = (const char*)(br ? P.k[1] : P.k[0]) + (size_t)(b * NH + h0) * (SK * DH * 2);
  const char* pv = (const char*)(br ? P.v[1] : P.v[0]) + (size_t)(b * NH + h0) * (SK * DH * 2);
  const char* pw = (const char*)(br ? P.wq[1] : P.wq[0]) + 4 * grp * (DH * 2);
  const char* px = (const char*)(br ? P.x[1] : P.x[0]) + (size_t)b * (SQ * DM * 2);
  char* po = ows + (size_t)((br * NB + b) * 2 + qh) * 786432;
  const unsigned oo = (32 * (grp >> 1) + 4 * (2 * (grp & 1) + it)) * 6144, tw = (36 * b + 9 * w) * 8192 + 256;
  const unsigned rot = ((b * 5 + grp * 2 + it + br * 7) & 15) << 13;
  const int ct = (idx & 3) == w ? (int)(idx >> 2) * 8192 : -1;
  const unsigned wj = b * 8 + grp, kr = wj >> 1, tng = 2 * (wj & 1) + it, tkp = 32 * (kr >> 4) + (kr & 15);
  const char* pwo = (const char*)(br ? P.wo[1] : P.wo[0]) + (size_t)((8 * (kr >> 4) + 2 * ((kr >> 2) & 3)) * DH + 32 * (kr & 3)) * (DM * 2) + (144 * tng + 64 * qh) * 2;
  char* pwt = wot + (size_t)((br * 4 + tng) * 128 + tkp) * 9216 + 4096 * qh;
  const unsigned mg = 2 * b + (grp >> 2), ng = grp & 3;
  const char* pa = ows + (size_t)(br * 32 + mg) * 786432 + w * (32 * 6144);
  const char* pb = wot + (size_t)(br * 4 + ng) * (128 * 9216) + w * (32 * 9216);
  char* pc = out + (size_t)mg * (96 * 2304) + br * (DM * 2) + ng * 288;
  char* pcnt = cnt;
  const unsigned eo = 128 * br;
  const unsigned uo = 512 + (((br * 16 + b) * 2 + qh) * 4 + (grp >> 1)) * 512 + (2 * (grp & 1) + it) * 8;
  const unsigned uw = 512 + (256 + (br * 4 + (kr >> 4)) * 4 + tng) * 512 + (2 * (kr & 15) + qh) * 8;
  const unsigned co = 512 + (((br * 16 + b) * 2 + (grp >> 2)) * 4 + w) * 512, cw = 512 + (256 + (br * 4 + w) * 4 + ng) * 512;
  unsigned flag;
  asm volatile(
    "s_nop 4\n"
    "v_mbcnt_lo_u32_b32 v255, -1, 0\n"
    "v_mbcnt_hi_u32_b32 v255, -1, v255\n"
    "v_lshlrev_b32 v242, 4, v255\n"
    "s_mov_b64 s[78:79], 0\n"
    "s_mov_b32 s83, 3\n"
    "s_mov_b64 s[44:45], %[pw]\n"
    "s_mov_b32 s46, 4711424\n"
    "s_mov_b32 s47, 0x20000\n"
    "s_mov_b64 s[48:49], %[px]\n"
    "s_mov_b32 s50, 221184\n"
    "s_mov_b32 s51, 0x20000\n"
    "s_lshr_b32 s62, %[wv], 1\n"
    "s_and_b32 s63, %[wv], 1\n"
    "s_lshl_b32 s65, s62, 13\n"
    "s_lshl_b32 s59, s63, 10\n"
    "s_add_u32 s65, s65, s59\n"
    "s_mul_i32 s69, %[wv], 6144\n"
    "s_mul_i32 s61, %[wv], 55296\n"
    "s_mov_b32 s58, 0\n"
    "s_lshl_b32 s59, s63, 7\n"
    "s_lshl_b32 s60, s62, 9\n"
    "s_add_u32 s60, s60, s59\n"
    "v_bfe_u32 v206, v255, 2, 3\n"
    "v_mul_u32_u24 v206, 1152, v206\n"
    "v_bfe_u32 v243, v255, 5, 1\n"
    "v_lshl_add_u32 v206, v243, 6, v206\n"
    "v_bfe_u32 v243, v255, 0, 2\n"
    "v_lshl_add_u32 v206, v243, 4, v206\n"
    "v_xor_b32 v207, 16, v206\n"
    "v_bfe_u32 v204, v255, 2, 3\n"
    "v_lshlrev_b32 v204, 13, v204\n"
    "v_bfe_u32 v243, v255, 5, 1\n"
    "v_lshl_add_u32 v204, v243, 6, v204\n"
    "v_bfe_u32 v243, v255, 0, 2\n"
    "v_lshl_add_u32 v204, v243, 4, v204\n"
    "v_add_u32 v204, s60, v204\n"
    "v_xor_b32 v205, 32, v204\n"
    "v_bfe_u32 v201, v255, 2, 3\n"
    "v_lshlrev_b32 v201, 8, v201\n"
    "v_bfe_u32 v243, v255, 5, 1\n"
    "v_lshl_add_u32 v201, v243, 6, v201\n"
    "v_bfe_u32 v243, v255, 0, 2\n"
    "v_lshl_add_u32 v201, v243, 4, v201\n"
    "v_add_u32 v201, s59, v201\n"
    "s_add_u32 m0, s69, 0\n"
    "s_add_u32 s59, s61, 0\n"
    "s_nop 0\n"
    "buffer_load_dwordx4 v206, s[48:51], s59 offen lds\n"
    "s_add_u32 m0, s69, 1024\n"
    "s_add_u32 s59, s61, 9216\n"
    "s_nop 0\n"
    "buffer_load_dwordx4 v207, s[48:51], s59 offen lds\n"
    "s_add_u32 m0, s69, 2048\n"
    "s_add_u32 s59, s61, 18432\n"
    "s_nop 0\n"
    "buffer_load_dwordx4 v206, s[48:51], s59 offen lds\n"
    "s_add_u32 m0, s69, 3072\n"
    "s_add_u32 s59, s61, 27648\n"
    "s_nop 0\n"
    "buffer_load_dwordx4 v207, s[48:51], s59 offen lds\n"
    "s_add_u32 m0, s69, 4096\n"
    "s_add_u32 s59, s61, 36864\n"
    "s_nop 0\n"
    "buffer_load_dwordx4 v206, s[48:51], s59 offen lds\n"
    "s_add_u32 m0, s69, 5120\n"
    "s_add_u32 s59, s61, 46080\n"
    "s_nop 0\n"
    "buffer_load_dwordx4 v207, s[48:51], s59 offen lds\n"
    "s_add_u32 m0, s65, 32768\n"
    "s_nop 0\n"
    "buffer_load_dwordx4 v204, s[44:47], s58 offen lds\n"
    "s_add_u32 s58, s58, 0x10000\n"
    "s_add_u32 m0, s65, 34816\n"
    "s_nop 0\n"
    "buffer_load_dwordx4 v204, s[44:47], s58 offen lds\n"
    "s_add_u32 s58, s58, 0x10000\n"
    "s_add_u32 m0, s65, 36864\n"
    "s_nop 0\n"
    "buffer_load_dwordx4 v205, s[44:47], s58 offen lds\n"
    "s_add_u32 s58, s58, 0x10000\n"
    "s_add_u32 m0, s65, 38912\n"
    "s_nop 0\n"
    "buffer_load_dwordx4 v205, s[44:47], s58 offen lds\n"
    "s_add_u32 s58, s58, 0x10000\n"
    "s_add_u32 m0, s65, 49152\n"
    "s_nop 0\n"
    "buffer_load_dwordx4 v204, s[44:47], s58 offen lds\n"
    "s_add_u32 s58, s58, 0x10000\n"
    "s_add_u32 m0, s65, 51200\n"
    "s_nop 0\n"
    "buffer_load_dwordx4 v204, s[44:47], s58 offen lds\n"
    "s_add_u32 s58, s58, 0x10000\n"
    "s_add_u32 m0, s65, 53248\n"
    "s_nop 0\n"
    "buffer_load_dwordx4 v205, s[44:47], s58 offen lds\n"
    "s_add_u32 s58, s58, 0x10000\n"
    "s_add_u32 m0, s65, 55296\n"
    "s_nop 0\n"
    "buffer_load_dwordx4 v205, s[44:47], s58 offen lds\n"
    "s_add_u32 s58, s58, 0x10000\n"
    "s_add_u32 m0, s69, 114688\n"
    "s_add_u32 s59, s61, 128\n"
    "s_nop 0\n"
    "buffer_load_dwordx4 v206, s[48:51], s59 offen lds\n"
    "s_add_u32 m0, s69, 115712\n"
    "s_add_u32 s59, s61, 9344\n"
    "s_nop 0\n"
    "buffer_load_dwordx4 v207, s[48:51], s59 offen lds\n"
    "s_add_u32 m0, s69, 116736\n"
    "s_add_u32 s59, s61, 18560\n"
    "s_nop 0\n"
    "buffer_load_dwordx4 v206, s[48:51], s59 offen lds\n"
    "s_add_u32 m0, s69, 117760\n"
    "s_add_u32 s59, s61, 27776\n"
    "s_nop 0\n"
    "buffer_load_dwordx4 v207, s[48:51], s59 offen lds\n"
    "s_add_u32 m0, s69, 118784\n"
    "s_add_u32 s59, s61, 36992\n"
    "s_nop 0\n"
    "buffer_load_dwordx4 v206, s[48:51], s59 offen lds\n"
    "s_add_u32 m0, s69, 119808\n"
    "s_add_u32 s59, s61, 46208\n"
    "s_nop 0\n"
    "buffer_load_dwordx4 v207, s[48:51], s59 offen lds\n"
    "s_add_u32 m0, s65, 98304\n"
    "s_nop 0\n"
    "buffer_load_dwordx4 v204, s[44:47], s58 offen lds\n"
    "s_add_u32 s58, s58, 0x10000\n"
    "s_add_u32 m0, s65, 100352\n"
    "s_nop 0\n"
    "buffer_load_dwordx4 v204, s[44:47], s58 offen lds\n"
    "s_add_u32 s58, s58, 0x10000\n"
    "s_add_u32 m0, s65, 102400\n"
    "s_nop 0\n"
    "buffer_load_dwordx4 v205, s[44:47], s58 offen lds\n"
    "s_add_u32 s58, s58, 0x10000\n"
    "s_add_u32 m0, s65, 104448\n"
    "s_nop 0\n"
    "buffer_load_dwordx4 v205, s[44:47], s58 offen lds\n"
    "s_add_u32 s58, s58, 0x10000\n"
    "s_add_u32 m0, s65, 65536\n"
    "s_nop 0\n"
    "buffer_load_dwordx4 v204, s[44:47], s58 offen lds\n"
    "s_add_u32 s58, s58, 0x10000\n"
    "s_add_u32 m0, s65, 67584\n"
    "s_nop 0\n"
    "buffer_load_dwordx4 v204, s[44:47], s58 offen lds\n"
    "s_add_u32 s58, s58, 0x10000\n"
    "s_add_u32 m0, s65, 69632\n"
    "s_nop 0\n"
    "buffer_load_dwordx4 v205, s[44:47], s58 offen lds\n"
    "s_add_u32 s58, s58, 0x10000\n"
    "s_add_u32 m0, s65, 71680\n"
    "s_nop 0\n"
    "buffer_load_dwordx4 v205, s[44:47], s58 offen lds\n"
    "s_add_u32 s58, s58, 0x10000\n"
    "s_cmp_lt_i32 %[ct], 0\n"
    "s_cbranch_scc1 L_noct%=\n"
    "s_getpc_b64 s[36:37]\n"
    "v_lshlrev_b32 v244, 7, v255\n"
    "v_add_u32 v244, %[ct], v244\n"
    "global_load_dword v246, v244, s[36:37]\n"
    "L_noct%=:\n"
    "v_mov_b32 v243, %[eo]\n"
    "global_load_dwordx2 v[248:249], v243, %[pcnt] sc1\n"
    "s_mov_b64 s[36:37], %[pk]\n"
    "s_mov_b32 s38, 262144\n"
    "s_mov_b32 s39, 0x20000\n"
    "s_mov_b64 s[40:41], %[pv]\n"
    "s_mov_b32 s42, 262144\n"
    "s_mov_b32 s43, 0x20000\n"
    "s_mov_b64 s[52:53], %[po]\n"
    "s_mov_b32 s54, 786432\n"
    "s_mov_b32 s55, 0x20000\n"
    "s_mov_b64 s[84:85], %[pwo]\n"
    "s_mov_b32 s86, 183328\n"
    "s_mov_b32 s87, 0x20000\n"
    "s_mov_b64 s[88:89], %[pwt]\n"
    "s_mov_b32 s90, 152576\n"
    "s_mov_b32 s91, 0x20000\n"
    "s_add_u32 s66, s65, 0x1000\n"
    "s_add_u32 s67, s65, 0x10000\n"
    "s_add_u32 s68, s65, 0x11000\n"
    "s_mov_b32 s72, %[oo]\n"
    "s_mov_b32 s56, %[rot]\n"
    "s_mov_b32 s57, s56\n"
    "s_mov_b32 s92, 0\n"
    "s_mov_b32 s93, 0\n"
    "s_mov_b32 s74, 0\n"
    "s_mul_i32 s77, %[wv], 5120\n"
    "s_add_u32 s77, s77, 0x20000\n"
    "s_mov_b32 s71, 0\n"
    "s_mov_b32 s75, 0x53800000\n"
    "s_mov_b32 s76, 0x3e0293ee\n"
    "v_bfe_u32 v192, v255, 0, 3\n"
    "v_lshlrev_b32 v192, 6, v192\n"
    "v_bfe_u32 v243, v255, 4, 1\n"
    "v_lshl_add_u32 v192, v243, 5, v192\n"
    "v_bfe_u32 v243, v255, 5, 1\n"
    "v_lshl_add_u32 v192, v243, 4, v192\n"
    "v_bfe_u32 v199, v255, 3, 1\n"
    "v_lshlrev_b32 v199, 4, v199\n"
    "v_xor_b32 v192, v199, v192\n"
    "v_lshl_add_u32 v200, v199, 6, v192\n"
    "v_lshl_add_u32 v192, v199, 7, v192\n"
    "s_lshl_b32 s59, s62, 13\n"
    "s_mul_i32 s60, s63, 12288\n"
    "v_add_u32 v192, s59, v192\n"
    "v_add_u32 v200, s60, v200\n"
    "v_add_u32 v199, 114688, v200\n"
    "s_add_u32 s60, s59, 0x8000\n"
    "v_bfe_u32 v195, v255, 4, 1\n"
    "v_mul_u32_u24 v195, 4128, v195\n"
    "v_bfe_u32 v243, v255, 5, 1\n"
    "v_lshl_add_u32 v195, v243, 11, v195\n"
    "v_bfe_u32 v243, v255, 2, 2\n"
    "v_lshl_add_u32 v195, v243, 6, v195\n"
    "v_bfe_u32 v243, v255, 0, 2\n"
    "v_lshl_add_u32 v195, v243, 3, v195\n"
    "v_add_u32 v195, s60, v195\n"
    "v_xor_b32 v196, 32, v195\n"
    "v_add_u32 v197, 0x10000, v195\n"
    "v_xor_b32 v198, 32, v197\n"
    "s_waitcnt vmcnt(19) lgkmcnt(0)\n"
    "s_barrier\n"
    "ds_read_b128 a[192:195], v200 offset:0\n"
    "ds_read_b64_tr_b16 a[216:217], v195 offset:0\n"
    "ds_read_b64_tr_b16 a[218:219], v195 offset:256\n"
    "ds_read_b128 a[196:199], v200 offset:2048\n"
    "ds_read_b128 a[200:203], v200 offset:4096\n"
    "ds_read_b128 a[204:207], v200 offset:6144\n"
    "ds_read_b128 a[208:211], v200 offset:8192\n"
    "ds_read_b128 a[212:215], v200 offset:10240\n"
    "ds_read_b64_tr_b16 a[220:221], v196 offset:0\n"
    "ds_read_b64_tr_b16 a[222:223], v196 offset:256\n"
    "s_add_u32 s60, s59, 0x10000\n"
    "v_bfe_u32 v193, v255, 5, 1\n"
    "v_lshlrev_b32 v193, 11, v193\n"
    "v_bfe_u32 v243, v255, 4, 1\n"
    "v_mul_u32_u24 v243, 288, v243\n"
    "v_add_u32 v193, v243, v193\n"
    "v_bfe_u32 v243, v255, 2, 2\n"
    "v_lshl_add_u32 v193, v243, 6, v193\n"
    "v_bfe_u32 v243, v255, 0, 2\n"
    "v_lshl_add_u32 v193, v243, 3, v193\n"
    "v_add_u32 v193, s60, v193\n"
    "v_xor_b32 v194, 32, v193\n"
    "v_xor_b32 v202, 16, v201\n"
    "v_bfe_u32 v203, v255, 4, 1\n"
    "v_lshlrev_b32 v203, 5, v203\n"
    "v_xor_b32 v203, v203, v201\n"
    "v_bfe_u32 v208, v255, 1, 5\n"
    "v_mul_u32_u24 v208, 1152, v208\n"
    "v_bfe_u32 v243, v255, 0, 1\n"
    "v_lshl_add_u32 v208, v243, 4, v208\n"
    "v_bfe_u32 v209, v255, 2, 4\n"
    "v_lshlrev_b32 v209, 5, v209\n"
    "v_bfe_u32 v243, v255, 0, 2\n"
    "v_lshl_add_u32 v209, v243, 3, v209\n"
    "v_add_u32 v209, s77, v209\n"
    "v_bfe_u32 v210, v255, 2, 4\n"
    "v_lshlrev_b32 v210, 13, v210\n"
    "v_bfe_u32 v243, v255, 0, 1\n"
    "v_lshl_add_u32 v210, v243, 7, v210\n"
    "v_bfe_u32 v243, v255, 1, 1\n"
    "v_lshl_add_u32 v210, v243, 9, v210\n"
    "v_mov_b32 v240, s76\n"
    "s_mov_b32 vcc_lo, 0\n"
    "s_mov_b32 vcc_hi, -1\n"
    "v_bfe_u32 v241, v255, 0, 5\n"
    "v_lshlrev_b32 v241, 3, v241\n"
    "v_add_u32 v241, %[cw], v241\n"
    "v_bfe_u32 v253, v255, 0, 2\n"
    "v_lshlrev_b32 v253, 3, v253\n"
    "v_add_u32 v253, %[co], v253\n"
    "v_cndmask_b32 v241, v241, v253, vcc\n"
    "s_mov_b64 vcc, 2\n"
    "v_mov_b32 v252, %[uo]\n"
    "v_mov_b32 v243, %[uw]\n"
    "v_cndmask_b32 v252, v252, v243, vcc\n"
    "s_waitcnt lgkmcnt(2)\n"
    "v_mfma_f32_16x16x32_bf16 v[0:3], a[216:219], a[192:195], 0\n"
    "ds_read_b64_tr_b16 a[252:253], v196 offset:512\n"
    "v_mfma_f32_16x16x32_bf16 v[32:35], a[216:219], a[196:199], 0\n"
    "ds_read_b64_tr_b16 a[248:249], v195 offset:512\n"
    "v_mfma_f32_16x16x32_bf16 v[64:67], a[216:219], a[200:203], 0\n"
    "ds_read_b64_tr_b16 a[254:255], v196 offset:768\n"
    "v_mfma_f32_16x16x32_bf16 v[96:99], a[216:219], a[204:207], 0\n"
    "ds_read_b64_tr_b16 a[250:251], v195 offset:768\n"
    "v_mfma_f32_16x16x32_bf16 v[128:131], a[216:219], a[208:211], 0\n"
    "s_add_u32 m0, s65, 81920\n"
    "v_mfma_f32_16x16x32_bf16 v[160:163], a[216:219], a[212:215], 0\n"
    "buffer_load_dwordx4 v204, s[44:47], s58 offen lds\n"
    "s_waitcnt lgkmcnt(4)\n"
    "v_mfma_f32_16x16x32_bf16 v[4:7], a[220:223], a[192:195], 0\n"
    "ds_read_b64_tr_b16 a[216:217], v195 offset:1024\n"
    "s_add_u32 s58, s58, 0x10000\n"
    "s_add_u32 m0, s65, 83968\n"
    "v_mfma_f32_16x16x32_bf16 v[36:39], a[220:223], a[196:199], 0\n"
    "buffer_load_dwordx4 v204, s[44:47], s58 offen lds\n"
    "v_mfma_f32_16x16x32_bf16 v[68:71], a[220:223], a[200:203], 0\n"
    "ds_read_b64_tr_b16 a[218:219], v195 offset:1280\n"
    "s_add_u32 s58, s58, 0x10000\n"
    "v_mfma_f32_16x16x32_bf16 v[100:103], a[220:223], a[204:207], 0\n"
    "s_add_u32 m0, s65, 86016\n"
    "v_mfma_f32_16x16x32_bf16 v[132:135], a[220:223], a[208:211], 0\n"
    "buffer_load_dwordx4 v205, s[44:47], s58 offen lds\n"
    "v_mfma_f32_16x16x32_bf16 v[164:167], a[220:223], a[212:215], 0\n"
    "s_add_u32 s58, s58, 0x10000\n"
    "s_waitcnt lgkmcnt(2)\n"
    "v_mfma_f32_16x16x32_bf16 v[8:11], a[248:251], a[192:195], 0\n"
    "ds_read_b64_tr_b16 a[220:221], v196 offset:1024\n"
    "s_add_u32 m0, s65, 88064\n"
    "v_mfma_f32_16x16x32_bf16 v[40:43], a[248:251], a[196:199], 0\n"
    "buffer_load_dwordx4 v205, s[44:47], s58 offen lds\n"
    "v_mfma_f32_16x16x32_bf16 v[72:75], a[248:251], a[200:203], 0\n"
    "ds_read_b64_tr_b16 a[222:223], v196 offset:1280\n"
    "s_add_u32 s58, s58, 0x10000\n"
    "s_add_u32 m0, s69, 139264\n"
    "v_mfma_f32_16x16x32_bf16 v[104:107], a[248:251], a[204:207], 0\n"
    "s_add_u32 s59, s61, 256\n"
    "v_mfma_f32_16x16x32_bf16 v[136:139], a[248:251], a[208:211], 0\n"
    "buffer_load_dwordx4 v206, s[48:51], s59 offen lds\n"
    "v_mfma_f32_16x16x32_bf16 v[168:171], a[248:251], a[212:215], 0\n"
    "s_add_u32 m0, s69, 140288\n"
    "v_mfma_f32_16x16x32_bf16 v[12:15], a[252:255], a[192:195], 0\n"
    "ds_read_b64_tr_b16 a[248:249], v195 offset:1536\n"
    "s_add_u32 s59, s61, 9472\n"
    "v_mfma_f32_16x16x32_bf16 v[44:47], a[252:255], a[196:199], 0\n"
    "buffer_load_dwordx4 v207, s[48:51], s59 offen lds\n"
    "v_mfma_f32_16x16x32_bf16 v[76:79], a[252:255], a[200:203], 0\n"
    "ds_read_b64_tr_b16 a[250:251], v195 offset:1792\n"
    "s_add_u32 m0, s69, 141312\n"
    "v_mfma_f32_16x16x32_bf16 v[108:111], a[252:255], a[204:207], 0\n"
    "s_add_u32 s59, s61, 18688\n"
    "v_mfma_f32_16x16x32_bf16 v[140:143], a[252:255], a[208:211], 0\n"
    "buffer_load_dwordx4 v206, s[48:51], s59 offen lds\n"
    "v_mfma_f32_16x16x32_bf16 v[172:175], a[252:255], a[212:215], 0\n"
    "s_add_u32 m0, s69, 142336\n"
    "s_waitcnt lgkmcnt(2)\n"
    "v_mfma_f32_16x16x32_bf16 v[16:19], a[216:219], a[192:195], 0\n"
    "ds_read_b64_tr_b16 a[252:253], v196 offset:1536\n"
    "s_add_u32 s59, s61, 27904\n"
    "v_mfma_f32_16x16x32_bf16 v[48:51], a[216:219], a[196:199], 0\n"
    "buffer_load_dwordx4 v207, s[48:51], s59 offen lds\n"
    "v_mfma_f32_16x16x32_bf16 v[80:83], a[216:219], a[200:203], 0\n"
    "ds_read_b64_tr_b16 a[254:255], v196 offset:1792\n"
    "s_add_u32 m0, s69, 143360\n"
    "v_mfma_f32_16x16x32_bf16 v[112:115], a[216:219], a[204:207], 0\n"
    "s_add_u32 s59, s61, 37120\n"
    "v_mfma_f32_16x16x32_bf16 v[144:147], a[216:219], a[208:211], 0\n"
    "buffer_load_dwordx4 v206, s[48:51], s59 offen lds\n"
    "v_mfma_f32_16x16x32_bf16 v[176:179], a[216:219], a[212:215], 0\n"
    "s_add_u32 m0, s69, 144384\n"
    "v_mfma_f32_16x16x32_bf16 v[20:23], a[220:223], a[192:195], 0\n"
    "s_add_u32 s59, s61, 46336\n"
    "v_mfma_f32_16x16x32_bf16 v[52:55], a[220:223], a[196:199], 0\n"
    "buffer_load_dwordx4 v207, s[48:51], s59 offen lds\n"
    "v_mfma_f32_16x16x32_bf16 v[84:87], a[220:223], a[200:203], 0\n"
    "s_waitcnt vmcnt(25) lgkmcnt(0)\n"
    "s_barrier\n"
    "v_mfma_f32_16x16x32_bf16 v[116:119], a[220:223], a[204:207], 0\n"
    "ds_read_b64_tr_b16 a[216:217], v195 offset:16384\n"
    "v_mfma_f32_16x16x32_bf16 v[148:151], a[220:223], a[208:211], 0\n"
    "ds_read_b64_tr_b16 a[218:219], v195 offset:16640\n"
    "v_mfma_f32_16x16x32_bf16 v[180:183], a[220:223], a[212:215], 0\n"
    "ds_read_b128 a[224:227], v200 offset:512\n"
    "v_mfma_f32_16x16x32_bf16 v[24:27], a[248:251], a[192:195], 0\n"
    "ds_read_b128 a[228:231], v200 offset:2560\n"
    "v_mfma_f32_16x16x32_bf16 v[56:59], a[248:251], a[196:199], 0\n"
    "ds_read_b128 a[232:235], v200 offset:4608\n"
    "v_mfma_f32_16x16x32_bf16 v[88:91], a[248:251], a[200:203], 0\n"
    "ds_read_b128 a[236:239], v200 offset:6656\n"
    "v_mfma_f32_16x16x32_bf16 v[120:123], a[248:251], a[204:207], 0\n"
    "ds_read_b128 a[240:243], v200 offset:8704\n"
    "v_mfma_f32_16x16x32_bf16 v[152:155], a[248:251], a[208:211], 0\n"
    "ds_read_b128 a[244:247], v200 offset:10752\n"
    "s_add_u32 m0, s65, 32768\n"
    "v_mfma_f32_16x16x32_bf16 v[184:187], a[248:251], a[212:215], 0\n"
    "buffer_load_dwordx4 v204, s[44:47], s58 offen lds\n"
    "v_mfma_f32_16x16x32_bf16 v[28:31], a[252:255], a[192:195], 0\n"
    "ds_read_b64_tr_b16 a[220:221], v196 offset:16384\n"
    "s_add_u32 s58, s58, 0x10000\n"
    "v_mfma_f32_16x16x32_bf16 v[60:63], a[252:255], a[196:199], 0\n"
    "ds_read_b64_tr_b16 a[222:223], v196 offset:16640\n"
    "s_add_u32 m0, s65, 34816\n"
    "v_mfma_f32_16x16x32_bf16 v[92:95], a[252:255], a[200:203], 0\n"
    "buffer_load_dwordx4 v204, s[44:47], s58 offen lds\n"
    "v_mfma_f32_16x16x32_bf16 v[124:127], a[252:255], a[204:207], 0\n"
    "ds_read_b64_tr_b16 a[248:249], v195 offset:16896\n"
    "s_add_u32 s58, s58, 0x10000\n"
    "v_mfma_f32_16x16x32_bf16 v[156:159], a[252:255], a[208:211], 0\n"
    "ds_read_b64_tr_b16 a[250:251], v195 offset:17152\n"
    "s_add_u32 m0, s65, 36864\n"
    "v_mfma_f32_16x16x32_bf16 v[188:191], a[252:255], a[212:215], 0\n"
    "buffer_load_dwordx4 v205, s[44:47], s58 offen lds\n"
    "s_waitcnt lgkmcnt(4)\n"
    "v_mfma_f32_16x16x32_bf16 v[0:3], a[216:219], a[224:227], v[0:3]\n"
    "ds_read_b64_tr_b16 a[252:253], v196 offset:16896\n"
    "s_add_u32 s58, s58, 0x10000\n"
    "v_mfma_f32_16x16x32_bf16 v[32:35], a[216:219], a[228:231], v[32:35]\n"
    "v_accvgpr_write_b32 a0, 0\n"
    "v_mfma_f32_16x16x32_bf16 v[64:67], a[216:219], a[232:235], v[64:67]\n"
    "ds_read_b64_tr_b16 a[254:255], v196 offset:17152\n"
    "s_add_u32 m0, s65, 38912\n"
    "v_mfma_f32_16x16x32_bf16 v[96:99], a[216:219], a[236:239], v[96:99]\n"
    "buffer_load_dwordx4 v205, s[44:47], s58 offen lds\n"
    "v_accvgpr_write_b32 a1, 0\n"
    "v_mfma_f32_16x16x32_bf16 v[128:131], a[216:219], a[240:243], v[128:131]\n"
    "s_add_u32 s58, s58, 0x10000\n"
    "v_accvgpr_write_b32 a2, 0\n"
    "v_mfma_f32_16x16x32_bf16 v[160:163], a[216:219], a[244:247], v[160:163]\n"
    "v_accvgpr_write_b32 a3, 0\n"
    "s_waitcnt lgkmcnt(2)\n"
    "v_mfma_f32_16x16x32_bf16 v[4:7], a[220:223], a[224:227], v[4:7]\n"
    "ds_read_b64_tr_b16 a[216:217], v195 offset:17408\n"
    "v_accvgpr_write_b32 a4, 0\n"
    "v_mfma_f32_16x16x32_bf16 v[36:39], a[220:223], a[228:231], v[36:39]\n"
    "v_accvgpr_write_b32 a5, 0\n"
    "v_mfma_f32_16x16x32_bf16 v[68:71], a[220:223], a[232:235], v[68:71]\n"
    "ds_read_b64_tr_b16 a[218:219], v195 offset:17664\n"
    "v_accvgpr_write_b32 a6, 0\n"
    "v_mfma_f32_16x16x32_bf16 v[100:103], a[220:223], a[236:239], v[100:103]\n"
    "v_accvgpr_write_b32 a7, 0\n"
    "v_mfma_f32_16x16x32_bf16 v[132:135], a[220:223], a[240:243], v[132:135]\n"
    "v_accvgpr_write_b32 a8, 0\n"
    "v_mfma_f32_16x16x32_bf16 v[164:167], a[220:223], a[244:247], v[164:167]\n"
    "v_accvgpr_write_b32 a9, 0\n"
    "v_mfma_f32_16x16x32_bf16 v[8:11], a[248:251], a[224:227], v[8:11]\n"
    "ds_read_b64_tr_b16 a[220:221], v196 offset:17408\n"
    "v_accvgpr_write_b32 a10, 0\n"
    "v_mfma_f32_16x16x32_bf16 v[40:43], a[248:251], a[228:231], v[40:43]\n"
    "v_accvgpr_write_b32 a11, 0\n"
    "v_mfma_f32_16x16x32_bf16 v[72:75], a[248:251], a[232:235], v[72:75]\n"
    "ds_read_b64_tr_b16 a[222:223], v196 offset:17664\n"
    "v_accvgpr_write_b32 a12, 0\n"
    "v_mfma_f32_16x16x32_bf16 v[104:107], a[248:251], a[236:239], v[104:107]\n"
    "v_accvgpr_write_b32 a13, 0\n"
    "v_mfma_f32_16x16x32_bf16 v[136:139], a[248:251], a[240:243], v[136:139]\n"
    "v_accvgpr_write_b32 a14, 0\n"
    "v_mfma_f32_16x16x32_bf16 v[168:171], a[248:251], a[244:247], v[168:171]\n"
    "v_accvgpr_write_b32 a15, 0\n"
    "s_waitcnt lgkmcnt(2)\n"
    "v_mfma_f32_16x16x32_bf16 v[12:15], a[252:255], a[224:227], v[12:15]\n"
    "ds_read_b64_tr_b16 a[248:249], v195 offset:17920\n"
    "v_accvgpr_write_b32 a16, 0\n"
    "v_mfma_f32_16x16x32_bf16 v[44:47], a[252:255], a[228:231], v[44:47]\n"
    "v_accvgpr_write_b32 a17, 0\n"
    "v_mfma_f32_16x16x32_bf16 v[76:79], a[252:255], a[232:235], v[76:79]\n"
    "ds_read_b64_tr_b16 a[250:251], v195 offset:18176\n"
    "v_accvgpr_write_b32 a18, 0\n"
    "v_mfma_f32_16x16x32_bf16 v[108:111], a[252:255], a[236:239], v[108:111]\n"
    "v_accvgpr_write_b32 a19, 0\n"
    "v_mfma_f32_16x16x32_bf16 v[140:143], a[252:255], a[240:243], v[140:143]\n"
    "v_accvgpr_write_b32 a20, 0\n"
    "v_mfma_f32_16x16x32_bf16 v[172:175], a[252:255], a[244:247], v[172:175]\n"
    "v_accvgpr_write_b32 a21, 0\n"
    "v_mfma_f32_16x16x32_bf16 v[16:19], a[216:219], a[224:227], v[16:19]\n"
    "ds_read_b64_tr_b16 a[252:253], v196 offset:17920\n"
    "v_accvgpr_write_b32 a22, 0\n"
    "v_mfma_f32_16x16x32_bf16 v[48:51], a[216:219], a[228:231], v[48:51]\n"
    "v_accvgpr_write_b32 a23, 0\n"
    "v_mfma_f32_16x16x32_bf16 v[80:83], a[216:219], a[232:235], v[80:83]\n"
    "ds_read_b64_tr_b16 a[254:255], v196 offset:18176\n"
    "v_accvgpr_write_b32 a24, 0\n"
    "v_mfma_f32_16x16x32_bf16 v[112:115], a[216:219], a[236:239], v[112:115]\n"
    "v_accvgpr_write_b32 a25, 0\n"
    "v_mfma_f32_16x16x32_bf16 v[144:147], a[216:219], a[240:243], v[144:147]\n"
    "v_accvgpr_write_b32 a26, 0\n"
    "v_mfma_f32_16x16x32_bf16 v[176:179], a[216:219], a[244:247], v[176:179]\n"
    "v_accvgpr_write_b32 a27, 0\n"
    "s_waitcnt lgkmcnt(2)\n"
    "v_mfma_f32_16x16x32_bf16 v[20:23], a[220:223], a[224:227], v[20:23]\n"
    "v_accvgpr_write_b32 a28, 0\n"
    "v_mfma_f32_16x16x32_bf16 v[52:55], a[220:223], a[228:231], v[52:55]\n"
    "v_accvgpr_write_b32 a29, 0\n"
    "v_mfma_f32_16x16x32_bf16 v[84:87], a[220:223], a[232:235], v[84:87]\n"
    "s_waitcnt vmcnt(19) lgkmcnt(0)\n"
    "s_barrier\n"
    "v_mfma_f32_16x16x32_bf16 v[116:119], a[220:223], a[236:239], v[116:119]\n"
    "ds_read_b64_tr_b16 a[216:217], v197 offset:0\n"
    "v_accvgpr_write_b32 a30, 0\n"
    "v_mfma_f32_16x16x32_bf16 v[148:151], a[220:223], a[240:243], v[148:151]\n"
    "ds_read_b64_tr_b16 a[218:219], v197 offset:256\n"
    "v_accvgpr_write_b32 a31, 0\n"
    "v_mfma_f32_16x16x32_bf16 v[180:183], a[220:223], a[244:247], v[180:183]\n"
    "ds_read_b128 a[192:195], v199 offset:0\n"
    "v_accvgpr_write_b32 a32, 0\n"
    "v_mfma_f32_16x16x32_bf16 v[24:27], a[248:251], a[224:227], v[24:27]\n"
    "ds_read_b128 a[196:199], v199 offset:2048\n"
    "v_accvgpr_write_b32 a33, 0\n"
    "v_mfma_f32_16x16x32_bf16 v[56:59], a[248:251], a[228:231], v[56:59]\n"
    "ds_read_b128 a[200:203], v199 offset:4096\n"
    "v_accvgpr_write_b32 a34, 0\n"
    "v_mfma_f32_16x16x32_bf16 v[88:91], a[248:251], a[232:235], v[88:91]\n"
    "ds_read_b128 a[204:207], v199 offset:6144\n"
    "v_accvgpr_write_b32 a35, 0\n"
    "v_mfma_f32_16x16x32_bf16 v[120:123], a[248:251], a[236:239], v[120:123]\n"
    "ds_read_b128 a[208:211], v199 offset:8192\n"
    "v_accvgpr_write_b32 a36, 0\n"
    "v_mfma_f32_16x16x32_bf16 v[152:155], a[248:251], a[240:243], v[152:155]\n"
    "ds_read_b128 a[212:215], v199 offset:10240\n"
    "s_add_u32 m0, s65, 49152\n"
    "v_mfma_f32_16x16x32_bf16 v[184:187], a[248:251], a[244:247], v[184:187]\n"
    "buffer_load_dwordx4 v204, s[44:47], s58 offen lds\n"
    "v_accvgpr_write_b32 a37, 0\n"
    "v_mfma_f32_16x16x32_bf16 v[28:31], a[252:255], a[224:227], v[28:31]\n"
    "ds_read_b64_tr_b16 a[220:221], v198 offset:0\n"
    "s_add_u32 s58, s58, 0x10000\n"
    "v_mfma_f32_16x16x32_bf16 v[60:63], a[252:255], a[228:231], v[60:63]\n"
    "ds_read_b64_tr_b16 a[222:223], v198 offset:256\n"
    "s_add_u32 m0, s65, 51200\n"
    "v_mfma_f32_16x16x32_bf16 v[92:95], a[252:255], a[232:235], v[92:95]\n"
    "buffer_load_dwordx4 v204, s[44:47], s58 offen lds\n"
    "v_accvgpr_write_b32 a38, 0\n"
    "v_mfma_f32_16x16x32_bf16 v[124:127], a[252:255], a[236:239], v[124:127]\n"
    "ds_read_b64_tr_b16 a[248:249], v197 offset:512\n"
    "s_add_u32 s58, s58, 0x10000\n"
    "v_mfma_f32_16x16x32_bf16 v[156:159], a[252:255], a[240:243], v[156:159]\n"
    "ds_read_b64_tr_b16 a[250:251], v197 offset:768\n"
    "s_add_u32 m0, s65, 53248\n"
    "v_mfma_f32_16x16x32_bf16 v[188:191], a[252:255], a[244:247], v[188:191]\n"
    "buffer_load_dwordx4 v205, s[44:47], s58 offen lds\n"
    "v_accvgpr_write_b32 a39, 0\n"
    "s_waitcnt lgkmcnt(4)\n"
    "v_mfma_f32_16x16x32_bf16 v[0:3], a[216:219], a[192:195], v[0:3]\n"
    "ds_read_b64_tr_b16 a[252:253], v198 offset:512\n"
    "s_add_u32 s58, s58, 0x10000\n"
    "v_mfma_f32_16x16x32_bf16 v[32:35], a[216:219], a[196:199], v[32:35]\n"
    "v_accvgpr_write_b32 a40, 0\n"
    "v_mfma_f32_16x16x32_bf16 v[64:67], a[216:219], a[200:203], v[64:67]\n"
    "ds_read_b64_tr_b16 a[254:255], v198 offset:768\n"
    "s_add_u32 m0, s65, 55296\n"
    "v_mfma_f32_16x16x32_bf16 v[96:99], a[216:219], a[204:207], v[96:99]\n"
    "buffer_load_dwordx4 v205, s[44:47], s58 offen lds\n"
    "v_accvgpr_write_b32 a41, 0\n"
    "v_mfma_f32_16x16x32_bf16 v[128:131], a[216:219], a[208:211], v[128:131]\n"
    "s_add_u32 s58, s58, 0x10000\n"
    "v_accvgpr_write_b32 a42, 0\n"
    "v_mfma_f32_16x16x32_bf16 v[160:163], a[216:219], a[212:215], v[160:163]\n"
    "s_add_u32 m0, s69, 0\n"
    "v_accvgpr_write_b32 a43, 0\n"
    "s_waitcnt lgkmcnt(2)\n"
    "v_mfma_f32_16x16x32_bf16 v[4:7], a[220:223], a[192:195], v[4:7]\n"
    "ds_read_b64_tr_b16 a[216:217], v197 offset:1024\n"
    "s_add_u32 s59, s61, 384\n"
    "v_mfma_f32_16x16x32_bf16 v[36:39], a[220:223], a[196:199], v[36:39]\n"
    "buffer_load_dwordx4 v206, s[48:51], s59 offen lds\n"
    "v_accvgpr_write_b32 a44, 0\n"
    "v_mfma_f32_16x16x32_bf16 v[68:71], a[220:223], a[200:203], v[68:71]\n"
    "ds_read_b64_tr_b16 a[218:219], v197 offset:1280\n"
    "v_accvgpr_write_b32 a45, 0\n"
    "v_mfma_f32_16x16x32_bf16 v[100:103], a[220:223], a[204:207], v[100:103]\n"
    "s_add_u32 m0, s69, 1024\n"
    "v_accvgpr_write_b32 a46, 0\n"
    "v_mfma_f32_16x16x32_bf16 v[132:135], a[220:223], a[208:211], v[132:135]\n"
    "s_add_u32 s59, s61, 9600\n"
    "v_accvgpr_write_b32 a47, 0\n"
    "v_mfma_f32_16x16x32_bf16 v[164:167], a[220:223], a[212:215], v[164:167]\n"
    "buffer_load_dwordx4 v207, s[48:51], s59 offen lds\n"
    "v_accvgpr_write_b32 a48, 0\n"
    "v_mfma_f32_16x16x32_bf16 v[8:11], a[248:251], a[192:195], v[8:11]\n"
    "ds_read_b64_tr_b16 a[220:221], v198 offset:1024\n"
    "v_accvgpr_write_b32 a49, 0\n"
    "v_mfma_f32_16x16x32_bf16 v[40:43], a[248:251], a[196:199], v[40:43]\n"
    "s_add_u32 m0, s69, 2048\n"
    "v_accvgpr_write_b32 a50, 0\n"
    "v_mfma_f32_16x16x32_bf16 v[72:75], a[248:251], a[200:203], v[72:75]\n"
    "ds_read_b64_tr_b16 a[222:223], v198 offset:1280\n"
    "s_add_u32 s59, s61, 18816\n"
    "v_mfma_f32_16x16x32_bf16 v[104:107], a[248:251], a[204:207], v[104:107]\n"
    "buffer_load_dwordx4 v206, s[48:51], s59 offen lds\n"
    "v_accvgpr_write_b32 a51, 0\n"
    "v_mfma_f32_16x16x32_bf16 v[136:139], a[248:251], a[208:211], v[136:139]\n"
    "v_accvgpr_write_b32 a52, 0\n"
    "v_mfma_f32_16x16x32_bf16 v[168:171], a[248:251], a[212:215], v[168:171]\n"
    "s_add_u32 m0, s69, 3072\n"
    "v_accvgpr_write_b32 a53, 0\n"
    "s_waitcnt lgkmcnt(2)\n"
    "v_mfma_f32_16x16x32_bf16 v[12:15], a[252:255], a[192:195], v[12:15]\n"
    "ds_read_b64_tr_b16 a[248:249], v197 offset:1536\n"
    "s_add_u32 s59, s61, 28032\n"
    "v_mfma_f32_16x16x32_bf16 v[44:47], a[252:255], a[196:199], v[44:47]\n"
    "buffer_load_dwordx4 v207, s[48:51], s59 offen lds\n"
    "v_accvgpr_write_b32 a54, 0\n"
    "v_mfma_f32_16x16x32_bf16 v[76:79], a[252:255], a[200:203], v[76:79]\n"
    "ds_read_b64_tr_b16 a[250:251], v197 offset:1792\n"
    "v_accvgpr_write_b32 a55, 0\n"
    "v_mfma_f32_16x16x32_bf16 v[108:111], a[252:255], a[204:207], v[108:111]\n"
    "s_add_u32 m0, s69, 4096\n"
    "v_accvgpr_write_b32 a56, 0\n"
    "v_mfma_f32_16x16x32_bf16 v[140:143], a[252:255], a[208:211], v[140:143]\n"
    "s_add_u32 s59, s61, 37248\n"
    "v_accvgpr_write_b32 a57, 0\n"
    "v_mfma_f32_16x16x32_bf16 v[172:175], a[252:255], a[212:215], v[172:175]\n"
    "buffer_load_dwordx4 v206, s[48:51], s59 offen lds\n"
    "v_accvgpr_write_b32 a58, 0\n"
    "v_mfma_f32_16x16x32_bf16 v[16:19], a[216:219], a[192:195], v[16:19]\n"
    "ds_read_b64_tr_b16 a[252:253], v198 offset:1536\n"
    "v_accvgpr_write_b32 a59, 0\n"
    "v_mfma_f32_16x16x32_bf16 v[48:51], a[216:219], a[196:199], v[48:51]\n"
    "s_add_u32 m0, s69, 5120\n"
    "v_accvgpr_write_b32 a60, 0\n"
    "v_mfma_f32_16x16x32_bf16 v[80:83], a[216:219], a[200:203], v[80:83]\n"
    "ds_read_b64_tr_b16 a[254:255], v198 offset:1792\n"
    "s_add_u32 s59, s61, 46464\n"
    "v_mfma_f32_16x16x32_bf16 v[112:115], a[216:219], a[204:207], v[112:115]\n"
    "buffer_load_dwordx4 v207, s[48:51], s59 offen lds\n"
    "v_accvgpr_write_b32 a61, 0\n"
    "v_mfma_f32_16x16x32_bf16 v[144:147], a[216:219], a[208:211], v[144:147]\n"
    "v_accvgpr_write_b32 a62, 0\n"
    "v_mfma_f32_16x16x32_bf16 v[176:179], a[216:219], a[212:215], v[176:179]\n"
    "v_accvgpr_write_b32 a63, 0\n"
    "s_waitcnt lgkmcnt(2)\n"
    "v_mfma_f32_16x16x32_bf16 v[20:23], a[220:223], a[192:195], v[20:23]\n"
    "v_accvgpr_write_b32 a64, 0\n"
    "v_mfma_f32_16x16x32_bf16 v[52:55], a[220:223], a[196:199], v[52:55]\n"
    "v_accvgpr_write_b32 a65, 0\n"
    "v_mfma_f32_16x16x32_bf16 v[84:87], a[220:223], a[200:203], v[84:87]\n"
    "s_waitcnt vmcnt(25) lgkmcnt(0)\n"
    "s_barrier\n"
    "v_mfma_f32_16x16x32_bf16 v[116:119], a[220:223], a[204:207], v[116:119]\n"
    "ds_read_b64_tr_b16 a[216:217], v195 offset:32768\n"
    "v_accvgpr_write_b32 a66, 0\n"
    "v_mfma_f32_16x16x32_bf16 v[148:151], a[220:223], a[208:211], v[148:151]\n"
    "ds_read_b64_tr_b16 a[218:219], v195 offset:33024\n"
    "v_accvgpr_write_b32 a67, 0\n"
    "v_mfma_f32_16x16x32_bf16 v[180:183], a[220:223], a[212:215], v[180:183]\n"
    "ds_read_b128 a[224:227], v199 offset:512\n"
    "v_accvgpr_write_b32 a68, 0\n"
    "v_mfma_f32_16x16x32_bf16 v[24:27], a[248:251], a[192:195], v[24:27]\n"
    "ds_read_b128 a[228:231], v199 offset:2560\n"
    "v_accvgpr_write_b32 a69, 0\n"
    "v_mfma_f32_16x16x32_bf16 v[56:59], a[248:251], a[196:199], v[56:59]\n"
    "ds_read_b128 a[232:235], v199 offset:4608\n"
    "v_accvgpr_write_b32 a70, 0\n"
    "v_mfma_f32_16x16x32_bf16 v[88:91], a[248:251], a[200:203], v[88:91]\n"
    "ds_read_b128 a[236:239], v199 offset:6656\n"
    "v_accvgpr_write_b32 a71, 0\n"
    "v_mfma_f32_16x16x32_bf16 v[120:123], a[248:251], a[204:207], v[120:123]\n"
    "ds_read_b128 a[240:243], v199 offset:8704\n"
    "v_accvgpr_write_b32 a72, 0\n"
    "v_mfma_f32_16x16x32_bf16 v[152:155], a[248:251], a[208:211], v[152:155]\n"
    "ds_read_b128 a[244:247], v199 offset:10752\n"
    "s_add_u32 m0, s65, 98304\n"
    "v_mfma_f32_16x16x32_bf16 v[184:187], a[248:251], a[212:215], v[184:187]\n"
    "buffer_load_dwordx4 v204, s[44:47], s58 offen lds\n"
    "v_accvgpr_write_b32 a73, 0\n"
    "v_mfma_f32_16x16x32_bf16 v[28:31], a[252:255], a[192:195], v[28:31]\n"
    "ds_read_b64_tr_b16 a[220:221], v196 offset:32768\n"
    "s_add_u32 s58, s58, 0x10000\n"
    "v_mfma_f32_16x16x32_bf16 v[60:63], a[252:255], a[196:199], v[60:63]\n"
    "ds_read_b64_tr_b16 a[222:223], v196 offset:33024\n"
    "s_add_u32 m0, s65, 100352\n"
    "v_mfma_f32_16x16x32_bf16 v[92:95], a[252:255], a[200:203], v[92:95]\n"
    "buffer_load_dwordx4 v204, s[44:47], s58 offen lds\n"
    "v_accvgpr_write_b32 a74, 0\n"
    "v_mfma_f32_16x16x32_bf16 v[124:127], a[252:255], a[204:207], v[124:127]\n"
    "ds_read_b64_tr_b16 a[248:249], v195 offset:33280\n"
    "s_add_u32 s58, s58, 0x10000\n"
    "v_mfma_f32_16x16x32_bf16 v[156:159], a[252:255], a[208:211], v[156:159]\n"
    "ds_read_b64_tr_b16 a[250:251], v195 offset:33536\n"
    "s_add_u32 m0, s65, 102400\n"
    "v_mfma_f32_16x16x32_bf16 v[188:191], a[252:255], a[212:215], v[188:191]\n"
    "buffer_load_dwordx4 v205, s[44:47], s58 offen lds\n"
    "v_accvgpr_write_b32 a75, 0\n"
    "s_waitcnt lgkmcnt(4)\n"
    "v_mfma_f32_16x16x32_bf16 v[0:3], a[216:219], a[224:227], v[0:3]\n"
    "ds_read_b64_tr_b16 a[252:253], v196 offset:33280\n"
    "s_add_u32 s58, s58, 0x10000\n"
    "v_mfma_f32_16x16x32_bf16 v[32:35], a[216:219], a[228:231], v[32:35]\n"
    "v_accvgpr_write_b32 a76, 0\n"
    "v_mfma_f32_16x16x32_bf16 v[64:67], a[216:219], a[232:235], v[64:67]\n"
    "ds_read_b64_tr_b16 a[254:255], v196 offset:33536\n"
    "s_add_u32 m0, s65, 104448\n"
    "v_mfma_f32_16x16x32_bf16 v[96:99], a[216:219], a[236:239], v[96:99]\n"
    "buffer_load_dwordx4 v205, s[44:47], s58 offen lds\n"
    "v_accvgpr_write_b32 a77, 0\n"
    "v_mfma_f32_16x16x32_bf16 v[128:131], a[216:219], a[240:243], v[128:131]\n"
    "s_add_u32 s58, s58, 0x10000\n"
    "v_accvgpr_write_b32 a78, 0\n"
    "v_mfma_f32_16x16x32_bf16 v[160:163], a[216:219], a[244:247], v[160:163]\n"
    "v_accvgpr_write_b32 a79, 0\n"
    "s_waitcnt lgkmcnt(2)\n"
    "v_mfma_f32_16x16x32_bf16 v[4:7], a[220:223], a[224:227], v[4:7]\n"
    "ds_read_b64_tr_b16 a[216:217], v195 offset:33792\n"
    "v_accvgpr_write_b32 a80, 0\n"
    "v_mfma_f32_16x16x32_bf16 v[36:39], a[220:223], a[228:231], v[36:39]\n"
    "v_accvgpr_write_b32 a81, 0\n"
    "v_mfma_f32_16x16x32_bf16 v[68:71], a[220:223], a[232:235], v[68:71]\n"
    "ds_read_b64_tr_b16 a[218:219], v195 offset:34048\n"
    "v_accvgpr_write_b32 a82, 0\n"
    "v_mfma_f32_16x16x32_bf16 v[100:103], a[220:223], a[236:239], v[100:103]\n"
    "v_accvgpr_write_b32 a83, 0\n"
    "v_mfma_f32_16x16x32_bf16 v[132:135], a[220:223], a[240:243], v[132:135]\n"
    "v_accvgpr_write_b32 a84, 0\n"
    "v_mfma_f32_16x16x32_bf16 v[164:167], a[220:223], a[244:247], v[164:167]\n"
    "v_accvgpr_write_b32 a85, 0\n"
    "v_mfma_f32_16x16x32_bf16 v[8:11], a[248:251], a[224:227], v[8:11]\n"
    "ds_read_b64_tr_b16 a[220:221], v196 offset:33792\n"
    "v_accvgpr_write_b32 a86, 0\n"
    "v_mfma_f32_16x16x32_bf16 v[40:43], a[248:251], a[228:231], v[40:43]\n"
    "v_accvgpr_write_b32 a87, 0\n"
    "v_mfma_f32_16x16x32_bf16 v[72:75], a[248:251], a[232:235], v[72:75]\n"
    "ds_read_b64_tr_b16 a[222:223], v196 offset:34048\n"
    "v_accvgpr_write_b32 a88, 0\n"
    "v_mfma_f32_16x16x32_bf16 v[104:107], a[248:251], a[236:239], v[104:107]\n"
    "v_accvgpr_write_b32 a89, 0\n"
    "v_mfma_f32_16x16x32_bf16 v[136:139], a[248:251], a[240:243], v[136:139]\n"
    "v_accvgpr_write_b32 a90, 0\n"
    "v_mfma_f32_16x16x32_bf16 v[168:171], a[248:251], a[244:247], v[168:171]\n"
    "v_accvgpr_write_b32 a91, 0\n"
    "s_waitcnt lgkmcnt(2)\n"
    "v_mfma_f32_16x16x32_bf16 v[12:15], a[252:255], a[224:227], v[12:15]\n"
    "ds_read_b64_tr_b16 a[248:249], v195 offset:34304\n"
    "v_accvgpr_write_b32 a92, 0\n"
    "v_mfma_f32_16x16x32_bf16 v[44:47], a[252:255], a[228:231], v[44:47]\n"
    "v_accvgpr_write_b32 a93, 0\n"
    "v_mfma_f32_16x16x32_bf16 v[76:79], a[252:255], a[232:235], v[76:79]\n"
    "ds_read_b64_tr_b16 a[250:251], v195 offset:34560\n"
    "v_accvgpr_write_b32 a94, 0\n"
    "v_mfma_f32_16x16x32_bf16 v[108:111], a[252:255], a[236:239], v[108:111]\n"
    "v_accvgpr_write_b32 a95, 0\n"
    "v_mfma_f32_16x16x32_bf16 v[140:143], a[252:255], a[240:243], v[140:143]\n"
    "v_accvgpr_write_b32 a96, 0\n"
    "v_mfma_f32_16x16x32_bf16 v[172:175], a[252:255], a[244:247], v[172:175]\n"
    "v_accvgpr_write_b32 a97, 0\n"
    "v_mfma_f32_16x16x32_bf16 v[16:19], a[216:219], a[224:227], v[16:19]\n"
    "ds_read_b64_tr_b16 a[252:253], v196 offset:34304\n"
    "v_accvgpr_write_b32 a98, 0\n"
    "v_mfma_f32_16x16x32_bf16 v[48:51], a[216:219], a[228:231], v[48:51]\n"
    "v_accvgpr_write_b32 a99, 0\n"
    "v_mfma_f32_16x16x32_bf16 v[80:83], a[216:219], a[232:235], v[80:83]\n"
    "ds_read_b64_tr_b16 a[254:255], v196 offset:34560\n"
    "v_accvgpr_write_b32 a100, 0\n"
    "v_mfma_f32_16x16x32_bf16 v[112:115], a[216:219], a[236:239], v[112:115]\n"
    "v_accvgpr_write_b32 a101, 0\n"
    "v_mfma_f32_16x16x32_bf16 v[144:147], a[216:219], a[240:243], v[144:147]\n"
    "v_accvgpr_write_b32 a102, 0\n"
    "v_mfma_f32_16x16x32_bf16 v[176:179], a[216:219], a[244:247], v[176:179]\n"
    "v_accvgpr_write_b32 a103, 0\n"
    "s_waitcnt lgkmcnt(2)\n"
    "v_mfma_f32_16x16x32_bf16 v[20:23], a[220:223], a[224:227], v[20:23]\n"
    "v_accvgpr_write_b32 a104, 0\n"
    "v_mfma_f32_16x16x32_bf16 v[52:55], a[220:223], a[228:231], v[52:55]\n"
    "v_accvgpr_write_b32 a105, 0\n"
    "v_mfma_f32_16x16x32_bf16 v[84:87], a[220:223], a[232:235], v[84:87]\n"
    "s_waitcnt vmcnt(18) lgkmcnt(0)\n"
    "s_barrier\n"
    "v_mfma_f32_16x16x32_bf16 v[116:119], a[220:223], a[236:239], v[116:119]\n"
    "ds_read_b64_tr_b16 a[216:217], v195 offset:49152\n"
    "v_accvgpr_write_b32 a106, 0\n"
    "v_mfma_f32_16x16x32_bf16 v[148:151], a[220:223], a[240:243], v[148:151]\n"
    "ds_read_b64_tr_b16 a[218:219], v195 offset:49408\n"
    "v_accvgpr_write_b32 a107, 0\n"
    "v_mfma_f32_16x16x32_bf16 v[180:183], a[220:223], a[244:247], v[180:183]\n"
    "ds_read_b128 a[192:195], v199 offset:24576\n"
    "v_accvgpr_write_b32 a108, 0\n"
    "v_mfma_f32_16x16x32_bf16 v[24:27], a[248:251], a[224:227], v[24:27]\n"
    "ds_read_b128 a[196:199], v199 offset:26624\n"
    "v_accvgpr_write_b32 a109, 0\n"
    "v_mfma_f32_16x16x32_bf16 v[56:59], a[248:251], a[228:231], v[56:59]\n"
    "ds_read_b128 a[200:203], v199 offset:28672\n"
    "v_accvgpr_write_b32 a110, 0\n"
    "v_mfma_f32_16x16x32_bf16 v[88:91], a[248:251], a[232:235], v[88:91]\n"
    "ds_read_b128 a[204:207], v199 offset:30720\n"
    "v_accvgpr_write_b32 a111, 0\n"
    "v_mfma_f32_16x16x32_bf16 v[120:123], a[248:251], a[236:239], v[120:123]\n"
    "ds_read_b128 a[208:211], v199 offset:32768\n"
    "v_accvgpr_write_b32 a112, 0\n"
    "v_mfma_f32_16x16x32_bf16 v[152:155], a[248:251], a[240:243], v[152:155]\n"
    "ds_read_b128 a[212:215], v199 offset:34816\n"
    "s_add_u32 m0, s65, 65536\n"
    "v_mfma_f32_16x16x32_bf16 v[184:187], a[248:251], a[244:247], v[184:187]\n"
    "buffer_load_dwordx4 v204, s[44:47], s58 offen lds\n"
    "v_accvgpr_write_b32 a113, 0\n"
    "v_mfma_f32_16x16x32_bf16 v[28:31], a[252:255], a[224:227], v[28:31]\n"
    "ds_read_b64_tr_b16 a[220:221], v196 offset:49152\n"
    "s_add_u32 s58, s58, 0x10000\n"
    "v_mfma_f32_16x16x32_bf16 v[60:63], a[252:255], a[228:231], v[60:63]\n"
    "ds_read_b64_tr_b16 a[222:223], v196 offset:49408\n"
    "s_add_u32 m0, s65, 67584\n"
    "v_mfma_f32_16x16x32_bf16 v[92:95], a[252:255], a[232:235], v[92:95]\n"
    "buffer_load_dwordx4 v204, s[44:47], s58 offen lds\n"
    "v_accvgpr_write_b32 a114, 0\n"
    "v_mfma_f32_16x16x32_bf16 v[124:127], a[252:255], a[236:239], v[124:127]\n"
    "ds_read_b64_tr_b16 a[248:249], v195 offset:49664\n"
    "s_add_u32 s58, s58, 0x10000\n"
    "v_mfma_f32_16x16x32_bf16 v[156:159], a[252:255], a[240:243], v[156:159]\n"
    "ds_read_b64_tr_b16 a[250:251], v195 offset:49920\n"
    "s_add_u32 m0, s65, 69632\n"
    "v_mfma_f32_16x16x32_bf16 v[188:191], a[252:255], a[244:247], v[188:191]\n"
    "buffer_load_dwordx4 v205, s[44:47], s58 offen lds\n"
    "v_accvgpr_write_b32 a115, 0\n"
    "s_waitcnt lgkmcnt(4)\n"
    "v_mfma_f32_16x16x32_bf16 v[0:3], a[216:219], a[192:195], v[0:3]\n"
    "ds_read_b64_tr_b16 a[252:253], v196 offset:49664\n"
    "s_add_u32 s58, s58, 0x10000\n"
    "v_mfma_f32_16x16x32_bf16 v[32:35], a[216:219], a[196:199], v[32:35]\n"
    "v_accvgpr_write_b32 a116, 0\n"
    "v_mfma_f32_16x16x32_bf16 v[64:67], a[216:219], a[200:203], v[64:67]\n"
    "ds_read_b64_tr_b16 a[254:255], v196 offset:49920\n"
    "s_add_u32 m0, s65, 71680\n"
    "v_mfma_f32_16x16x32_bf16 v[96:99], a[216:219], a[204:207], v[96:99]\n"
    "buffer_load_dwordx4 v205, s[44:47], s58 offen lds\n"
    "v_accvgpr_write_b32 a117, 0\n"
    "v_mfma_f32_16x16x32_bf16 v[128:131], a[216:219], a[208:211], v[128:131]\n"
    "s_add_u32 s58, s58, 0x10000\n"
    "v_accvgpr_write_b32 a118, 0\n"
    "v_mfma_f32_16x16x32_bf16 v[160:163], a[216:219], a[212:215], v[160:163]\n"
    "s_add_u32 m0, s69, 114688\n"
    "v_accvgpr_write_b32 a119, 0\n"
    "s_waitcnt lgkmcnt(2)\n"
    "v_mfma_f32_16x16x32_bf16 v[4:7], a[220:223], a[192:195], v[4:7]\n"
    "ds_read_b64_tr_b16 a[216:217], v195 offset:50176\n"
    "s_add_u32 s59, s61, 512\n"
    "v_mfma_f32_16x16x32_bf16 v[36:39], a[220:223], a[196:199], v[36:39]\n"
    "buffer_load_dwordx4 v206, s[48:51], s59 offen lds\n"
    "v_accvgpr_write_b32 a120, 0\n"
    "v_mfma_f32_16x16x32_bf16 v[68:71], a[220:223], a[200:203], v[68:71]\n"
    "ds_read_b64_tr_b16 a[218:219], v195 offset:50432\n"
    "v_accvgpr_write_b32 a121, 0\n"
    "v_mfma_f32_16x16x32_bf16 v[100:103], a[220:223], a[204:207], v[100:103]\n"
    "s_add_u32 m0, s69, 115712\n"
    "v_accvgpr_write_b32 a122, 0\n"
    "v_mfma_f32_16x16x32_bf16 v[132:135], a[220:223], a[208:211], v[132:135]\n"
    "s_add_u32 s59, s61, 9728\n"
    "v_accvgpr_write_b32 a123, 0\n"
    "v_mfma_f32_16x16x32_bf16 v[164:167], a[220:223], a[212:215], v[164:167]\n"
    "buffer_load_dwordx4 v207, s[48:51], s59 offen lds\n"
    "v_accvgpr_write_b32 a124, 0\n"
    "v_mfma_f32_16x16x32_bf16 v[8:11], a[248:251], a[192:195], v[8:11]\n"
    "ds_read_b64_tr_b16 a[220:221], v196 offset:50176\n"
    "v_accvgpr_write_b32 a125, 0\n"
    "v_mfma_f32_16x16x32_bf16 v[40:43], a[248:251], a[196:199], v[40:43]\n"
    "s_add_u32 m0, s69, 116736\n"
    "v_accvgpr_write_b32 a126, 0\n"
    "v_mfma_f32_16x16x32_bf16 v[72:75], a[248:251], a[200:203], v[72:75]\n"
    "ds_read_b64_tr_b16 a[222:223], v196 offset:50432\n"
    "s_add_u32 s59, s61, 18944\n"
    "v_mfma_f32_16x16x32_bf16 v[104:107], a[248:251], a[204:207], v[104:107]\n"
    "buffer_load_dwordx4 v206, s[48:51], s59 offen lds\n"
    "v_accvgpr_write_b32 a127, 0\n"
    "v_mfma_f32_16x16x32_bf16 v[136:139], a[248:251], a[208:211], v[136:139]\n"
    "v_accvgpr_write_b32 a128, 0\n"
    "v_mfma_f32_16x16x32_bf16 v[168:171], a[248:251], a[212:215], v[168:171]\n"
    "s_add_u32 m0, s69, 117760\n"
    "v_accvgpr_write_b32 a129, 0\n"
    "s_waitcnt lgkmcnt(2)\n"
    "v_mfma_f32_16x16x32_bf16 v[12:15], a[252:255], a[192:195], v[12:15]\n"
    "ds_read_b64_tr_b16 a[248:249], v195 offset:50688\n"
    "s_add_u32 s59, s61, 28160\n"
    "v_mfma_f32_16x16x32_bf16 v[44:47], a[252:255], a[196:199], v[44:47]\n"
    "buffer_load_dwordx4 v207, s[48:51], s59 offen lds\n"
    "v_accvgpr_write_b32 a130, 0\n"
    "v_mfma_f32_16x16x32_bf16 v[76:79], a[252:255], a[200:203], v[76:79]\n"
    "ds_read_b64_tr_b16 a[250:251], v195 offset:50944\n"
    "v_accvgpr_write_b32 a131, 0\n"
    "v_mfma_f32_16x16x32_bf16 v[108:111], a[252:255], a[204:207], v[108:111]\n"
    "s_add_u32 m0, s69, 118784\n"
    "v_accvgpr_write_b32 a132, 0\n"
    "v_mfma_f32_16x16x32_bf16 v[140:143], a[252:255], a[208:211], v[140:143]\n"
    "s_add_u32 s59, s61, 37376\n"
    "v_accvgpr_write_b32 a133, 0\n"
    "v_mfma_f32_16x16x32_bf16 v[172:175], a[252:255], a[212:215], v[172:175]\n"
    "buffer_load_dwordx4 v206, s[48:51], s59 offen lds\n"
    "v_accvgpr_write_b32 a134, 0\n"
    "v_mfma_f32_16x16x32_bf16 v[16:19], a[216:219], a[192:195], v[16:19]\n"
    "ds_read_b64_tr_b16 a[252:253], v196 offset:50688\n"
    "v_accvgpr_write_b32 a135, 0\n"
    "v_mfma_f32_16x16x32_bf16 v[48:51], a[216:219], a[196:199], v[48:51]\n"
    "s_add_u32 m0, s69, 119808\n"
    "v_accvgpr_write_b32 a136, 0\n"
    "v_mfma_f32_16x16x32_bf16 v[80:83], a[216:219], a[200:203], v[80:83]\n"
    "ds_read_b64_tr_b16 a[254:255], v196 offset:50944\n"
    "s_add_u32 s59, s61, 46592\n"
    "v_mfma_f32_16x16x32_bf16 v[112:115], a[216:219], a[204:207], v[112:115]\n"
    "buffer_load_dwordx4 v207, s[48:51], s59 offen lds\n"
    "v_accvgpr_write_b32 a137, 0\n"
    "v_mfma_f32_16x16x32_bf16 v[144:147], a[216:219], a[208:211], v[144:147]\n"
    "v_accvgpr_write_b32 a138, 0\n"
    "v_mfma_f32_16x16x32_bf16 v[176:179], a[216:219], a[212:215], v[176:179]\n"
    "v_accvgpr_write_b32 a139, 0\n"
    "s_waitcnt lgkmcnt(2)\n"
    "v_mfma_f32_16x16x32_bf16 v[20:23], a[220:223], a[192:195], v[20:23]\n"
    "v_accvgpr_write_b32 a140, 0\n"
    "v_mfma_f32_16x16x32_bf16 v[52:55], a[220:223], a[196:199], v[52:55]\n"
    "v_accvgpr_write_b32 a141, 0\n"
    "v_mfma_f32_16x16x32_bf16 v[84:87], a[220:223], a[200:203], v[84:87]\n"
    "s_waitcnt vmcnt(24) lgkmcnt(0)\n"
    "s_barrier\n"
    "v_mfma_f32_16x16x32_bf16 v[116:119], a[220:223], a[204:207], v[116:119]\n"
    "ds_read_b64_tr_b16 a[216:217], v195 offset:0\n"
    "v_accvgpr_write_b32 a142, 0\n"
    "v_mfma_f32_16x16x32_bf16 v[148:151], a[220:223], a[208:211], v[148:151]\n"
    "ds_read_b64_tr_b16 a[218:219], v195 offset:256\n"
    "v_accvgpr_write_b32 a143, 0\n"
    "v_mfma_f32_16x16x32_bf16 v[180:183], a[220:223], a[212:215], v[180:183]\n"
    "ds_read_b128 a[224:227], v199 offset:25088\n"
    "v_accvgpr_write_b32 a144, 0\n"
    "v_mfma_f32_16x16x32_bf16 v[24:27], a[248:251], a[192:195], v[24:27]\n"
    "ds_read_b128 a[228:231], v199 offset:27136\n"
    "v_accvgpr_write_b32 a145, 0\n"
    "v_mfma_f32_16x16x32_bf16 v[56:59], a[248:251], a[196:199], v[56:59]\n"
    "ds_read_b128 a[232:235], v199 offset:29184\n"
    "v_accvgpr_write_b32 a146, 0\n"
    "v_mfma_f32_16x16x32_bf16 v[88:91], a[248:251], a[200:203], v[88:91]\n"
    "ds_read_b128 a[236:239], v199 offset:31232\n"
    "v_accvgpr_write_b32 a147, 0\n"
    "v_mfma_f32_16x16x32_bf16 v[120:123], a[248:251], a[204:207], v[120:123]\n"
    "ds_read_b128 a[240:243], v199 offset:33280\n"
    "v_accvgpr_write_b32 a148, 0\n"
    "v_mfma_f32_16x16x32_bf16 v[152:155], a[248:251], a[208:211], v[152:155]\n"
    "ds_read_b128 a[244:247], v199 offset:35328\n"
    "s_add_u32 m0, s65, 81920\n"
    "v_mfma_f32_16x16x32_bf16 v[184:187], a[248:251], a[212:215], v[184:187]\n"
    "buffer_load_dwordx4 v204, s[44:47], s58 offen lds\n"
    "v_accvgpr_write_b32 a149, 0\n"
    "v_mfma_f32_16x16x32_bf16 v[28:31], a[252:255], a[192:195], v[28:31]\n"
    "ds_read_b64_tr_b16 a[220:221], v196 offset:0\n"
    "s_add_u32 s58, s58, 0x10000\n"
    "v_mfma_f32_16x16x32_bf16 v[60:63], a[252:255], a[196:199], v[60:63]\n"
    "ds_read_b64_tr_b16 a[222:223], v196 offset:256\n"
    "s_add_u32 m0, s65, 83968\n"
    "v_mfma_f32_16x16x32_bf16 v[92:95], a[252:255], a[200:203], v[92:95]\n"
    "buffer_load_dwordx4 v204, s[44:47], s58 offen lds\n"
    "v_accvgpr_write_b32 a150, 0\n"
    "v_mfma_f32_16x16x32_bf16 v[124:127], a[252:255], a[204:207], v[124:127]\n"
    "ds_read_b64_tr_b16 a[248:249], v195 offset:512\n"
    "s_add_u32 s58, s58, 0x10000\n"
    "v_mfma_f32_16x16x32_bf16 v[156:159], a[252:255], a[208:211], v[156:159]\n"
    "ds_read_b64_tr_b16 a[250:251], v195 offset:768\n"
    "s_add_u32 m0, s65, 86016\n"
    "v_mfma_f32_16x16x32_bf16 v[188:191], a[252:255], a[212:215], v[188:191]\n"
    "buffer_load_dwordx4 v205, s[44:47], s58 offen lds\n"
    "v_accvgpr_write_b32 a151, 0\n"
    "s_waitcnt lgkmcnt(4)\n"
    "v_mfma_f32_16x16x32_bf16 v[0:3], a[216:219], a[224:227], v[0:3]\n"
    "ds_read_b64_tr_b16 a[252:253], v196 offset:512\n"
    "s_add_u32 s58, s58, 0x10000\n"
    "v_mfma_f32_16x16x32_bf16 v[32:35], a[216:219], a[228:231], v[32:35]\n"
    "v_accvgpr_write_b32 a152, 0\n"
    "v_mfma_f32_16x16x32_bf16 v[64:67], a[216:219], a[232:235], v[64:67]\n"
    "ds_read_b64_tr_b16 a[254:255], v196 offset:768\n"
    "s_add_u32 m0, s65, 88064\n"
    "v_mfma_f32_16x16x32_bf16 v[96:99], a[216:219], a[236:239], v[96:99]\n"
    "buffer_load_dwordx4 v205, s[44:47], s58 offen lds\n"
    "v_accvgpr_write_b32 a153, 0\n"
    "v_mfma_f32_16x16x32_bf16 v[128:131], a[216:219], a[240:243], v[128:131]\n"
    "s_add_u32 s58, s58, 0x10000\n"
    "v_accvgpr_write_b32 a154, 0\n"
    "v_mfma_f32_16x16x32_bf16 v[160:163], a[216:219], a[244:247], v[160:163]\n"
    "v_accvgpr_write_b32 a155, 0\n"
    "s_waitcnt lgkmcnt(2)\n"
    "v_mfma_f32_16x16x32_bf16 v[4:7], a[220:223], a[224:227], v[4:7]\n"
    "ds_read_b64_tr_b16 a[216:217], v195 offset:1024\n"
    "v_accvgpr_write_b32 a156, 0\n"
    "v_mfma_f32_16x16x32_bf16 v[36:39], a[220:223], a[228:231], v[36:39]\n"
    "buffer_load_dword v243, v210, s[44:47], %[tw] offen\n"
    "v_accvgpr_write_b32 a157, 0\n"
    "v_mfma_f32_16x16x32_bf16 v[68:71], a[220:223], a[232:235], v[68:71]\n"
    "ds_read_b64_tr_b16 a[218:219], v195 offset:1280\n"
    "v_accvgpr_write_b32 a158, 0\n"
    "v_mfma_f32_16x16x32_bf16 v[100:103], a[220:223], a[236:239], v[100:103]\n"
    "v_accvgpr_write_b32 a159, 0\n"
    "v_mfma_f32_16x16x32_bf16 v[132:135], a[220:223], a[240:243], v[132:135]\n"
    "v_accvgpr_write_b32 a160, 0\n"
    "v_mfma_f32_16x16x32_bf16 v[164:167], a[220:223], a[244:247], v[164:167]\n"
    "v_accvgpr_write_b32 a161, 0\n"
    "v_mfma_f32_16x16x32_bf16 v[8:11], a[248:251], a[224:227], v[8:11]\n"
    "ds_read_b64_tr_b16 a[220:221], v196 offset:1024\n"
    "v_accvgpr_write_b32 a162, 0\n"
    "v_mfma_f32_16x16x32_bf16 v[40:43], a[248:251], a[228:231], v[40:43]\n"
    "v_accvgpr_write_b32 a163, 0\n"
    "v_mfma_f32_16x16x32_bf16 v[72:75], a[248:251], a[232:235], v[72:75]\n"
    "ds_read_b64_tr_b16 a[222:223], v196 offset:1280\n"
    "v_accvgpr_write_b32 a164, 0\n"
    "v_mfma_f32_16x16x32_bf16 v[104:107], a[248:251], a[236:239], v[104:107]\n"
    "v_accvgpr_write_b32 a165, 0\n"
    "v_mfma_f32_16x16x32_bf16 v[136:139], a[248:251], a[240:243], v[136:139]\n"
    "v_accvgpr_write_b32 a166, 0\n"
    "v_mfma_f32_16x16x32_bf16 v[168:171], a[248:251], a[244:247], v[168:171]\n"
    "v_accvgpr_write_b32 a167, 0\n"
    "s_waitcnt lgkmcnt(2)\n"
    "v_mfma_f32_16x16x32_bf16 v[12:15], a[252:255], a[224:227], v[12:15]\n"
    "ds_read_b64_tr_b16 a[248:249], v195 offset:1536\n"
    "v_accvgpr_write_b32 a168, 0\n"
    "v_mfma_f32_16x16x32_bf16 v[44:47], a[252:255], a[228:231], v[44:47]\n"
    "v_accvgpr_write_b32 a169, 0\n"
    "v_mfma_f32_16x16x32_bf16 v[76:79], a[252:255], a[232:235], v[76:79]\n"
    "ds_read_b64_tr_b16 a[250:251], v195 offset:1792\n"
    "v_accvgpr_write_b32 a170, 0\n"
    "v_mfma_f32_16x16x32_bf16 v[108:111], a[252:255], a[236:239], v[108:111]\n"
    "v_accvgpr_write_b32 a171, 0\n"
    "v_mfma_f32_16x16x32_bf16 v[140:143], a[252:255], a[240:243], v[140:143]\n"
    "v_accvgpr_write_b32 a172, 0\n"
    "v_mfma_f32_16x16x32_bf16 v[172:175], a[252:255], a[244:247], v[172:175]\n"
    "v_accvgpr_write_b32 a173, 0\n"
    "v_mfma_f32_16x16x32_bf16 v[16:19], a[216:219], a[224:227], v[16:19]\n"
    "ds_read_b64_tr_b16 a[252:253], v196 offset:1536\n"
    "v_accvgpr_write_b32 a174, 0\n"
    "v_mfma_f32_16x16x32_bf16 v[48:51], a[216:219], a[228:231], v[48:51]\n"
    "v_accvgpr_write_b32 a175, 0\n"
    "v_mfma_f32_16x16x32_bf16 v[80:83], a[216:219], a[232:235], v[80:83]\n"
    "ds_read_b64_tr_b16 a[254:255], v196 offset:1792\n"
    "v_accvgpr_write_b32 a176, 0\n"
    "v_mfma_f32_16x16x32_bf16 v[112:115], a[216:219], a[236:239], v[112:115]\n"
    "v_accvgpr_write_b32 a177, 0\n"
    "v_mfma_f32_16x16x32_bf16 v[144:147], a[216:219], a[240:243], v[144:147]\n"
    "v_accvgpr_write_b32 a178, 0\n"
    "v_mfma_f32_16x16x32_bf16 v[176:179], a[216:219], a[244:247], v[176:179]\n"
    "v_accvgpr_write_b32 a179, 0\n"
    "s_waitcnt lgkmcnt(2)\n"
    "v_mfma_f32_16x16x32_bf16 v[20:23], a[220:223], a[224:227], v[20:23]\n"
    "v_accvgpr_write_b32 a180, 0\n"
    "v_mfma_f32_16x16x32_bf16 v[52:55], a[220:223], a[228:231], v[52:55]\n"
    "v_accvgpr_write_b32 a181, 0\n"
    "v_mfma_f32_16x16x32_bf16 v[84:87], a[220:223], a[232:235], v[84:87]\n"
    "s_waitcnt vmcnt(19) lgkmcnt(0)\n"
    "s_barrier\n"
    "v_mfma_f32_16x16x32_bf16 v[116:119], a[220:223], a[236:239], v[116:119]\n"
    "ds_read_b64_tr_b16 a[216:217], v195 offset:16384\n"
    "v_accvgpr_write_b32 a182, 0\n"
    "v_mfma_f32_16x16x32_bf16 v[148:151], a[220:223], a[240:243], v[148:151]\n"
    "ds_read_b64_tr_b16 a[218:219], v195 offset:16640\n"
    "v_accvgpr_write_b32 a183, 0\n"
    "v_mfma_f32_16x16x32_bf16 v[180:183], a[220:223], a[244:247], v[180:183]\n"
    "ds_read_b128 a[192:195], v200 offset:0\n"
    "v_accvgpr_write_b32 a184, 0\n"
    "v_mfma_f32_16x16x32_bf16 v[24:27], a[248:251], a[224:227], v[24:27]\n"
    "ds_read_b128 a[196:199], v200 offset:2048\n"
    "v_accvgpr_write_b32 a185, 0\n"
    "v_mfma_f32_16x16x32_bf16 v[56:59], a[248:251], a[228:231], v[56:59]\n"
    "ds_read_b128 a[200:203], v200 offset:4096\n"
    "v_accvgpr_write_b32 a186, 0\n"
    "v_mfma_f32_16x16x32_bf16 v[88:91], a[248:251], a[232:235], v[88:91]\n"
    "ds_read_b128 a[204:207], v200 offset:6144\n"
    "v_accvgpr_write_b32 a187, 0\n"
    "v_mfma_f32_16x16x32_bf16 v[120:123], a[248:251], a[236:239], v[120:123]\n"
    "ds_read_b128 a[208:211], v200 offset:8192\n"
    "v_accvgpr_write_b32 a188, 0\n"
    "v_mfma_f32_16x16x32_bf16 v[152:155], a[248:251], a[240:243], v[152:155]\n"
    "ds_read_b128 a[212:215], v200 offset:10240\n"
    "s_add_u32 m0, s65, 32768\n"
    "v_mfma_f32_16x16x32_bf16 v[184:187], a[248:251], a[244:247], v[184:187]\n"
    "buffer_load_dwordx4 v204, s[44:47], s58 offen lds\n"
    "v_accvgpr_write_b32 a189, 0\n"
    "v_mfma_f32_16x16x32_bf16 v[28:31], a[252:255], a[224:227], v[28:31]\n"
    "ds_read_b64_tr_b16 a[220:221], v196 offset:16384\n"
    "s_add_u32 s58, s58, 0x10000\n"
    "v_mfma_f32_16x16x32_bf16 v[60:63], a[252:255], a[228:231], v[60:63]\n"
    "ds_read_b64_tr_b16 a[222:223], v196 offset:16640\n"
    "s_add_u32 m0, s65, 34816\n"
    "v_mfma_f32_16x16x32_bf16 v[92:95], a[252:255], a[232:235], v[92:95]\n"
    "buffer_load_dwordx4 v204, s[44:47], s58 offen lds\n"
    "v_accvgpr_write_b32 a190, 0\n"
    "v_mfma_f32_16x16x32_bf16 v[124:127], a[252:255], a[236:239], v[124:127]\n"
    "ds_read_b64_tr_b16 a[248:249], v195 offset:16896\n"
    "s_add_u32 s58, s58, 0x10000\n"
    "v_mfma_f32_16x16x32_bf16 v[156:159], a[252:255], a[240:243], v[156:159]\n"
    "ds_read_b64_tr_b16 a[250:251], v195 offset:17152\n"
    "s_add_u32 m0, s65, 36864\n"
    "v_mfma_f32_16x16x32_bf16 v[188:191], a[252:255], a[244:247], v[188:191]\n"
    "buffer_load_dwordx4 v205, s[44:47], s58 offen lds\n"
    "v_accvgpr_write_b32 a191, 0\n"
    "s_waitcnt lgkmcnt(4)\n"
    "v_mfma_f32_16x16x32_bf16 v[0:3], a[216:219], a[192:195], v[0:3]\n"
    "ds_read_b64_tr_b16 a[252:253], v196 offset:16896\n"
    "s_add_u32 s58, s58, 0x10000\n"
    "v_mfma_f32_16x16x32_bf16 v[32:35], a[216:219], a[196:199], v[32:35]\n"
    "v_mfma_f32_16x16x32_bf16 v[64:67], a[216:219], a[200:203], v[64:67]\n"
    "ds_read_b64_tr_b16 a[254:255], v196 offset:17152\n"
    "s_add_u32 m0, s65, 38912\n"
    "v_mfma_f32_16x16x32_bf16 v[96:99], a[216:219], a[204:207], v[96:99]\n"
    "buffer_load_dwordx4 v205, s[44:47], s58 offen lds\n"
    "v_mfma_f32_16x16x32_bf16 v[128:131], a[216:219], a[208:211], v[128:131]\n"
    "s_add_u32 s58, s58, 0x10000\n"
    "v_mfma_f32_16x16x32_bf16 v[160:163], a[216:219], a[212:215], v[160:163]\n"
    "s_add_u32 m0, s69, 139264\n"
    "s_waitcnt lgkmcnt(2)\n"
    "v_mfma_f32_16x16x32_bf16 v[4:7], a[220:223], a[192:195], v[4:7]\n"
    "ds_read_b64_tr_b16 a[216:217], v195 offset:17408\n"
    "s_add_u32 s59, s61, 640\n"
    "v_mfma_f32_16x16x32_bf16 v[36:39], a[220:223], a[196:199], v[36:39]\n"
    "buffer_load_dwordx4 v206, s[48:51], s59 offen lds\n"
    "v_mfma_f32_16x16x32_bf16 v[68:71], a[220:223], a[200:203], v[68:71]\n"
    "ds_read_b64_tr_b16 a[218:219], v195 offset:17664\n"
    "v_mfma_f32_16x16x32_bf16 v[100:103], a[220:223], a[204:207], v[100:103]\n"
    "s_add_u32 m0, s69, 140288\n"
    "v_mfma_f32_16x16x32_bf16 v[132:135], a[220:223], a[208:211], v[132:135]\n"
    "s_add_u32 s59, s61, 9856\n"
    "v_mfma_f32_16x16x32_bf16 v[164:167], a[220:223], a[212:215], v[164:167]\n"
    "buffer_load_dwordx4 v207, s[48:51], s59 offen lds\n"
    "v_mfma_f32_16x16x32_bf16 v[8:11], a[248:251], a[192:195], v[8:11]\n"
    "ds_read_b64_tr_b16 a[220:221], v196 offset:17408\n"
    "v_mfma_f32_16x16x32_bf16 v[40:43], a[248:251], a[196:199], v[40:43]\n"
    "s_add_u32 m0, s69, 141312\n"
    "v_mfma_f32_16x16x32_bf16 v[72:75], a[248:251], a[200:203], v[72:75]\n"
    "ds_read_b64_tr_b16 a[222:223], v196 offset:17664\n"
    "s_add_u32 s59, s61, 19072\n"
    "v_mfma_f32_16x16x32_bf16 v[104:107], a[248:251], a[204:207], v[104:107]\n"
    "buffer_load_dwordx4 v206, s[48:51], s59 offen lds\n"
    "v_mfma_f32_16x16x32_bf16 v[136:139], a[248:251], a[208:211], v[136:139]\n"
    "v_mfma_f32_16x16x32_bf16 v[168:171], a[248:251], a[212:215], v[168:171]\n"
    "s_add_u32 m0, s69, 142336\n"
    "s_waitcnt lgkmcnt(2)\n"
    "v_mfma_f32_16x16x32_bf16 v[12:15], a[252:255], a[192:195], v[12:15]\n"
    "ds_read_b64_tr_b16 a[248:249], v195 offset:17920\n"
    "s_add_u32 s59, s61, 28288\n"
    "v_mfma_f32_16x16x32_bf16 v[44:47], a[252:255], a[196:199], v[44:47]\n"
    "buffer_load_dwordx4 v207, s[48:51], s59 offen lds\n"
    "v_mfma_f32_16x16x32_bf16 v[76:79], a[252:255], a[200:203], v[76:79]\n"
    "ds_read_b64_tr_b16 a[250:251], v195 offset:18176\n"
    "v_mfma_f32_16x16x32_bf16 v[108:111], a[252:255], a[204:207], v[108:111]\n"
    "s_add_u32 m0, s69, 143360\n"
    "v_mfma_f32_16x16x32_bf16 v[140:143], a[252:255], a[208:211], v[140:143]\n"
    "s_add_u32 s59, s61, 37504\n"
    "v_mfma_f32_16x16x32_bf16 v[172:175], a[252:255], a[212:215], v[172:175]\n"
    "buffer_load_dwordx4 v206, s[48:51], s59 offen lds\n"
    "v_mfma_f32_16x16x32_bf16 v[16:19], a[216:219], a[192:195], v[16:19]\n"
    "ds_read_b64_tr_b16 a[252:253], v196 offset:17920\n"
    "v_mfma_f32_16x16x32_bf16 v[48:51], a[216:219], a[196:199], v[48:51]\n"
    "s_add_u32 m0, s69, 144384\n"
    "v_mfma_f32_16x16x32_bf16 v[80:83], a[216:219], a[200:203], v[80:83]\n"
    "ds_read_b64_tr_b16 a[254:255], v196 offset:18176\n"
    "s_add_u32 s59, s61, 46720\n"
    "v_mfma_f32_16x16x32_bf16 v[112:115], a[216:219], a[204:207], v[112:115]\n"
    "buffer_load_dwordx4 v207, s[48:51], s59 offen lds\n"
    "v_mfma_f32_16x16x32_bf16 v[144:147], a[216:219], a[208:211], v[144:147]\n"
    "v_mfma_f32_16x16x32_bf16 v[176:179], a[216:219], a[212:215], v[176:179]\n"
    "s_waitcnt lgkmcnt(2)\n"
    "v_mfma_f32_16x16x32_bf16 v[20:23], a[220:223], a[192:195], v[20:23]\n"
    "v_mfma_f32_16x16x32_bf16 v[52:55], a[220:223], a[196:199], v[52:55]\n"
    "v_mfma_f32_16x16x32_bf16 v[84:87], a[220:223], a[200:203], v[84:87]\n"
    "s_waitcnt vmcnt(25) lgkmcnt(0)\n"
    "s_barrier\n"
    "v_mfma_f32_16x16x32_bf16 v[116:119], a[220:223], a[204:207], v[116:119]\n"
    "ds_read_b64_tr_b16 a[216:217], v197 offset:0\n"
    "v_mfma_f32_16x16x32_bf16 v[148:151], a[220:223], a[208:211], v[148:151]\n"
    "ds_read_b64_tr_b16 a[218:219], v197 offset:256\n"
    "v_mfma_f32_16x16x32_bf16 v[180:183], a[220:223], a[212:215], v[180:183]\n"
    "ds_read_b128 a[224:227], v200 offset:512\n"
    "v_mfma_f32_16x16x32_bf16 v[24:27], a[248:251], a[192:195], v[24:27]\n"
    "ds_read_b128 a[228:231], v200 offset:2560\n"
    "v_mfma_f32_16x16x32_bf16 v[56:59], a[248:251], a[196:199], v[56:59]\n"
    "ds_read_b128 a[232:235], v200 offset:4608\n"
    "v_mfma_f32_16x16x32_bf16 v[88:91], a[248:251], a[200:203], v[88:91]\n"
    "ds_read_b128 a[236:239], v200 offset:6656\n"
    "v_mfma_f32_16x16x32_bf16 v[120:123], a[248:251], a[204:207], v[120:123]\n"
    "ds_read_b128 a[240:243], v200 offset:8704\n"
    "v_mfma_f32_16x16x32_bf16 v[152:155], a[248:251], a[208:211], v[152:155]\n"
    "ds_read_b128 a[244:247], v200 offset:10752\n"
    "s_add_u32 m0, s65, 49152\n"
    "v_mfma_f32_16x16x32_bf16 v[184:187], a[248:251], a[212:215], v[184:187]\n"
    "buffer_load_dwordx4 v204, s[44:47], s58 offen lds\n"
    "v_mfma_f32_16x16x32_bf16 v[28:31], a[252:255], a[192:195], v[28:31]\n"
    "ds_read_b64_tr_b16 a[220:221], v198 offset:0\n"
    "s_add_u32 s58, s58, 0x10000\n"
    "v_mfma_f32_16x16x32_bf16 v[60:63], a[252:255], a[196:199], v[60:63]\n"
    "ds_read_b64_tr_b16 a[222:223], v198 offset:256\n"
    "s_add_u32 m0, s65, 51200\n"
    "v_mfma_f32_16x16x32_bf16 v[92:95], a[252:255], a[200:203], v[92:95]\n"
    "buffer_load_dwordx4 v204, s[44:47], s58 offen lds\n"
    "v_mfma_f32_16x16x32_bf16 v[124:127], a[252:255], a[204:207], v[124:127]\n"
    "ds_read_b64_tr_b16 a[248:249], v197 offset:512\n"
    "s_add_u32 s58, s58, 0x10000\n"
    "v_mfma_f32_16x16x32_bf16 v[156:159], a[252:255], a[208:211], v[156:159]\n"
    "ds_read_b64_tr_b16 a[250:251], v197 offset:768\n"
    "s_add_u32 m0, s65, 53248\n"
    "v_mfma_f32_16x16x32_bf16 v[188:191], a[252:255], a[212:215], v[188:191]\n"
    "buffer_load_dwordx4 v205, s[44:47], s58 offen lds\n"
    "s_waitcnt lgkmcnt(4)\n"
    "v_mfma_f32_16x16x32_bf16 v[0:3], a[216:219], a[224:227], v[0:3]\n"
    "ds_read_b64_tr_b16 a[252:253], v198 offset:512\n"
    "s_add_u32 s58, s58, 0x10000\n"
    "v_mfma_f32_16x16x32_bf16 v[32:35], a[216:219], a[228:231], v[32:35]\n"
    "v_mfma_f32_16x16x32_bf16 v[64:67], a[216:219], a[232:235], v[64:67]\n"
    "ds_read_b64_tr_b16 a[254:255], v198 offset:768\n"
    "s_add_u32 m0, s65, 55296\n"
    "v_mfma_f32_16x16x32_bf16 v[96:99], a[216:219], a[236:239], v[96:99]\n"
    "buffer_load_dwordx4 v205, s[44:47], s58 offen lds\n"
    "v_mfma_f32_16x16x32_bf16 v[128:131], a[216:219], a[240:243], v[128:131]\n"
    "s_add_u32 s58, s58, 0x10000\n"
    "v_mfma_f32_16x16x32_bf16 v[160:163], a[216:219], a[244:247], v[160:163]\n"
    "s_waitcnt lgkmcnt(2)\n"
    "v_mfma_f32_16x16x32_bf16 v[4:7], a[220:223], a[224:227], v[4:7]\n"
    "ds_read_b64_tr_b16 a[216:217], v197 offset:1024\n"
    "v_mfma_f32_16x16x32_bf16 v[36:39], a[220:223], a[228:231], v[36:39]\n"
    "v_mfma_f32_16x16x32_bf16 v[68:71], a[220:223], a[232:235], v[68:71]\n"
    "ds_read_b64_tr_b16 a[218:219], v197 offset:1280\n"
    "v_mfma_f32_16x16x32_bf16 v[100:103], a[220:223], a[236:239], v[100:103]\n"
    "v_mfma_f32_16x16x32_bf16 v[132:135], a[220:223], a[240:243], v[132:135]\n"
    "v_mfma_f32_16x16x32_bf16 v[164:167], a[220:223], a[244:247], v[164:167]\n"
    "v_mfma_f32_16x16x32_bf16 v[8:11], a[248:251], a[224:227], v[8:11]\n"
    "ds_read_b64_tr_b16 a[220:221], v198 offset:1024\n"
    "v_mfma_f32_16x16x32_bf16 v[40:43], a[248:251], a[228:231], v[40:43]\n"
    "v_mfma_f32_16x16x32_bf16 v[72:75], a[248:251], a[232:235], v[72:75]\n"
    "ds_read_b64_tr_b16 a[222:223], v198 offset:1280\n"
    "v_mfma_f32_16x16x32_bf16 v[104:107], a[248:251], a[236:239], v[104:107]\n"
    "v_mfma_f32_16x16x32_bf16 v[136:139], a[248:251], a[240:243], v[136:139]\n"
    "v_mfma_f32_16x16x32_bf16 v[168:171], a[248:251], a[244:247], v[168:171]\n"
    "s_waitcnt lgkmcnt(2)\n"
    "v_mfma_f32_16x16x32_bf16 v[12:15], a[252:255], a[224:227], v[12:15]\n"
    "ds_read_b64_tr_b16 a[248:249], v197 offset:1536\n"
    "v_mfma_f32_16x16x32_bf16 v[44:47], a[252:255], a[228:231], v[44:47]\n"
    "v_mfma_f32_16x16x32_bf16 v[76:79], a[252:255], a[232:235], v[76:79]\n"
    "ds_read_b64_tr_b16 a[250:251], v197 offset:1792\n"
    "v_mfma_f32_16x16x32_bf16 v[108:111], a[252:255], a[236:239], v[108:111]\n"
    "v_mfma_f32_16x16x32_bf16 v[140:143], a[252:255], a[240:243], v[140:143]\n"
    "v_mfma_f32_16x16x32_bf16 v[172:175], a[252:255], a[244:247], v[172:175]\n"
    "v_mfma_f32_16x16x32_bf16 v[16:19], a[216:219], a[224:227], v[16:19]\n"
    "ds_read_b64_tr_b16 a[252:253], v198 offset:1536\n"
    "v_mfma_f32_16x16x32_bf16 v[48:51], a[216:219], a[228:231], v[48:51]\n"
    "v_mfma_f32_16x16x32_bf16 v[80:83], a[216:219], a[232:235], v[80:83]\n"
    "ds_read_b64_tr_b16 a[254:255], v198 offset:1792\n"
    "v_mfma_f32_16x16x32_bf16 v[112:115], a[216:219], a[236:239], v[112:115]\n"
    "v_mfma_f32_16x16x32_bf16 v[144:147], a[216:219], a[240:243], v[144:147]\n"
    "v_mfma_f32_16x16x32_bf16 v[176:179], a[216:219], a[244:247], v[176:179]\n"
    "s_waitcnt lgkmcnt(2)\n"
    "v_mfma_f32_16x16x32_bf16 v[20:23], a[220:223], a[224:227], v[20:23]\n"
    "v_mfma_f32_16x16x32_bf16 v[52:55], a[220:223], a[228:231], v[52:55]\n"
    "v_mfma_f32_16x16x32_bf16 v[84:87], a[220:223], a[232:235], v[84:87]\n"
    "s_waitcnt vmcnt(19) lgkmcnt(0)\n"
    "s_barrier\n"
    "v_mfma_f32_16x16x32_bf16 v[116:119], a[220:223], a[236:239], v[116:119]\n"
    "ds_read_b64_tr_b16 a[216:217], v195 offset:32768\n"
    "v_mfma_f32_16x16x32_bf16 v[148:151], a[220:223], a[240:243], v[148:151]\n"
    "ds_read_b64_tr_b16 a[218:219], v195 offset:33024\n"
    "v_mfma_f32_16x16x32_bf16 v[180:183], a[220:223], a[244:247], v[180:183]\n"
    "ds_read_b128 a[192:195], v199 offset:0\n"
    "v_mfma_f32_16x16x32_bf16 v[24:27], a[248:251], a[224:227], v[24:27]\n"
    "ds_read_b128 a[196:199], v199 offset:2048\n"
    "v_mfma_f32_16x16x32_bf16 v[56:59], a[248:251], a[228:231], v[56:59]\n"
    "ds_read_b128 a[200:203], v199 offset:4096\n"
    "v_mfma_f32_16x16x32_bf16 v[88:91], a[248:251], a[232:235], v[88:91]\n"
    "ds_read_b128 a[204:207], v199 offset:6144\n"
    "v_mfma_f32_16x16x32_bf16 v[120:123], a[248:251], a[236:239], v[120:123]\n"
    "ds_read_b128 a[208:211], v199 offset:8192\n"
    "v_mfma_f32_16x16x32_bf16 v[152:155], a[248:251], a[240:243], v[152:155]\n"
    "ds_read_b128 a[212:215], v199 offset:10240\n"
    "s_add_u32 m0, s65, 98304\n"
    "v_mfma_f32_16x16x32_bf16 v[184:187], a[248:251], a[244:247], v[184:187]\n"
    "buffer_load_dwordx4 v204, s[44:47], s58 offen lds\n"
    "v_mfma_f32_16x16x32_bf16 v[28:31], a[252:255], a[224:227], v[28:31]\n"
    "ds_read_b64_tr_b16 a[220:221], v196 offset:32768\n"
    "s_add_u32 s58, s58, 0x10000\n"
    "v_mfma_f32_16x16x32_bf16 v[60:63], a[252:255], a[228:231], v[60:63]\n"
    "ds_read_b64_tr_b16 a[222:223], v196 offset:33024\n"
    "s_add_u32 m0, s65, 100352\n"
    "v_mfma_f32_16x16x32_bf16 v[92:95], a[252:255], a[232:235], v[92:95]\n"
    "buffer_load_dwordx4 v204, s[44:47], s58 offen lds\n"
    "v_mfma_f32_16x16x32_bf16 v[124:127], a[252:255], a[236:239], v[124:127]\n"
    "ds_read_b64_tr_b16 a[248:249], v195 offset:33280\n"
    "s_add_u32 s58, s58, 0x10000\n"
    "v_mfma_f32_16x16x32_bf16 v[156:159], a[252:255], a[240:243], v[156:159]\n"
    "ds_read_b64_tr_b16 a[250:251], v195 offset:33536\n"
    "s_add_u32 m0, s65, 102400\n"
    "v_mfma_f32_16x16x32_bf16 v[188:191], a[252:255], a[244:247], v[188:191]\n"
    "buffer_load_dwordx4 v205, s[44:47], s58 offen lds\n"
    "s_waitcnt lgkmcnt(4)\n"
    "v_mfma_f32_16x16x32_bf16 v[0:3], a[216:219], a[192:195], v[0:3]\n"
    "ds_read_b64_tr_b16 a[252:253], v196 offset:33280\n"
    "s_add_u32 s58, s58, 0x10000\n"
    "v_mfma_f32_16x16x32_bf16 v[32:35], a[216:219], a[196:199], v[32:35]\n"
    "v_mfma_f32_16x16x32_bf16 v[64:67], a[216:219], a[200:203], v[64:67]\n"
    "ds_read_b64_tr_b16 a[254:255], v196 offset:33536\n"
    "s_add_u32 m0, s65, 104448\n"
    "v_mfma_f32_16x16x32_bf16 v[96:99], a[216:219], a[204:207], v[96:99]\n"
    "buffer_load_dwordx4 v205, s[44:47], s58 offen lds\n"
    "v_mfma_f32_16x16x32_bf16 v[128:131], a[216:219], a[208:211], v[128:131]\n"
    "s_add_u32 s58, s58, 0x10000\n"
    "v_mfma_f32_16x16x32_bf16 v[160:163], a[216:219], a[212:215], v[160:163]\n"
    "s_add_u32 m0, s69, 0\n"
    "s_waitcnt lgkmcnt(2)\n"
    "v_mfma_f32_16x16x32_bf16 v[4:7], a[220:223], a[192:195], v[4:7]\n"
    "ds_read_b64_tr_b16 a[216:217], v195 offset:33792\n"
    "s_add_u32 s59, s61, 768\n"
    "v_mfma_f32_16x16x32_bf16 v[36:39], a[220:223], a[196:199], v[36:39]\n"
    "buffer_load_dwordx4 v206, s[48:51], s59 offen lds\n"
    "v_mfma_f32_16x16x32_bf16 v[68:71], a[220:223], a[200:203], v[68:71]\n"
    "ds_read_b64_tr_b16 a[218:219], v195 offset:34048\n"
    "v_mfma_f32_16x16x32_bf16 v[100:103], a[220:223], a[204:207], v[100:103]\n"
    "s_add_u32 m0, s69, 1024\n"
    "v_mfma_f32_16x16x32_bf16 v[132:135], a[220:223], a[208:211], v[132:135]\n"
    "s_add_u32 s59, s61, 9984\n"
    "v_mfma_f32_16x16x32_bf16 v[164:167], a[220:223], a[212:215], v[164:167]\n"
    "buffer_load_dwordx4 v207, s[48:51], s59 offen lds\n"
    "v_mfma_f32_16x16x32_bf16 v[8:11], a[248:251], a[192:195], v[8:11]\n"
    "ds_read_b64_tr_b16 a[220:221], v196 offset:33792\n"
    "v_mfma_f32_16x16x32_bf16 v[40:43], a[248:251], a[196:199], v[40:43]\n"
    "s_add_u32 m0, s69, 2048\n"
    "v_mfma_f32_16x16x32_bf16 v[72:75], a[248:251], a[200:203], v[72:75]\n"
    "ds_read_b64_tr_b16 a[222:223], v196 offset:34048\n"
    "s_add_u32 s59, s61, 19200\n"
    "v_mfma_f32_16x16x32_bf16 v[104:107], a[248:251], a[204:207], v[104:107]\n"
    "buffer_load_dwordx4 v206, s[48:51], s59 offen lds\n"
    "v_mfma_f32_16x16x32_bf16 v[136:139], a[248:251], a[208:211], v[136:139]\n"
    "v_mfma_f32_16x16x32_bf16 v[168:171], a[248:251], a[212:215], v[168:171]\n"
    "s_add_u32 m0, s69, 3072\n"
    "s_waitcnt lgkmcnt(2)\n"
    "v_mfma_f32_16x16x32_bf16 v[12:15], a[252:255], a[192:195], v[12:15]\n"
    "ds_read_b64_tr_b16 a[248:249], v195 offset:34304\n"
    "s_add_u32 s59, s61, 28416\n"
    "v_mfma_f32_16x16x32_bf16 v[44:47], a[252:255], a[196:199], v[44:47]\n"
    "buffer_load_dwordx4 v207, s[48:51], s59 offen lds\n"
    "v_mfma_f32_16x16x32_bf16 v[76:79], a[252:255], a[200:203], v[76:79]\n"
    "ds_read_b64_tr_b16 a[250:251], v195 offset:34560\n"
    "v_mfma_f32_16x16x32_bf16 v[108:111], a[252:255], a[204:207], v[108:111]\n"
    "s_add_u32 m0, s69, 4096\n"
    "v_mfma_f32_16x16x32_bf16 v[140:143], a[252:255], a[208:211], v[140:143]\n"
    "s_add_u32 s59, s61, 37632\n"
    "v_mfma_f32_16x16x32_bf16 v[172:175], a[252:255], a[212:215], v[172:175]\n"
    "buffer_load_dwordx4 v206, s[48:51], s59 offen lds\n"
    "v_mfma_f32_16x16x32_bf16 v[16:19], a[216:219], a[192:195], v[16:19]\n"
    "ds_read_b64_tr_b16 a[252:253], v196 offset:34304\n"
    "v_mfma_f32_16x16x32_bf16 v[48:51], a[216:219], a[196:199], v[48:51]\n"
    "s_add_u32 m0, s69, 5120\n"
    "v_mfma_f32_16x16x32_bf16 v[80:83], a[216:219], a[200:203], v[80:83]\n"
    "ds_read_b64_tr_b16 a[254:255], v196 offset:34560\n"
    "s_add_u32 s59, s61, 46848\n"
    "v_mfma_f32_16x16x32_bf16 v[112:115], a[216:219], a[204:207], v[112:115]\n"
    "buffer_load_dwordx4 v207, s[48:51], s59 offen lds\n"
    "v_mfma_f32_16x16x32_bf16 v[144:147], a[216:219], a[208:211], v[144:147]\n"
    "v_mfma_f32_16x16x32_bf16 v[176:179], a[216:219], a[212:215], v[176:179]\n"
    "s_waitcnt lgkmcnt(2)\n"
    "v_mfma_f32_16x16x32_bf16 v[20:23], a[220:223], a[192:195], v[20:23]\n"
    "v_mfma_f32_16x16x32_bf16 v[52:55], a[220:223], a[196:199], v[52:55]\n"
    "v_mfma_f32_16x16x32_bf16 v[84:87], a[220:223], a[200:203], v[84:87]\n"
    "s_waitcnt vmcnt(25) lgkmcnt(0)\n"
    "s_barrier\n"
    "v_mfma_f32_16x16x32_bf16 v[116:119], a[220:223], a[204:207], v[116:119]\n"
    "ds_read_b64_tr_b16 a[216:217], v195 offset:49152\n"
    "v_mfma_f32_16x16x32_bf16 v[148:151], a[220:223], a[208:211], v[148:151]\n"
    "ds_read_b64_tr_b16 a[218:219], v195 offset:49408\n"
    "v_mfma_f32_16x16x32_bf16 v[180:183], a[220:223], a[212:215], v[180:183]\n"
    "ds_read_b128 a[224:227], v199 offset:512\n"
    "v_mfma_f32_16x16x32_bf16 v[24:27], a[248:251], a[192:195], v[24:27]\n"
    "ds_read_b128 a[228:231], v199 offset:2560\n"
    "v_mfma_f32_16x16x32_bf16 v[56:59], a[248:251], a[196:199], v[56:59]\n"
    "ds_read_b128 a[232:235], v199 offset:4608\n"
    "v_mfma_f32_16x16x32_bf16 v[88:91], a[248:251], a[200:203], v[88:91]\n"
    "ds_read_b128 a[236:239], v199 offset:6656\n"
    "v_mfma_f32_16x16x32_bf16 v[120:123], a[248:251], a[204:207], v[120:123]\n"
    "ds_read_b128 a[240:243], v199 offset:8704\n"
    "v_mfma_f32_16x16x32_bf16 v[152:155], a[248:251], a[208:211], v[152:155]\n"
    "ds_read_b128 a[244:247], v199 offset:10752\n"
    "s_add_u32 m0, s65, 65536\n"
    "v_mfma_f32_16x16x32_bf16 v[184:187], a[248:251], a[212:215], v[184:187]\n"
    "buffer_load_dwordx4 v204, s[44:47], s58 offen lds\n"
    "v_mfma_f32_16x16x32_bf16 v[28:31], a[252:255], a[192:195], v[28:31]\n"
    "ds_read_b64_tr_b16 a[220:221], v196 offset:49152\n"
    "s_add_u32 s58, s58, 0x10000\n"
    "v_mfma_f32_16x16x32_bf16 v[60:63], a[252:255], a[196:199], v[60:63]\n"
    "ds_read_b64_tr_b16 a[222:223], v196 offset:49408\n"
    "s_add_u32 m0, s65, 67584\n"
    "v_mfma_f32_16x16x32_bf16 v[92:95], a[252:255], a[200:203], v[92:95]\n"
    "buffer_load_dwordx4 v204, s[44:47], s58 offen lds\n"
    "v_mfma_f32_16x16x32_bf16 v[124:127], a[252:255], a[204:207], v[124:127]\n"
    "ds_read_b64_tr_b16 a[248:249], v195 offset:49664\n"
    "s_add_u32 s58, s58, 0x10000\n"
    "v_mfma_f32_16x16x32_bf16 v[156:159], a[252:255], a[208:211], v[156:159]\n"
    "ds_read_b64_tr_b16 a[250:251], v195 offset:49920\n"
    "s_add_u32 m0, s65, 69632\n"
    "v_mfma_f32_16x16x32_bf16 v[188:191], a[252:255], a[212:215], v[188:191]\n"
    "buffer_load_dwordx4 v205, s[44:47], s58 offen lds\n"
    "s_waitcnt lgkmcnt(4)\n"
    "v_mfma_f32_16x16x32_bf16 v[0:3], a[216:219], a[224:227], v[0:3]\n"
    "ds_read_b64_tr_b16 a[252:253], v196 offset:49664\n"
    "s_add_u32 s58, s58, 0x10000\n"
    "v_mfma_f32_16x16x32_bf16 v[32:35], a[216:219], a[228:231], v[32:35]\n"
    "v_mfma_f32_16x16x32_bf16 v[64:67], a[216:219], a[232:235], v[64:67]\n"
    "ds_read_b64_tr_b16 a[254:255], v196 offset:49920\n"
    "s_add_u32 m0, s65, 71680\n"
    "v_mfma_f32_16x16x32_bf16 v[96:99], a[216:219], a[236:239], v[96:99]\n"
    "buffer_load_dwordx4 v205, s[44:47], s58 offen lds\n"
    "v_mfma_f32_16x16x32_bf16 v[128:131], a[216:219], a[240:243], v[128:131]\n"
    "s_add_u32 s58, s58, 0x10000\n"
    "v_mfma_f32_16x16x32_bf16 v[160:163], a[216:219], a[244:247], v[160:163]\n"
    "s_waitcnt lgkmcnt(2)\n"
    "v_mfma_f32_16x16x32_bf16 v[4:7], a[220:223], a[224:227], v[4:7]\n"
    "ds_read_b64_tr_b16 a[216:217], v195 offset:50176\n"
    "v_mfma_f32_16x16x32_bf16 v[36:39], a[220:223], a[228:231], v[36:39]\n"
    "v_mfma_f32_16x16x32_bf16 v[68:71], a[220:223], a[232:235], v[68:71]\n"
    "ds_read_b64_tr_b16 a[218:219], v195 offset:50432\n"
    "v_mfma_f32_16x16x32_bf16 v[100:103], a[220:223], a[236:239], v[100:103]\n"
    "v_mfma_f32_16x16x32_bf16 v[132:135], a[220:223], a[240:243], v[132:135]\n"
    "v_mfma_f32_16x16x32_bf16 v[164:167], a[220:223], a[244:247], v[164:167]\n"
    "v_mfma_f32_16x16x32_bf16 v[8:11], a[248:251], a[224:227], v[8:11]\n"
    "ds_read_b64_tr_b16 a[220:221], v196 offset:50176\n"
    "v_mfma_f32_16x16x32_bf16 v[40:43], a[248:251], a[228:231], v[40:43]\n"
    "v_mfma_f32_16x16x32_bf16 v[72:75], a[248:251], a[232:235], v[72:75]\n"
    "ds_read_b64_tr_b16 a[222:223], v196 offset:50432\n"
    "v_mfma_f32_16x16x32_bf16 v[104:107], a[248:251], a[236:239], v[104:107]\n"
    "v_mfma_f32_16x16x32_bf16 v[136:139], a[248:251], a[240:243], v[136:139]\n"
    "v_mfma_f32_16x16x32_bf16 v[168:171], a[248:251], a[244:247], v[168:171]\n"
    "s_waitcnt lgkmcnt(2)\n"
    "v_mfma_f32_16x16x32_bf16 v[12:15], a[252:255], a[224:227], v[12:15]\n"
    "ds_read_b64_tr_b16 a[248:249], v195 offset:50688\n"
    "v_mfma_f32_16x16x32_bf16 v[44:47], a[252:255], a[228:231], v[44:47]\n"
    "v_mfma_f32_16x16x32_bf16 v[76:79], a[252:255], a[232:235], v[76:79]\n"
    "ds_read_b64_tr_b16 a[250:251], v195 offset:50944\n"
    "v_mfma_f32_16x16x32_bf16 v[108:111], a[252:255], a[236:239], v[108:111]\n"
    "v_mfma_f32_16x16x32_bf16 v[140:143], a[252:255], a[240:243], v[140:143]\n"
    "v_mfma_f32_16x16x32_bf16 v[172:175], a[252:255], a[244:247], v[172:175]\n"
    "v_mfma_f32_16x16x32_bf16 v[16:19], a[216:219], a[224:227], v[16:19]\n"
    "ds_read_b64_tr_b16 a[252:253], v196 offset:50688\n"
    "v_mfma_f32_16x16x32_bf16 v[48:51], a[216:219], a[228:231], v[48:51]\n"
    "v_mfma_f32_16x16x32_bf16 v[80:83], a[216:219], a[232:235], v[80:83]\n"
    "ds_read_b64_tr_b16 a[254:255], v196 offset:50944\n"
    "v_mfma_f32_16x16x32_bf16 v[112:115], a[216:219], a[236:239], v[112:115]\n"
    "v_mfma_f32_16x16x32_bf16 v[144:147], a[216:219], a[240:243], v[144:147]\n"
    "v_mfma_f32_16x16x32_bf16 v[176:179], a[216:219], a[244:247], v[176:179]\n"
    "s_waitcnt lgkmcnt(2)\n"
    "v_mfma_f32_16x16x32_bf16 v[20:23], a[220:223], a[224:227], v[20:23]\n"
    "v_mfma_f32_16x16x32_bf16 v[52:55], a[220:223], a[228:231], v[52:55]\n"
    "v_mfma_f32_16x16x32_bf16 v[84:87], a[220:223], a[232:235], v[84:87]\n"
    "s_waitcnt vmcnt(18) lgkmcnt(0)\n"
    "s_barrier\n"
    "v_mfma_f32_16x16x32_bf16 v[116:119], a[220:223], a[236:239], v[116:119]\n"
    "ds_read_b64_tr_b16 a[216:217], v195 offset:0\n"
    "v_mfma_f32_16x16x32_bf16 v[148:151], a[220:223], a[240:243], v[148:151]\n"
    "ds_read_b64_tr_b16 a[218:219], v195 offset:256\n"
    "v_mfma_f32_16x16x32_bf16 v[180:183], a[220:223], a[244:247], v[180:183]\n"
    "ds_read_b128 a[192:195], v199 offset:24576\n"
    "v_mfma_f32_16x16x32_bf16 v[24:27], a[248:251], a[224:227], v[24:27]\n"
    "ds_read_b128 a[196:199], v199 offset:26624\n"
    "v_mfma_f32_16x16x32_bf16 v[56:59], a[248:251], a[228:231], v[56:59]\n"
    "ds_read_b128 a[200:203], v199 offset:28672\n"
    "v_mfma_f32_16x16x32_bf16 v[88:91], a[248:251], a[232:235], v[88:91]\n"
    "ds_read_b128 a[204:207], v199 offset:30720\n"
    "v_mfma_f32_16x16x32_bf16 v[120:123], a[248:251], a[236:239], v[120:123]\n"
    "ds_read_b128 a[208:211], v199 offset:32768\n"
    "v_mfma_f32_16x16x32_bf16 v[152:155], a[248:251], a[240:243], v[152:155]\n"
    "ds_read_b128 a[212:215], v199 offset:34816\n"
    "s_add_u32 m0, s65, 81920\n"
    "v_mfma_f32_16x16x32_bf16 v[184:187], a[248:251], a[244:247], v[184:187]\n"
    "buffer_load_dwordx4 v204, s[44:47], s58 offen lds\n"
    "v_mfma_f32_16x16x32_bf16 v[28:31], a[252:255], a[224:227], v[28:31]\n"
    "ds_read_b64_tr_b16 a[220:221], v196 offset:0\n"
    "s_add_u32 s58, s58, 0x10000\n"
    "v_mfma_f32_16x16x32_bf16 v[60:63], a[252:255], a[228:231], v[60:63]\n"
    "ds_read_b64_tr_b16 a[222:223], v196 offset:256\n"
    "s_add_u32 m0, s65, 83968\n"
    "v_mfma_f32_16x16x32_bf16 v[92:95], a[252:255], a[232:235], v[92:95]\n"
    "buffer_load_dwordx4 v204, s[44:47], s58 offen lds\n"
    "v_mfma_f32_16x16x32_bf16 v[124:127], a[252:255], a[236:239], v[124:127]\n"
    "ds_read_b64_tr_b16 a[248:249], v195 offset:512\n"
    "s_add_u32 s58, s58, 0x10000\n"
    "v_mfma_f32_16x16x32_bf16 v[156:159], a[252:255], a[240:243], v[156:159]\n"
    "ds_read_b64_tr_b16 a[250:251], v195 offset:768\n"
    "s_add_u32 m0, s65, 86016\n"
    "v_mfma_f32_16x16x32_bf16 v[188:191], a[252:255], a[244:247], v[188:191]\n"
    "buffer_load_dwordx4 v205, s[44:47], s58 offen lds\n"
    "s_waitcnt lgkmcnt(4)\n"
    "v_mfma_f32_16x16x32_bf16 v[0:3], a[216:219], a[192:195], v[0:3]\n"
    "ds_read_b64_tr_b16 a[252:253], v196 offset:512\n"
    "s_add_u32 s58, s58, 0x10000\n"
    "v_mfma_f32_16x16x32_bf16 v[32:35], a[216:219], a[196:199], v[32:35]\n"
    "v_mfma_f32_16x16x32_bf16 v[64:67], a[216:219], a[200:203], v[64:67]\n"
    "ds_read_b64_tr_b16 a[254:255], v196 offset:768\n"
    "s_add_u32 m0, s65, 88064\n"
    "v_mfma_f32_16x16x32_bf16 v[96:99], a[216:219], a[204:207], v[96:99]\n"
    "buffer_load_dwordx4 v205, s[44:47], s58 offen lds\n"
    "v_mfma_f32_16x16x32_bf16 v[128:131], a[216:219], a[208:211], v[128:131]\n"
    "s_add_u32 s58, s58, 0x10000\n"
    "v_mfma_f32_16x16x32_bf16 v[160:163], a[216:219], a[212:215], v[160:163]\n"
    "s_add_u32 m0, s69, 114688\n"
    "s_waitcnt lgkmcnt(2)\n"
    "v_mfma_f32_16x16x32_bf16 v[4:7], a[220:223], a[192:195], v[4:7]\n"
    "ds_read_b64_tr_b16 a[216:217], v195 offset:1024\n"
    "s_add_u32 s59, s61, 896\n"
    "v_mfma_f32_16x16x32_bf16 v[36:39], a[220:223], a[196:199], v[36:39]\n"
    "buffer_load_dwordx4 v206, s[48:51], s59 offen lds\n"
    "v_mfma_f32_16x16x32_bf16 v[68:71], a[220:223], a[200:203], v[68:71]\n"
    "ds_read_b64_tr_b16 a[218:219], v195 offset:1280\n"
    "v_mfma_f32_16x16x32_bf16 v[100:103], a[220:223], a[204:207], v[100:103]\n"
    "s_add_u32 m0, s69, 115712\n"
    "v_mfma_f32_16x16x32_bf16 v[132:135], a[220:223], a[208:211], v[132:135]\n"
    "s_add_u32 s59, s61, 10112\n"
    "v_mfma_f32_16x16x32_bf16 v[164:167], a[220:223], a[212:215], v[164:167]\n"
    "buffer_load_dwordx4 v207, s[48:51], s59 offen lds\n"
    "v_mfma_f32_16x16x32_bf16 v[8:11], a[248:251], a[192:195], v[8:11]\n"
    "ds_read_b64_tr_b16 a[220:221], v196 offset:1024\n"
    "v_mfma_f32_16x16x32_bf16 v[40:43], a[248:251], a[196:199], v[40:43]\n"
    "s_add_u32 m0, s69, 116736\n"
    "v_mfma_f32_16x16x32_bf16 v[72:75], a[248:251], a[200:203], v[72:75]\n"
    "ds_read_b64_tr_b16 a[222:223], v196 offset:1280\n"
    "s_add_u32 s59, s61, 19328\n"
    "v_mfma_f32_16x16x32_bf16 v[104:107], a[248:251], a[204:207], v[104:107]\n"
    "buffer_load_dwordx4 v206, s[48:51], s59 offen lds\n"
    "v_mfma_f32_16x16x32_bf16 v[136:139], a[248:251], a[208:211], v[136:139]\n"
    "v_mfma_f32_16x16x32_bf16 v[168:171], a[248:251], a[212:215], v[168:171]\n"
    "s_add_u32 m0, s69, 117760\n"
    "s_waitcnt lgkmcnt(2)\n"
    "v_mfma_f32_16x16x32_bf16 v[12:15], a[252:255], a[192:195], v[12:15]\n"
    "ds_read_b64_tr_b16 a[248:249], v195 offset:1536\n"
    "s_add_u32 s59, s61, 28544\n"
    "v_mfma_f32_16x16x32_bf16 v[44:47], a[252:255], a[196:199], v[44:47]\n"
    "buffer_load_dwordx4 v207, s[48:51], s59 offen lds\n"
    "v_mfma_f32_16x16x32_bf16 v[76:79], a[252:255], a[200:203], v[76:79]\n"
    "ds_read_b64_tr_b16 a[250:251], v195 offset:1792\n"
    "v_mfma_f32_16x16x32_bf16 v[108:111], a[252:255], a[204:207], v[108:111]\n"
    "s_add_u32 m0, s69, 118784\n"
    "v_mfma_f32_16x16x32_bf16 v[140:143], a[252:255], a[208:211], v[140:143]\n"
    "s_add_u32 s59, s61, 37760\n"
    "v_mfma_f32_16x16x32_bf16 v[172:175], a[252:255], a[212:215], v[172:175]\n"
    "buffer_load_dwordx4 v206, s[48:51], s59 offen lds\n"
    "v_mfma_f32_16x16x32_bf16 v[16:19], a[216:219], a[192:195], v[16:19]\n"
    "ds_read_b64_tr_b16 a[252:253], v196 offset:1536\n"
    "v_mfma_f32_16x16x32_bf16 v[48:51], a[216:219], a[196:199], v[48:51]\n"
    "s_add_u32 m0, s69, 119808\n"
    "v_mfma_f32_16x16x32_bf16 v[80:83], a[216:219], a[200:203], v[80:83]\n"
    "ds_read_b64_tr_b16 a[254:255], v196 offset:1792\n"
    "s_add_u32 s59, s61, 46976\n"
    "v_mfma_f32_16x16x32_bf16 v[112:115], a[216:219], a[204:207], v[112:115]\n"
    "buffer_load_dwordx4 v207, s[48:51], s59 offen lds\n"
    "v_mfma_f32_16x16x32_bf16 v[144:147], a[216:219], a[208:211], v[144:147]\n"
    "v_mfma_f32_16x16x32_bf16 v[176:179], a[216:219], a[212:215], v[176:179]\n"
    "s_waitcnt lgkmcnt(2)\n"
    "v_mfma_f32_16x16x32_bf16 v[20:23], a[220:223], a[192:195], v[20:23]\n"
    "v_mfma_f32_16x16x32_bf16 v[52:55], a[220:223], a[196:199], v[52:55]\n"
    "v_mfma_f32_16x16x32_bf16 v[84:87], a[220:223], a[200:203], v[84:87]\n"
    "s_waitcnt vmcnt(24) lgkmcnt(0)\n"
    "s_barrier\n"
    "v_mfma_f32_16x16x32_bf16 v[116:119], a[220:223], a[204:207], v[116:119]\n"
    "ds_read_b64_tr_b16 a[216:217], v195 offset:16384\n"
    "v_mfma_f32_16x16x32_bf16 v[148:151], a[220:223], a[208:211], v[148:151]\n"
    "ds_read_b64_tr_b16 a[218:219], v195 offset:16640\n"
    "v_mfma_f32_16x16x32_bf16 v[180:183], a[220:223], a[212:215], v[180:183]\n"
    "ds_read_b128 a[224:227], v199 offset:25088\n"
    "v_mfma_f32_16x16x32_bf16 v[24:27], a[248:251], a[192:195], v[24:27]\n"
    "ds_read_b128 a[228:231], v199 offset:27136\n"
    "v_mfma_f32_16x16x32_bf16 v[56:59], a[248:251], a[196:199], v[56:59]\n"
    "ds_read_b128 a[232:235], v199 offset:29184\n"
    "v_mfma_f32_16x16x32_bf16 v[88:91], a[248:251], a[200:203], v[88:91]\n"
    "ds_read_b128 a[236:239], v199 offset:31232\n"
    "v_mfma_f32_16x16x32_bf16 v[120:123], a[248:251], a[204:207], v[120:123]\n"
    "ds_read_b128 a[240:243], v199 offset:33280\n"
    "v_mfma_f32_16x16x32_bf16 v[152:155], a[248:251], a[208:211], v[152:155]\n"
    "ds_read_b128 a[244:247], v199 offset:35328\n"
    "s_add_u32 m0, s65, 32768\n"
    "v_mfma_f32_16x16x32_bf16 v[184:187], a[248:251], a[212:215], v[184:187]\n"
    "buffer_load_dwordx4 v204, s[44:47], s58 offen lds\n"
    "v_mfma_f32_16x16x32_bf16 v[28:31], a[252:255], a[192:195], v[28:31]\n"
    "ds_read_b64_tr_b16 a[220:221], v196 offset:16384\n"
    "s_add_u32 s58, s58, 0x10000\n"
    "v_mfma_f32_16x16x32_bf16 v[60:63], a[252:255], a[196:199], v[60:63]\n"
    "ds_read_b64_tr_b16 a[222:223], v196 offset:16640\n"
    "s_add_u32 m0, s65, 34816\n"
    "v_mfma_f32_16x16x32_bf16 v[92:95], a[252:255], a[200:203], v[92:95]\n"
    "buffer_load_dwordx4 v204, s[44:47], s58 offen lds\n"
    "v_mfma_f32_16x16x32_bf16 v[124:127], a[252:255], a[204:207], v[124:127]\n"
    "ds_read_b64_tr_b16 a[248:249], v195 offset:16896\n"
    "s_add_u32 s58, s58, 0x10000\n"
    "v_mfma_f32_16x16x32_bf16 v[156:159], a[252:255], a[208:211], v[156:159]\n"
    "ds_read_b64_tr_b16 a[250:251], v195 offset:17152\n"
    "s_add_u32 m0, s65, 36864\n"
    "v_mfma_f32_16x16x32_bf16 v[188:191], a[252:255], a[212:215], v[188:191]\n"
    "buffer_load_dwordx4 v205, s[44:47], s58 offen lds\n"
    "s_waitcnt lgkmcnt(4)\n"
    "v_mfma_f32_16x16x32_bf16 v[0:3], a[216:219], a[224:227], v[0:3]\n"
    "ds_read_b64_tr_b16 a[252:253], v196 offset:16896\n"
    "s_add_u32 s58, s58, 0x10000\n"
    "v_mfma_f32_16x16x32_bf16 v[32:35], a[216:219], a[228:231], v[32:35]\n"
    "v_mfma_f32_16x16x32_bf16 v[64:67], a[216:219], a[232:235], v[64:67]\n"
    "ds_read_b64_tr_b16 a[254:255], v196 offset:17152\n"
    "s_add_u32 m0, s65, 38912\n"
    "v_mfma_f32_16x16x32_bf16 v[96:99], a[216:219], a[236:239], v[96:99]\n"
    "buffer_load_dwordx4 v205, s[44:47], s58 offen lds\n"
    "v_mfma_f32_16x16x32_bf16 v[128:131], a[216:219], a[240:243], v[128:131]\n"
    "s_add_u32 s58, s58, 0x10000\n"
    "v_mfma_f32_16x16x32_bf16 v[160:163], a[216:219], a[244:247], v[160:163]\n"
    "s_waitcnt lgkmcnt(2)\n"
    "v_mfma_f32_16x16x32_bf16 v[4:7], a[220:223], a[224:227], v[4:7]\n"
    "ds_read_b64_tr_b16 a[216:217], v195 offset:17408\n"
    "v_mfma_f32_16x16x32_bf16 v[36:39], a[220:223], a[228:231], v[36:39]\n"
    "v_mfma_f32_16x16x32_bf16 v[68:71], a[220:223], a[232:235], v[68:71]\n"
    "ds_read_b64_tr_b16 a[218:219], v195 offset:17664\n"
    "v_mfma_f32_16x16x32_bf16 v[100:103], a[220:223], a[236:239], v[100:103]\n"
    "v_mfma_f32_16x16x32_bf16 v[132:135], a[220:223], a[240:243], v[132:135]\n"
    "v_mfma_f32_16x16x32_bf16 v[164:167], a[220:223], a[244:247], v[164:167]\n"
    "v_mfma_f32_16x16x32_bf16 v[8:11], a[248:251], a[224:227], v[8:11]\n"
    "ds_read_b64_tr_b16 a[220:221], v196 offset:17408\n"
    "v_mfma_f32_16x16x32_bf16 v[40:43], a[248:251], a[228:231], v[40:43]\n"
    "v_mfma_f32_16x16x32_bf16 v[72:75], a[248:251], a[232:235], v[72:75]\n"
    "ds_read_b64_tr_b16 a[222:223], v196 offset:17664\n"
    "v_mfma_f32_16x16x32_bf16 v[104:107], a[248:251], a[236:239], v[104:107]\n"
    "v_mfma_f32_16x16x32_bf16 v[136:139], a[248:251], a[240:243], v[136:139]\n"
    "v_mfma_f32_16x16x32_bf16 v[168:171], a[248:251], a[244:247], v[168:171]\n"
    "s_waitcnt lgkmcnt(2)\n"
    "v_mfma_f32_16x16x32_bf16 v[12:15], a[252:255], a[224:227], v[12:15]\n"
    "ds_read_b64_tr_b16 a[248:249], v195 offset:17920\n"
    "v_mfma_f32_16x16x32_bf16 v[44:47], a[252:255], a[228:231], v[44:47]\n"
    "v_mfma_f32_16x16x32_bf16 v[76:79], a[252:255], a[232:235], v[76:79]\n"
    "ds_read_b64_tr_b16 a[250:251], v195 offset:18176\n"
    "v_mfma_f32_16x16x32_bf16 v[108:111], a[252:255], a[236:239], v[108:111]\n"
    "v_mfma_f32_16x16x32_bf16 v[140:143], a[252:255], a[240:243], v[140:143]\n"
    "v_mfma_f32_16x16x32_bf16 v[172:175], a[252:255], a[244:247], v[172:175]\n"
    "v_mfma_f32_16x16x32_bf16 v[16:19], a[216:219], a[224:227], v[16:19]\n"
    "ds_read_b64_tr_b16 a[252:253], v196 offset:17920\n"
    "v_mfma_f32_16x16x32_bf16 v[48:51], a[216:219], a[228:231], v[48:51]\n"
    "v_mfma_f32_16x16x32_bf16 v[80:83], a[216:219], a[232:235], v[80:83]\n"
    "ds_read_b64_tr_b16 a[254:255], v196 offset:18176\n"
    "v_mfma_f32_16x16x32_bf16 v[112:115], a[216:219], a[236:239], v[112:115]\n"
    "v_mfma_f32_16x16x32_bf16 v[144:147], a[216:219], a[240:243], v[144:147]\n"
    "v_mfma_f32_16x16x32_bf16 v[176:179], a[216:219], a[244:247], v[176:179]\n"
    "s_waitcnt lgkmcnt(2)\n"
    "v_mfma_f32_16x16x32_bf16 v[20:23], a[220:223], a[224:227], v[20:23]\n"
    "v_mfma_f32_16x16x32_bf16 v[52:55], a[220:223], a[228:231], v[52:55]\n"
    "v_mfma_f32_16x16x32_bf16 v[84:87], a[220:223], a[232:235], v[84:87]\n"
    "s_waitcnt vmcnt(18) lgkmcnt(0)\n"
    "s_barrier\n"
    "v_mfma_f32_16x16x32_bf16 v[116:119], a[220:223], a[236:239], v[116:119]\n"
    "ds_read_b64_tr_b16 a[216:217], v197 offset:0\n"
    "v_mfma_f32_16x16x32_bf16 v[148:151], a[220:223], a[240:243], v[148:151]\n"
    "ds_read_b64_tr_b16 a[218:219], v197 offset:256\n"
    "v_mfma_f32_16x16x32_bf16 v[180:183], a[220:223], a[244:247], v[180:183]\n"
    "ds_read_b128 a[192:195], v200 offset:0\n"
    "v_mfma_f32_16x16x32_bf16 v[24:27], a[248:251], a[224:227], v[24:27]\n"
    "ds_read_b128 a[196:199], v200 offset:2048\n"
    "v_mfma_f32_16x16x32_bf16 v[56:59], a[248:251], a[228:231], v[56:59]\n"
    "ds_read_b128 a[200:203], v200 offset:4096\n"
    "v_mfma_f32_16x16x32_bf16 v[88:91], a[248:251], a[232:235], v[88:91]\n"
    "ds_read_b128 a[204:207], v200 offset:6144\n"
    "v_mfma_f32_16x16x32_bf16 v[120:123], a[248:251], a[236:239], v[120:123]\n"
    "ds_read_b128 a[208:211], v200 offset:8192\n"
    "v_mfma_f32_16x16x32_bf16 v[152:155], a[248:251], a[240:243], v[152:155]\n"
    "ds_read_b128 a[212:215], v200 offset:10240\n"
    "s_add_u32 m0, s65, 49152\n"
    "v_mfma_f32_16x16x32_bf16 v[184:187], a[248:251], a[244:247], v[184:187]\n"
    "buffer_load_dwordx4 v204, s[44:47], s58 offen lds\n"
    "v_mfma_f32_16x16x32_bf16 v[28:31], a[252:255], a[224:227], v[28:31]\n"
    "ds_read_b64_tr_b16 a[220:221], v198 offset:0\n"
    "s_add_u32 s58, s58, 0x10000\n"
    "v_mfma_f32_16x16x32_bf16 v[60:63], a[252:255], a[228:231], v[60:63]\n"
    "ds_read_b64_tr_b16 a[222:223], v198 offset:256\n"
    "s_add_u32 m0, s65, 51200\n"
    "v_mfma_f32_16x16x32_bf16 v[92:95], a[252:255], a[232:235], v[92:95]\n"
    "buffer_load_dwordx4 v204, s[44:47], s58 offen lds\n"
    "v_mfma_f32_16x16x32_bf16 v[124:127], a[252:255], a[236:239], v[124:127]\n"
    "ds_read_b64_tr_b16 a[248:249], v197 offset:512\n"
    "s_add_u32 s58, s58, 0x10000\n"
    "v_mfma_f32_16x16x32_bf16 v[156:159], a[252:255], a[240:243], v[156:159]\n"
    "ds_read_b64_tr_b16 a[250:251], v197 offset:768\n"
    "s_add_u32 m0, s65, 53248\n"
    "v_mfma_f32_16x16x32_bf16 v[188:191], a[252:255], a[244:247], v[188:191]\n"
    "buffer_load_dwordx4 v205, s[44:47], s58 offen lds\n"
    "s_waitcnt lgkmcnt(4)\n"
    "v_mfma_f32_16x16x32_bf16 v[0:3], a[216:219], a[192:195], v[0:3]\n"
    "ds_read_b64_tr_b16 a[252:253], v198 offset:512\n"
    "s_add_u32 s58, s58, 0x10000\n"
    "v_mfma_f32_16x16x32_bf16 v[32:35], a[216:219], a[196:199], v[32:35]\n"
    "v_mfma_f32_16x16x32_bf16 v[64:67], a[216:219], a[200:203], v[64:67]\n"
    "ds_read_b64_tr_b16 a[254:255], v198 offset:768\n"
    "s_add_u32 m0, s65, 55296\n"
    "v_mfma_f32_16x16x32_bf16 v[96:99], a[216:219], a[204:207], v[96:99]\n"
    "buffer_load_dwordx4 v205, s[44:47], s58 offen lds\n"
    "v_mfma_f32_16x16x32_bf16 v[128:131], a[216:219], a[208:211], v[128:131]\n"
    "s_add_u32 s58, s58, 0x10000\n"
    "v_mfma_f32_16x16x32_bf16 v[160:163], a[216:219], a[212:215], v[160:163]\n"
    "s_add_u32 m0, s69, 139264\n"
    "s_waitcnt lgkmcnt(2)\n"
    "v_mfma_f32_16x16x32_bf16 v[4:7], a[220:223], a[192:195], v[4:7]\n"
    "ds_read_b64_tr_b16 a[216:217], v197 offset:1024\n"
    "s_add_u32 s59, s61, 1024\n"
    "v_mfma_f32_16x16x32_bf16 v[36:39], a[220:223], a[196:199], v[36:39]\n"
    "buffer_load_dwordx4 v206, s[48:51], s59 offen lds\n"
    "v_mfma_f32_16x16x32_bf16 v[68:71], a[220:223], a[200:203], v[68:71]\n"
    "ds_read_b64_tr_b16 a[218:219], v197 offset:1280\n"
    "v_mfma_f32_16x16x32_bf16 v[100:103], a[220:223], a[204:207], v[100:103]\n"
    "s_add_u32 m0, s69, 140288\n"
    "v_mfma_f32_16x16x32_bf16 v[132:135], a[220:223], a[208:211], v[132:135]\n"
    "s_add_u32 s59, s61, 10240\n"
    "v_mfma_f32_16x16x32_bf16 v[164:167], a[220:223], a[212:215], v[164:167]\n"
    "buffer_load_dwordx4 v207, s[48:51], s59 offen lds\n"
    "v_mfma_f32_16x16x32_bf16 v[8:11], a[248:251], a[192:195], v[8:11]\n"
    "ds_read_b64_tr_b16 a[220:221], v198 offset:1024\n"
    "v_mfma_f32_16x16x32_bf16 v[40:43], a[248:251], a[196:199], v[40:43]\n"
    "s_add_u32 m0, s69, 141312\n"
    "v_mfma_f32_16x16x32_bf16 v[72:75], a[248:251], a[200:203], v[72:75]\n"
    "ds_read_b64_tr_b16 a[222:223], v198 offset:1280\n"
    "s_add_u32 s59, s61, 19456\n"
    "v_mfma_f32_16x16x32_bf16 v[104:107], a[248:251], a[204:207], v[104:107]\n"
    "buffer_load_dwordx4 v206, s[48:51], s59 offen lds\n"
    "v_mfma_f32_16x16x32_bf16 v[136:139], a[248:251], a[208:211], v[136:139]\n"
    "v_mfma_f32_16x16x32_bf16 v[168:171], a[248:251], a[212:215], v[168:171]\n"
    "s_add_u32 m0, s69, 142336\n"
    "s_waitcnt lgkmcnt(2)\n"
    "v_mfma_f32_16x16x32_bf16 v[12:15], a[252:255], a[192:195], v[12:15]\n"
    "ds_read_b64_tr_b16 a[248:249], v197 offset:1536\n"
    "s_add_u32 s59, s61, 28672\n"
    "v_mfma_f32_16x16x32_bf16 v[44:47], a[252:255], a[196:199], v[44:47]\n"
    "buffer_load_dwordx4 v207, s[48:51], s59 offen lds\n"
    "v_mfma_f32_16x16x32_bf16 v[76:79], a[252:255], a[200:203], v[76:79]\n"
    "ds_read_b64_tr_b16 a[250:251], v197 offset:1792\n"
    "v_mfma_f32_16x16x32_bf16 v[108:111], a[252:255], a[204:207], v[108:111]\n"
    "s_add_u32 m0, s69, 143360\n"
    "v_mfma_f32_16x16x32_bf16 v[140:143], a[252:255], a[208:211], v[140:143]\n"
    "s_add_u32 s59, s61, 37888\n"
    "v_mfma_f32_16x16x32_bf16 v[172:175], a[252:255], a[212:215], v[172:175]\n"
    "buffer_load_dwordx4 v206, s[48:51], s59 offen lds\n"
    "v_mfma_f32_16x16x32_bf16 v[16:19], a[216:219], a[192:195], v[16:19]\n"
    "ds_read_b64_tr_b16 a[252:253], v198 offset:1536\n"
    "v_mfma_f32_16x16x32_bf16 v[48:51], a[216:219], a[196:199], v[48:51]\n"
    "s_add_u32 m0, s69, 144384\n"
    "v_mfma_f32_16x16x32_bf16 v[80:83], a[216:219], a[200:203], v[80:83]\n"
    "ds_read_b64_tr_b16 a[254:255], v198 offset:1792\n"
    "s_add_u32 s59, s61, 47104\n"
    "v_mfma_f32_16x16x32_bf16 v[112:115], a[216:219], a[204:207], v[112:115]\n"
    "buffer_load_dwordx4 v207, s[48:51], s59 offen lds\n"
    "v_mfma_f32_16x16x32_bf16 v[144:147], a[216:219], a[208:211], v[144:147]\n"
    "v_mfma_f32_16x16x32_bf16 v[176:179], a[216:219], a[212:215], v[176:179]\n"
    "s_waitcnt lgkmcnt(2)\n"
    "v_mfma_f32_16x16x32_bf16 v[20:23], a[220:223], a[192:195], v[20:23]\n"
    "v_mfma_f32_16x16x32_bf16 v[52:55], a[220:223], a[196:199], v[52:55]\n"
    "v_mfma_f32_16x16x32_bf16 v[84:87], a[220:223], a[200:203], v[84:87]\n"
    "s_waitcnt vmcnt(24) lgkmcnt(0)\n"
    "s_barrier\n"
    "v_mfma_f32_16x16x32_bf16 v[116:119], a[220:223], a[204:207], v[116:119]\n"
    "ds_read_b64_tr_b16 a[216:217], v195 offset:32768\n"
    "v_mfma_f32_16x16x32_bf16 v[148:151], a[220:223], a[208:211], v[148:151]\n"
    "ds_read_b64_tr_b16 a[218:219], v195 offset:33024\n"
    "v_mfma_f32_16x16x32_bf16 v[180:183], a[220:223], a[212:215], v[180:183]\n"
    "ds_read_b128 a[224:227], v200 offset:512\n"
    "v_mfma_f32_16x16x32_bf16 v[24:27], a[248:251], a[192:195], v[24:27]\n"
    "ds_read_b128 a[228:231], v200 offset:2560\n"
    "v_mfma_f32_16x16x32_bf16 v[56:59], a[248:251], a[196:199], v[56:59]\n"
    "ds_read_b128 a[232:235], v200 offset:4608\n"
    "v_mfma_f32_16x16x32_bf16 v[88:91], a[248:251], a[200:203], v[88:91]\n"
    "ds_read_b128 a[236:239], v200 offset:6656\n"
    "v_mfma_f32_16x16x32_bf16 v[120:123], a[248:251], a[204:207], v[120:123]\n"
    "ds_read_b128 a[240:243], v200 offset:8704\n"
    "v_mfma_f32_16x16x32_bf16 v[152:155], a[248:251], a[208:211], v[152:155]\n"
    "ds_read_b128 a[244:247], v200 offset:10752\n"
    "s_add_u32 m0, s65, 98304\n"
    "v_mfma_f32_16x16x32_bf16 v[184:187], a[248:251], a[212:215], v[184:187]\n"
    "buffer_load_dwordx4 v204, s[44:47], s58 offen lds\n"
    "v_mfma_f32_16x16x32_bf16 v[28:31], a[252:255], a[192:195], v[28:31]\n"
    "ds_read_b64_tr_b16 a[220:221], v196 offset:32768\n"
    "s_add_u32 s58, s58, 0x10000\n"
    "v_mfma_f32_16x16x32_bf16 v[60:63], a[252:255], a[196:199], v[60:63]\n"
    "ds_read_b64_tr_b16 a[222:223], v196 offset:33024\n"
    "s_add_u32 m0, s65, 100352\n"
    "v_mfma_f32_16x16x32_bf16 v[92:95], a[252:255], a[200:203], v[92:95]\n"
    "buffer_load_dwordx4 v204, s[44:47], s58 offen lds\n"
    "v_mfma_f32_16x16x32_bf16 v[124:127], a[252:255], a[204:207], v[124:127]\n"
    "ds_read_b64_tr_b16 a[248:249], v195 offset:33280\n"
    "s_add_u32 s58, s58, 0x10000\n"
    "v_mfma_f32_16x16x32_bf16 v[156:159], a[252:255], a[208:211], v[156:159]\n"
    "ds_read_b64_tr_b16 a[250:251], v195 offset:33536\n"
    "s_add_u32 m0, s65, 102400\n"
    "v_mfma_f32_16x16x32_bf16 v[188:191], a[252:255], a[212:215], v[188:191]\n"
    "buffer_load_dwordx4 v205, s[44:47], s58 offen lds\n"
    "s_waitcnt lgkmcnt(4)\n"
    "v_mfma_f32_16x16x32_bf16 v[0:3], a[216:219], a[224:227], v[0:3]\n"
    "ds_read_b64_tr_b16 a[252:253], v196 offset:33280\n"
    "s_add_u32 s58, s58, 0x10000\n"
    "v_mfma_f32_16x16x32_bf16 v[32:35], a[216:219], a[228:231], v[32:35]\n"
    "v_mfma_f32_16x16x32_bf16 v[64:67], a[216:219], a[232:235], v[64:67]\n"
    "ds_read_b64_tr_b16 a[254:255], v196 offset:33536\n"
    "s_add_u32 m0, s65, 104448\n"
    "v_mfma_f32_16x16x32_bf16 v[96:99], a[216:219], a[236:239], v[96:99]\n"
    "buffer_load_dwordx4 v205, s[44:47], s58 offen lds\n"
    "v_mfma_f32_16x16x32_bf16 v[128:131], a[216:219], a[240:243], v[128:131]\n"
    "s_add_u32 s58, s58, 0x10000\n"
    "v_mfma_f32_16x16x32_bf16 v[160:163], a[216:219], a[244:247], v[160:163]\n"
    "s_waitcnt lgkmcnt(2)\n"
    "v_mfma_f32_16x16x32_bf16 v[4:7], a[220:223], a[224:227], v[4:7]\n"
    "ds_read_b64_tr_b16 a[216:217], v195 offset:33792\n"
    "v_mfma_f32_16x16x32_bf16 v[36:39], a[220:223], a[228:231], v[36:39]\n"
    "v_mfma_f32_16x16x32_bf16 v[68:71], a[220:223], a[232:235], v[68:71]\n"
    "ds_read_b64_tr_b16 a[218:219], v195 offset:34048\n"
    "v_mfma_f32_16x16x32_bf16 v[100:103], a[220:223], a[236:239], v[100:103]\n"
    "v_mfma_f32_16x16x32_bf16 v[132:135], a[220:223], a[240:243], v[132:135]\n"
    "v_mfma_f32_16x16x32_bf16 v[164:167], a[220:223], a[244:247], v[164:167]\n"
    "v_mfma_f32_16x16x32_bf16 v[8:11], a[248:251], a[224:227], v[8:11]\n"
    "ds_read_b64_tr_b16 a[220:221], v196 offset:33792\n"
    "v_mfma_f32_16x16x32_bf16 v[40:43], a[248:251], a[228:231], v[40:43]\n"
    "v_mfma_f32_16x16x32_bf16 v[72:75], a[248:251], a[232:235], v[72:75]\n"
    "ds_read_b64_tr_b16 a[222:223], v196 offset:34048\n"
    "v_mfma_f32_16x16x32_bf16 v[104:107], a[248:251], a[236:239], v[104:107]\n"
    "v_mfma_f32_16x16x32_bf16 v[136:139], a[248:251], a[240:243], v[136:139]\n"
    "v_mfma_f32_16x16x32_bf16 v[168:171], a[248:251], a[244:247], v[168:171]\n"
    "s_waitcnt lgkmcnt(2)\n"
    "v_mfma_f32_16x16x32_bf16 v[12:15], a[252:255], a[224:227], v[12:15]\n"
    "ds_read_b64_tr_b16 a[248:249], v195 offset:34304\n"
    "v_mfma_f32_16x16x32_bf16 v[44:47], a[252:255], a[228:231], v[44:47]\n"
    "v_mfma_f32_16x16x32_bf16 v[76:79], a[252:255], a[232:235], v[76:79]\n"
    "ds_read_b64_tr_b16 a[250:251], v195 offset:34560\n"
    "v_mfma_f32_16x16x32_bf16 v[108:111], a[252:255], a[236:239], v[108:111]\n"
    "v_mfma_f32_16x16x32_bf16 v[140:143], a[252:255], a[240:243], v[140:143]\n"
    "v_mfma_f32_16x16x32_bf16 v[172:175], a[252:255], a[244:247], v[172:175]\n"
    "v_mfma_f32_16x16x32_bf16 v[16:19], a[216:219], a[224:227], v[16:19]\n"
    "ds_read_b64_tr_b16 a[252:253], v196 offset:34304\n"
    "v_mfma_f32_16x16x32_bf16 v[48:51], a[216:219], a[228:231], v[48:51]\n"
    "v_mfma_f32_16x16x32_bf16 v[80:83], a[216:219], a[232:235], v[80:83]\n"
    "ds_read_b64_tr_b16 a[254:255], v196 offset:34560\n"
    "v_mfma_f32_16x16x32_bf16 v[112:115], a[216:219], a[236:239], v[112:115]\n"
    "v_mfma_f32_16x16x32_bf16 v[144:147], a[216:219], a[240:243], v[144:147]\n"
    "v_mfma_f32_16x16x32_bf16 v[176:179], a[216:219], a[244:247], v[176:179]\n"
    "s_waitcnt lgkmcnt(2)\n"
    "v_mfma_f32_16x16x32_bf16 v[20:23], a[220:223], a[224:227], v[20:23]\n"
    "v_mfma_f32_16x16x32_bf16 v[52:55], a[220:223], a[228:231], v[52:55]\n"
    "v_mfma_f32_16x16x32_bf16 v[84:87], a[220:223], a[232:235], v[84:87]\n"
    "s_waitcnt vmcnt(18) lgkmcnt(0)\n"
    "s_barrier\n"
    "v_mfma_f32_16x16x32_bf16 v[116:119], a[220:223], a[236:239], v[116:119]\n"
    "ds_read_b64_tr_b16 a[216:217], v195 offset:49152\n"
    "v_mfma_f32_16x16x32_bf16 v[148:151], a[220:223], a[240:243], v[148:151]\n"
    "ds_read_b64_tr_b16 a[218:219], v195 offset:49408\n"
    "v_mfma_f32_16x16x32_bf16 v[180:183], a[220:223], a[244:247], v[180:183]\n"
    "ds_read_b128 a[192:195], v199 offset:0\n"
    "v_mfma_f32_16x16x32_bf16 v[24:27], a[248:251], a[224:227], v[24:27]\n"
    "ds_read_b128 a[196:199], v199 offset:2048\n"
    "v_mfma_f32_16x16x32_bf16 v[56:59], a[248:251], a[228:231], v[56:59]\n"
    "ds_read_b128 a[200:203], v199 offset:4096\n"
    "v_mfma_f32_16x16x32_bf16 v[88:91], a[248:251], a[232:235], v[88:91]\n"
    "ds_read_b128 a[204:207], v199 offset:6144\n"
    "v_mfma_f32_16x16x32_bf16 v[120:123], a[248:251], a[236:239], v[120:123]\n"
    "ds_read_b128 a[208:211], v199 offset:8192\n"
    "v_mfma_f32_16x16x32_bf16 v[152:155], a[248:251], a[240:243], v[152:155]\n"
    "ds_read_b128 a[212:215], v199 offset:10240\n"
    "s_add_u32 m0, s65, 0\n"
    "v_mfma_f32_16x16x32_bf16 v[184:187], a[248:251], a[244:247], v[184:187]\n"
    "buffer_load_dwordx4 v201, s[36:39], s56 offen nt lds\n"
    "v_mfma_f32_16x16x32_bf16 v[28:31], a[252:255], a[224:227], v[28:31]\n"
    "ds_read_b64_tr_b16 a[220:221], v196 offset:49152\n"
    "v_mfma_f32_16x16x32_bf16 v[60:63], a[252:255], a[228:231], v[60:63]\n"
    "ds_read_b64_tr_b16 a[222:223], v196 offset:49408\n"
    "v_mfma_f32_16x16x32_bf16 v[92:95], a[252:255], a[232:235], v[92:95]\n"
    "buffer_load_dwordx4 v202, s[36:39], s56 offen offset:2048 nt lds\n"
    "v_mfma_f32_16x16x32_bf16 v[124:127], a[252:255], a[236:239], v[124:127]\n"
    "ds_read_b64_tr_b16 a[248:249], v195 offset:49664\n"
    "s_add_u32 s92, s92, 0x1000\n"
    "v_mfma_f32_16x16x32_bf16 v[156:159], a[252:255], a[240:243], v[156:159]\n"
    "ds_read_b64_tr_b16 a[250:251], v195 offset:49920\n"
    "s_xor_b32 s56, s92, %[rot]\n"
    "s_add_u32 m0, s65, 4096\n"
    "v_mfma_f32_16x16x32_bf16 v[188:191], a[252:255], a[244:247], v[188:191]\n"
    "buffer_load_dwordx4 v201, s[36:39], s56 offen nt lds\n"
    "s_waitcnt lgkmcnt(4)\n"
    "v_mfma_f32_16x16x32_bf16 v[0:3], a[216:219], a[192:195], v[0:3]\n"
    "ds_read_b64_tr_b16 a[252:253], v196 offset:49664\n"
    "v_mfma_f32_16x16x32_bf16 v[32:35], a[216:219], a[196:199], v[32:35]\n"
    "v_mfma_f32_16x16x32_bf16 v[64:67], a[216:219], a[200:203], v[64:67]\n"
    "ds_read_b64_tr_b16 a[254:255], v196 offset:49920\n"
    "v_mfma_f32_16x16x32_bf16 v[96:99], a[216:219], a[204:207], v[96:99]\n"
    "buffer_load_dwordx4 v202, s[36:39], s56 offen offset:2048 nt lds\n"
    "v_mfma_f32_16x16x32_bf16 v[128:131], a[216:219], a[208:211], v[128:131]\n"
    "s_add_u32 s92, s92, 0x1000\n"
    "v_mfma_f32_16x16x32_bf16 v[160:163], a[216:219], a[212:215], v[160:163]\n"
    "s_xor_b32 s56, s92, %[rot]\n"
    "s_waitcnt lgkmcnt(2)\n"
    "v_mfma_f32_16x16x32_bf16 v[4:7], a[220:223], a[192:195], v[4:7]\n"
    "ds_read_b64_tr_b16 a[216:217], v195 offset:50176\n"
    "s_add_u32 m0, s65, 16384\n"
    "v_mfma_f32_16x16x32_bf16 v[36:39], a[220:223], a[196:199], v[36:39]\n"
    "buffer_load_dwordx4 v201, s[36:39], s56 offen nt lds\n"
    "v_mfma_f32_16x16x32_bf16 v[68:71], a[220:223], a[200:203], v[68:71]\n"
    "ds_read_b64_tr_b16 a[218:219], v195 offset:50432\n"
    "v_mfma_f32_16x16x32_bf16 v[100:103], a[220:223], a[204:207], v[100:103]\n"
    "v_mfma_f32_16x16x32_bf16 v[132:135], a[220:223], a[208:211], v[132:135]\n"
    "v_mfma_f32_16x16x32_bf16 v[164:167], a[220:223], a[212:215], v[164:167]\n"
    "buffer_load_dwordx4 v202, s[36:39], s56 offen offset:2048 nt lds\n"
    "v_mfma_f32_16x16x32_bf16 v[8:11], a[248:251], a[192:195], v[8:11]\n"
    "ds_read_b64_tr_b16 a[220:221], v196 offset:50176\n"
    "s_add_u32 s92, s92, 0x1000\n"
    "v_mfma_f32_16x16x32_bf16 v[40:43], a[248:251], a[196:199], v[40:43]\n"
    "s_xor_b32 s56, s92, %[rot]\n"
    "v_mfma_f32_16x16x32_bf16 v[72:75], a[248:251], a[200:203], v[72:75]\n"
    "ds_read_b64_tr_b16 a[222:223], v196 offset:50432\n"
    "s_add_u32 m0, s65, 20480\n"
    "v_mfma_f32_16x16x32_bf16 v[104:107], a[248:251], a[204:207], v[104:107]\n"
    "buffer_load_dwordx4 v201, s[36:39], s56 offen nt lds\n"
    "v_mfma_f32_16x16x32_bf16 v[136:139], a[248:251], a[208:211], v[136:139]\n"
    "v_mfma_f32_16x16x32_bf16 v[168:171], a[248:251], a[212:215], v[168:171]\n"
    "s_waitcnt lgkmcnt(2)\n"
    "v_mfma_f32_16x16x32_bf16 v[12:15], a[252:255], a[192:195], v[12:15]\n"
    "ds_read_b64_tr_b16 a[248:249], v195 offset:50688\n"
    "v_mfma_f32_16x16x32_bf16 v[44:47], a[252:255], a[196:199], v[44:47]\n"
    "buffer_load_dwordx4 v202, s[36:39], s56 offen offset:2048 nt lds\n"
    "v_mfma_f32_16x16x32_bf16 v[76:79], a[252:255], a[200:203], v[76:79]\n"
    "ds_read_b64_tr_b16 a[250:251], v195 offset:50944\n"
    "s_add_u32 s92, s92, 0x1000\n"
    "v_mfma_f32_16x16x32_bf16 v[108:111], a[252:255], a[204:207], v[108:111]\n"
    "s_xor_b32 s56, s92, %[rot]\n"
    "v_mfma_f32_16x16x32_bf16 v[140:143], a[252:255], a[208:211], v[140:143]\n"
    "v_mfma_f32_16x16x32_bf16 v[172:175], a[252:255], a[212:215], v[172:175]\n"
    "v_mfma_f32_16x16x32_bf16 v[16:19], a[216:219], a[192:195], v[16:19]\n"
    "ds_read_b64_tr_b16 a[252:253], v196 offset:50688\n"
    "v_mfma_f32_16x16x32_bf16 v[48:51], a[216:219], a[196:199], v[48:51]\n"
    "v_mfma_f32_16x16x32_bf16 v[80:83], a[216:219], a[200:203], v[80:83]\n"
    "ds_read_b64_tr_b16 a[254:255], v196 offset:50944\n"
    "v_mfma_f32_16x16x32_bf16 v[112:115], a[216:219], a[204:207], v[112:115]\n"
    "v_mfma_f32_16x16x32_bf16 v[144:147], a[216:219], a[208:211], v[144:147]\n"
    "v_mfma_f32_16x16x32_bf16 v[176:179], a[216:219], a[212:215], v[176:179]\n"
    "s_waitcnt lgkmcnt(2)\n"
    "v_mfma_f32_16x16x32_bf16 v[20:23], a[220:223], a[192:195], v[20:23]\n"
    "v_mfma_f32_16x16x32_bf16 v[52:55], a[220:223], a[196:199], v[52:55]\n"
    "v_mfma_f32_16x16x32_bf16 v[84:87], a[220:223], a[200:203], v[84:87]\n"
    "s_waitcnt vmcnt(22) lgkmcnt(0)\n"
    "s_barrier\n"
    "v_mfma_f32_16x16x32_bf16 v[116:119], a[220:223], a[204:207], v[116:119]\n"
    "ds_read_b64_tr_b16 a[216:217], v195 offset:0\n"
    "v_mfma_f32_16x16x32_bf16 v[148:151], a[220:223], a[208:211], v[148:151]\n"
    "ds_read_b64_tr_b16 a[218:219], v195 offset:256\n"
    "v_mfma_f32_16x16x32_bf16 v[180:183], a[220:223], a[212:215], v[180:183]\n"
    "ds_read_b128 a[224:227], v199 offset:512\n"
    "v_mfma_f32_16x16x32_bf16 v[24:27], a[248:251], a[192:195], v[24:27]\n"
    "ds_read_b128 a[228:231], v199 offset:2560\n"
    "v_mfma_f32_16x16x32_bf16 v[56:59], a[248:251], a[196:199], v[56:59]\n"
    "ds_read_b128 a[232:235], v199 offset:4608\n"
    "v_mfma_f32_16x16x32_bf16 v[88:91], a[248:251], a[200:203], v[88:91]\n"
    "ds_read_b128 a[236:239], v199 offset:6656\n"
    "v_mfma_f32_16x16x32_bf16 v[120:123], a[248:251], a[204:207], v[120:123]\n"
    "ds_read_b128 a[240:243], v199 offset:8704\n"
    "v_mfma_f32_16x16x32_bf16 v[152:155], a[248:251], a[208:211], v[152:155]\n"
    "ds_read_b128 a[244:247], v199 offset:10752\n"
    "s_add_u32 m0, s65, 65536\n"
    "v_mfma_f32_16x16x32_bf16 v[184:187], a[248:251], a[212:215], v[184:187]\n"
    "buffer_load_dwordx4 v203, s[40:43], s57 offen nt lds\n"
    "v_mfma_f32_16x16x32_bf16 v[28:31], a[252:255], a[192:195], v[28:31]\n"
    "ds_read_b64_tr_b16 a[220:221], v196 offset:0\n"
    "v_mfma_f32_16x16x32_bf16 v[60:63], a[252:255], a[196:199], v[60:63]\n"
    "ds_read_b64_tr_b16 a[222:223], v196 offset:256\n"
    "v_mfma_f32_16x16x32_bf16 v[92:95], a[252:255], a[200:203], v[92:95]\n"
    "buffer_load_dwordx4 v203, s[40:43], s57 offen offset:2048 nt lds\n"
    "v_mfma_f32_16x16x32_bf16 v[124:127], a[252:255], a[204:207], v[124:127]\n"
    "ds_read_b64_tr_b16 a[248:249], v195 offset:512\n"
    "s_add_u32 s93, s93, 0x1000\n"
    "v_mfma_f32_16x16x32_bf16 v[156:159], a[252:255], a[208:211], v[156:159]\n"
    "ds_read_b64_tr_b16 a[250:251], v195 offset:768\n"
    "s_xor_b32 s57, s93, %[rot]\n"
    "s_add_u32 m0, s65, 69632\n"
    "v_mfma_f32_16x16x32_bf16 v[188:191], a[252:255], a[212:215], v[188:191]\n"
    "buffer_load_dwordx4 v203, s[40:43], s57 offen nt lds\n"
    "s_waitcnt lgkmcnt(4)\n"
    "v_mfma_f32_16x16x32_bf16 v[0:3], a[216:219], a[224:227], v[0:3]\n"
    "ds_read_b64_tr_b16 a[252:253], v196 offset:512\n"
    "v_mfma_f32_16x16x32_bf16 v[32:35], a[216:219], a[228:231], v[32:35]\n"
    "v_mfma_f32_16x16x32_bf16 v[64:67], a[216:219], a[232:235], v[64:67]\n"
    "ds_read_b64_tr_b16 a[254:255], v196 offset:768\n"
    "v_mfma_f32_16x16x32_bf16 v[96:99], a[216:219], a[236:239], v[96:99]\n"
    "buffer_load_dwordx4 v203, s[40:43], s57 offen offset:2048 nt lds\n"
    "v_mfma_f32_16x16x32_bf16 v[128:131], a[216:219], a[240:243], v[128:131]\n"
    "s_add_u32 s93, s93, 0x1000\n"
    "v_mfma_f32_16x16x32_bf16 v[160:163], a[216:219], a[244:247], v[160:163]\n"
    "s_xor_b32 s57, s93, %[rot]\n"
    "s_waitcnt lgkmcnt(2)\n"
    "v_mfma_f32_16x16x32_bf16 v[4:7], a[220:223], a[224:227], v[4:7]\n"
    "ds_read_b64_tr_b16 a[216:217], v195 offset:1024\n"
    "s_add_u32 m0, s65, 81920\n"
    "v_mfma_f32_16x16x32_bf16 v[36:39], a[220:223], a[228:231], v[36:39]\n"
    "buffer_load_dwordx4 v203, s[40:43], s57 offen nt lds\n"
    "v_mfma_f32_16x16x32_bf16 v[68:71], a[220:223], a[232:235], v[68:71]\n"
    "ds_read_b64_tr_b16 a[218:219], v195 offset:1280\n"
    "v_mfma_f32_16x16x32_bf16 v[100:103], a[220:223], a[236:239], v[100:103]\n"
    "v_mfma_f32_16x16x32_bf16 v[132:135], a[220:223], a[240:243], v[132:135]\n"
    "v_mfma_f32_16x16x32_bf16 v[164:167], a[220:223], a[244:247], v[164:167]\n"
    "buffer_load_dwordx4 v203, s[40:43], s57 offen offset:2048 nt lds\n"
    "v_mfma_f32_16x16x32_bf16 v[8:11], a[248:251], a[224:227], v[8:11]\n"
    "ds_read_b64_tr_b16 a[220:221], v196 offset:1024\n"
    "s_add_u32 s93, s93, 0x1000\n"
    "v_mfma_f32_16x16x32_bf16 v[40:43], a[248:251], a[228:231], v[40:43]\n"
    "s_xor_b32 s57, s93, %[rot]\n"
    "v_mfma_f32_16x16x32_bf16 v[72:75], a[248:251], a[232:235], v[72:75]\n"
    "ds_read_b64_tr_b16 a[222:223], v196 offset:1280\n"
    "s_add_u32 m0, s65, 86016\n"
    "v_mfma_f32_16x16x32_bf16 v[104:107], a[248:251], a[236:239], v[104:107]\n"
    "buffer_load_dwordx4 v203, s[40:43], s57 offen nt lds\n"
    "v_mfma_f32_16x16x32_bf16 v[136:139], a[248:251], a[240:243], v[136:139]\n"
    "v_mfma_f32_16x16x32_bf16 v[168:171], a[248:251], a[244:247], v[168:171]\n"
    "s_waitcnt lgkmcnt(2)\n"
    "v_mfma_f32_16x16x32_bf16 v[12:15], a[252:255], a[224:227], v[12:15]\n"
    "ds_read_b64_tr_b16 a[248:249], v195 offset:1536\n"
    "v_mfma_f32_16x16x32_bf16 v[44:47], a[252:255], a[228:231], v[44:47]\n"
    "buffer_load_dwordx4 v203, s[40:43], s57 offen offset:2048 nt lds\n"
    "v_mfma_f32_16x16x32_bf16 v[76:79], a[252:255], a[232:235], v[76:79]\n"
    "ds_read_b64_tr_b16 a[250:251], v195 offset:1792\n"
    "s_add_u32 s93, s93, 0x1000\n"
    "v_mfma_f32_16x16x32_bf16 v[108:111], a[252:255], a[236:239], v[108:111]\n"
    "s_xor_b32 s57, s93, %[rot]\n"
    "v_mfma_f32_16x16x32_bf16 v[140:143], a[252:255], a[240:243], v[140:143]\n"
    "v_mfma_f32_16x16x32_bf16 v[172:175], a[252:255], a[244:247], v[172:175]\n"
    "v_mfma_f32_16x16x32_bf16 v[16:19], a[216:219], a[224:227], v[16:19]\n"
    "ds_read_b64_tr_b16 a[252:253], v196 offset:1536\n"
    "v_mfma_f32_16x16x32_bf16 v[48:51], a[216:219], a[228:231], v[48:51]\n"
    "v_mfma_f32_16x16x32_bf16 v[80:83], a[216:219], a[232:235], v[80:83]\n"
    "ds_read_b64_tr_b16 a[254:255], v196 offset:1792\n"
    "v_mfma_f32_16x16x32_bf16 v[112:115], a[216:219], a[236:239], v[112:115]\n"
    "v_mfma_f32_16x16x32_bf16 v[144:147], a[216:219], a[240:243], v[144:147]\n"
    "v_mfma_f32_16x16x32_bf16 v[176:179], a[216:219], a[244:247], v[176:179]\n"
    "s_waitcnt lgkmcnt(2)\n"
    "v_mfma_f32_16x16x32_bf16 v[20:23], a[220:223], a[224:227], v[20:23]\n"
    "v_mfma_f32_16x16x32_bf16 v[52:55], a[220:223], a[228:231], v[52:55]\n"
    "v_mfma_f32_16x16x32_bf16 v[84:87], a[220:223], a[232:235], v[84:87]\n"
    "s_waitcnt vmcnt(20) lgkmcnt(0)\n"
    "s_barrier\n"
    "v_mfma_f32_16x16x32_bf16 v[116:119], a[220:223], a[236:239], v[116:119]\n"
    "ds_read_b64_tr_b16 a[216:217], v195 offset:16384\n"
    "v_mfma_f32_16x16x32_bf16 v[148:151], a[220:223], a[240:243], v[148:151]\n"
    "ds_read_b64_tr_b16 a[218:219], v195 offset:16640\n"
    "v_mfma_f32_16x16x32_bf16 v[180:183], a[220:223], a[244:247], v[180:183]\n"
    "ds_read_b128 a[192:195], v199 offset:24576\n"
    "v_mfma_f32_16x16x32_bf16 v[24:27], a[248:251], a[224:227], v[24:27]\n"
    "ds_read_b128 a[196:199], v199 offset:26624\n"
    "v_mfma_f32_16x16x32_bf16 v[56:59], a[248:251], a[228:231], v[56:59]\n"
    "ds_read_b128 a[200:203], v199 offset:28672\n"
    "v_mfma_f32_16x16x32_bf16 v[88:91], a[248:251], a[232:235], v[88:91]\n"
    "ds_read_b128 a[204:207], v199 offset:30720\n"
    "v_mfma_f32_16x16x32_bf16 v[120:123], a[248:251], a[236:239], v[120:123]\n"
    "ds_read_b128 a[208:211], v199 offset:32768\n"
    "v_mfma_f32_16x16x32_bf16 v[152:155], a[248:251], a[240:243], v[152:155]\n"
    "ds_read_b128 a[212:215], v199 offset:34816\n"
    "s_add_u32 m0, s65, 32768\n"
    "v_mfma_f32_16x16x32_bf16 v[184:187], a[248:251], a[244:247], v[184:187]\n"
    "buffer_load_dwordx4 v201, s[36:39], s56 offen nt lds\n"
    "v_mfma_f32_16x16x32_bf16 v[28:31], a[252:255], a[224:227], v[28:31]\n"
    "ds_read_b64_tr_b16 a[220:221], v196 offset:16384\n"
    "v_mfma_f32_16x16x32_bf16 v[60:63], a[252:255], a[228:231], v[60:63]\n"
    "ds_read_b64_tr_b16 a[222:223], v196 offset:16640\n"
    "v_mfma_f32_16x16x32_bf16 v[92:95], a[252:255], a[232:235], v[92:95]\n"
    "buffer_load_dwordx4 v202, s[36:39], s56 offen offset:2048 nt lds\n"
    "v_mfma_f32_16x16x32_bf16 v[124:127], a[252:255], a[236:239], v[124:127]\n"
    "ds_read_b64_tr_b16 a[248:249], v195 offset:16896\n"
    "s_add_u32 s92, s92, 0x1000\n"
    "v_mfma_f32_16x16x32_bf16 v[156:159], a[252:255], a[240:243], v[156:159]\n"
    "ds_read_b64_tr_b16 a[250:251], v195 offset:17152\n"
    "s_xor_b32 s56, s92, %[rot]\n"
    "s_add_u32 m0, s65, 36864\n"
    "v_mfma_f32_16x16x32_bf16 v[188:191], a[252:255], a[244:247], v[188:191]\n"
    "buffer_load_dwordx4 v201, s[36:39], s56 offen nt lds\n"
    "s_waitcnt lgkmcnt(4)\n"
    "v_mfma_f32_16x16x32_bf16 v[0:3], a[216:219], a[192:195], v[0:3]\n"
    "ds_read_b64_tr_b16 a[252:253], v196 offset:16896\n"
    "v_mfma_f32_16x16x32_bf16 v[32:35], a[216:219], a[196:199], v[32:35]\n"
    "v_mfma_f32_16x16x32_bf16 v[64:67], a[216:219], a[200:203], v[64:67]\n"
    "ds_read_b64_tr_b16 a[254:255], v196 offset:17152\n"
    "v_mfma_f32_16x16x32_bf16 v[96:99], a[216:219], a[204:207], v[96:99]\n"
    "buffer_load_dwordx4 v202, s[36:39], s56 offen offset:2048 nt lds\n"
    "v_mfma_f32_16x16x32_bf16 v[128:131], a[216:219], a[208:211], v[128:131]\n"
    "s_add_u32 s92, s92, 0x1000\n"
    "v_mfma_f32_16x16x32_bf16 v[160:163], a[216:219], a[212:215], v[160:163]\n"
    "s_xor_b32 s56, s92, %[rot]\n"
    "s_waitcnt lgkmcnt(2)\n"
    "v_mfma_f32_16x16x32_bf16 v[4:7], a[220:223], a[192:195], v[4:7]\n"
    "ds_read_b64_tr_b16 a[216:217], v195 offset:17408\n"
    "v_mfma_f32_16x16x32_bf16 v[36:39], a[220:223], a[196:199], v[36:39]\n"
    "v_mfma_f32_16x16x32_bf16 v[68:71], a[220:223], a[200:203], v[68:71]\n"
    "ds_read_b64_tr_b16 a[218:219], v195 offset:17664\n"
    "v_mfma_f32_16x16x32_bf16 v[100:103], a[220:223], a[204:207], v[100:103]\n"
    "v_mfma_f32_16x16x32_bf16 v[132:135], a[220:223], a[208:211], v[132:135]\n"
    "v_mfma_f32_16x16x32_bf16 v[164:167], a[220:223], a[212:215], v[164:167]\n"
    "v_mfma_f32_16x16x32_bf16 v[8:11], a[248:251], a[192:195], v[8:11]\n"
    "ds_read_b64_tr_b16 a[220:221], v196 offset:17408\n"
    "v_mfma_f32_16x16x32_bf16 v[40:43], a[248:251], a[196:199], v[40:43]\n"
    "v_mfma_f32_16x16x32_bf16 v[72:75], a[248:251], a[200:203], v[72:75]\n"
    "ds_read_b64_tr_b16 a[222:223], v196 offset:17664\n"
    "v_mfma_f32_16x16x32_bf16 v[104:107], a[248:251], a[204:207], v[104:107]\n"
    "v_mfma_f32_16x16x32_bf16 v[136:139], a[248:251], a[208:211], v[136:139]\n"
    "v_mfma_f32_16x16x32_bf16 v[168:171], a[248:251], a[212:215], v[168:171]\n"
    "s_waitcnt lgkmcnt(2)\n"
    "v_mfma_f32_16x16x32_bf16 v[12:15], a[252:255], a[192:195], v[12:15]\n"
    "ds_read_b64_tr_b16 a[248:249], v195 offset:17920\n"
    "v_mfma_f32_16x16x32_bf16 v[44:47], a[252:255], a[196:199], v[44:47]\n"
    "v_mfma_f32_16x16x32_bf16 v[76:79], a[252:255], a[200:203], v[76:79]\n"
    "ds_read_b64_tr_b16 a[250:251], v195 offset:18176\n"
    "v_mfma_f32_16x16x32_bf16 v[108:111], a[252:255], a[204:207], v[108:111]\n"
    "v_mfma_f32_16x16x32_bf16 v[140:143], a[252:255], a[208:211], v[140:143]\n"
    "v_mfma_f32_16x16x32_bf16 v[172:175], a[252:255], a[212:215], v[172:175]\n"
    "v_mfma_f32_16x16x32_bf16 v[16:19], a[216:219], a[192:195], v[16:19]\n"
    "ds_read_b64_tr_b16 a[252:253], v196 offset:17920\n"
    "v_mfma_f32_16x16x32_bf16 v[48:51], a[216:219], a[196:199], v[48:51]\n"
    "v_mfma_f32_16x16x32_bf16 v[80:83], a[216:219], a[200:203], v[80:83]\n"
    "ds_read_b64_tr_b16 a[254:255], v196 offset:18176\n"
    "v_mfma_f32_16x16x32_bf16 v[112:115], a[216:219], a[204:207], v[112:115]\n"
    "v_mfma_f32_16x16x32_bf16 v[144:147], a[216:219], a[208:211], v[144:147]\n"
    "v_mfma_f32_16x16x32_bf16 v[176:179], a[216:219], a[212:215], v[176:179]\n"
    "s_waitcnt lgkmcnt(2)\n"
    "v_mfma_f32_16x16x32_bf16 v[20:23], a[220:223], a[192:195], v[20:23]\n"
    "v_mfma_f32_16x16x32_bf16 v[52:55], a[220:223], a[196:199], v[52:55]\n"
    "v_mfma_f32_16x16x32_bf16 v[84:87], a[220:223], a[200:203], v[84:87]\n"
    "s_waitcnt vmcnt(20) lgkmcnt(0)\n"
    "s_barrier\n"
    "v_mfma_f32_16x16x32_bf16 v[116:119], a[220:223], a[204:207], v[116:119]\n"
    "ds_read_b64_tr_b16 a[216:217], v197 offset:0\n"
    "v_mfma_f32_16x16x32_bf16 v[148:151], a[220:223], a[208:211], v[148:151]\n"
    "ds_read_b64_tr_b16 a[218:219], v197 offset:256\n"
    "v_mfma_f32_16x16x32_bf16 v[180:183], a[220:223], a[212:215], v[180:183]\n"
    "ds_read_b128 a[224:227], v199 offset:25088\n"
    "v_mfma_f32_16x16x32_bf16 v[24:27], a[248:251], a[192:195], v[24:27]\n"
    "ds_read_b128 a[228:231], v199 offset:27136\n"
    "v_mfma_f32_16x16x32_bf16 v[56:59], a[248:251], a[196:199], v[56:59]\n"
    "ds_read_b128 a[232:235], v199 offset:29184\n"
    "v_mfma_f32_16x16x32_bf16 v[88:91], a[248:251], a[200:203], v[88:91]\n"
    "ds_read_b128 a[236:239], v199 offset:31232\n"
    "v_mfma_f32_16x16x32_bf16 v[120:123], a[248:251], a[204:207], v[120:123]\n"
    "ds_read_b128 a[240:243], v199 offset:33280\n"
    "v_mfma_f32_16x16x32_bf16 v[152:155], a[248:251], a[208:211], v[152:155]\n"
    "ds_read_b128 a[244:247], v199 offset:35328\n"
    "s_add_u32 m0, s65, 49152\n"
    "v_mfma_f32_16x16x32_bf16 v[184:187], a[248:251], a[212:215], v[184:187]\n"
    "buffer_load_dwordx4 v201, s[36:39], s56 offen nt lds\n"
    "v_mfma_f32_16x16x32_bf16 v[28:31], a[252:255], a[192:195], v[28:31]\n"
    "ds_read_b64_tr_b16 a[220:221], v198 offset:0\n"
    "v_mfma_f32_16x16x32_bf16 v[60:63], a[252:255], a[196:199], v[60:63]\n"
    "ds_read_b64_tr_b16 a[222:223], v198 offset:256\n"
    "v_mfma_f32_16x16x32_bf16 v[92:95], a[252:255], a[200:203], v[92:95]\n"
    "buffer_load_dwordx4 v202, s[36:39], s56 offen offset:2048 nt lds\n"
    "v_mfma_f32_16x16x32_bf16 v[124:127], a[252:255], a[204:207], v[124:127]\n"
    "ds_read_b64_tr_b16 a[248:249], v197 offset:512\n"
    "s_add_u32 s92, s92, 0x1000\n"
    "v_mfma_f32_16x16x32_bf16 v[156:159], a[252:255], a[208:211], v[156:159]\n"
    "ds_read_b64_tr_b16 a[250:251], v197 offset:768\n"
    "s_xor_b32 s56, s92, %[rot]\n"
    "s_add_u32 m0, s65, 53248\n"
    "v_mfma_f32_16x16x32_bf16 v[188:191], a[252:255], a[212:215], v[188:191]\n"
    "buffer_load_dwordx4 v201, s[36:39], s56 offen nt lds\n"
    "s_waitcnt lgkmcnt(4)\n"
    "v_mfma_f32_16x16x32_bf16 v[0:3], a[216:219], a[224:227], v[0:3]\n"
    "ds_read_b64_tr_b16 a[252:253], v198 offset:512\n"
    "v_mfma_f32_16x16x32_bf16 v[32:35], a[216:219], a[228:231], v[32:35]\n"
    "v_mfma_f32_16x16x32_bf16 v[64:67], a[216:219], a[232:235], v[64:67]\n"
    "ds_read_b64_tr_b16 a[254:255], v198 offset:768\n"
    "v_mfma_f32_16x16x32_bf16 v[96:99], a[216:219], a[236:239], v[96:99]\n"
    "buffer_load_dwordx4 v202, s[36:39], s56 offen offset:2048 nt lds\n"
    "v_mfma_f32_16x16x32_bf16 v[128:131], a[216:219], a[240:243], v[128:131]\n"
    "s_add_u32 s92, s92, 0x1000\n"
    "v_mfma_f32_16x16x32_bf16 v[160:163], a[216:219], a[244:247], v[160:163]\n"
    "s_xor_b32 s56, s92, %[rot]\n"
    "s_waitcnt lgkmcnt(2)\n"
    "v_mfma_f32_16x16x32_bf16 v[4:7], a[220:223], a[224:227], v[4:7]\n"
    "ds_read_b64_tr_b16 a[216:217], v197 offset:1024\n"
    "v_mfma_f32_16x16x32_bf16 v[36:39], a[220:223], a[228:231], v[36:39]\n"
    "v_mfma_f32_16x16x32_bf16 v[68:71], a[220:223], a[232:235], v[68:71]\n"
    "ds_read_b64_tr_b16 a[218:219], v197 offset:1280\n"
    "v_mfma_f32_16x16x32_bf16 v[100:103], a[220:223], a[236:239], v[100:103]\n"
    "v_mfma_f32_16x16x32_bf16 v[132:135], a[220:223], a[240:243], v[132:135]\n"
    "v_mfma_f32_16x16x32_bf16 v[164:167], a[220:223], a[244:247], v[164:167]\n"
    "v_mfma_f32_16x16x32_bf16 v[8:11], a[248:251], a[224:227], v[8:11]\n"
    "ds_read_b64_tr_b16 a[220:221], v198 offset:1024\n"
    "v_mfma_f32_16x16x32_bf16 v[40:43], a[248:251], a[228:231], v[40:43]\n"
    "v_mfma_f32_16x16x32_bf16 v[72:75], a[248:251], a[232:235], v[72:75]\n"
    "ds_read_b64_tr_b16 a[222:223], v198 offset:1280\n"
    "v_mfma_f32_16x16x32_bf16 v[104:107], a[248:251], a[236:239], v[104:107]\n"
    "v_mfma_f32_16x16x32_bf16 v[136:139], a[248:251], a[240:243], v[136:139]\n"
    "v_mul_f32 v0, s76, v0\n"
    "v_mul_f32 v1, s76, v1\n"
    "v_mul_f32 v2, s76, v2\n"
    "v_mfma_f32_16x16x32_bf16 v[168:171], a[248:251], a[244:247], v[168:171]\n"
    "v_mul_f32 v3, s76, v3\n"
    "v_mul_f32 v4, s76, v4\n"
    "s_waitcnt lgkmcnt(2)\n"
    "v_mfma_f32_16x16x32_bf16 v[12:15], a[252:255], a[224:227], v[12:15]\n"
    "ds_read_b64_tr_b16 a[248:249], v197 offset:1536\n"
    "v_mul_f32 v5, s76, v5\n"
    "v_mul_f32 v6, s76, v6\n"
    "v_mfma_f32_16x16x32_bf16 v[44:47], a[252:255], a[228:231], v[44:47]\n"
    "v_mul_f32 v7, s76, v7\n"
    "v_cvt_pk_bf16_f32 v0, v0, v1\n"
    "v_cvt_pk_bf16_f32 v1, v2, v3\n"
    "v_mfma_f32_16x16x32_bf16 v[76:79], a[252:255], a[232:235], v[76:79]\n"
    "ds_read_b64_tr_b16 a[250:251], v197 offset:1792\n"
    "v_cvt_pk_bf16_f32 v2, v4, v5\n"
    "v_cvt_pk_bf16_f32 v3, v6, v7\n"
    "v_mfma_f32_16x16x32_bf16 v[108:111], a[252:255], a[236:239], v[108:111]\n"
    "v_mul_f32 v32, s76, v32\n"
    "v_mul_f32 v33, s76, v33\n"
    "v_mfma_f32_16x16x32_bf16 v[140:143], a[252:255], a[240:243], v[140:143]\n"
    "v_permlane16_swap_b32 v0, v2\n"
    "v_mfma_f32_16x16x32_bf16 v[172:175], a[252:255], a[244:247], v[172:175]\n"
    "v_permlane16_swap_b32 v1, v3\n"
    "v_mfma_f32_16x16x32_bf16 v[16:19], a[216:219], a[224:227], v[16:19]\n"
    "ds_read_b64_tr_b16 a[252:253], v198 offset:1536\n"
    "v_mul_f32 v34, s76, v34\n"
    "v_mul_f32 v35, s76, v35\n"
    "v_mfma_f32_16x16x32_bf16 v[48:51], a[216:219], a[228:231], v[48:51]\n"
    "v_mul_f32 v36, s76, v36\n"
    "v_mul_f32 v37, s76, v37\n"
    "v_mfma_f32_16x16x32_bf16 v[80:83], a[216:219], a[232:235], v[80:83]\n"
    "ds_read_b64_tr_b16 a[254:255], v198 offset:1792\n"
    "v_mul_f32 v38, s76, v38\n"
    "v_mul_f32 v39, s76, v39\n"
    "v_mfma_f32_16x16x32_bf16 v[112:115], a[216:219], a[236:239], v[112:115]\n"
    "v_cvt_pk_bf16_f32 v32, v32, v33\n"
    "v_cvt_pk_bf16_f32 v33, v34, v35\n"
    "v_mfma_f32_16x16x32_bf16 v[144:147], a[216:219], a[240:243], v[144:147]\n"
    "v_cvt_pk_bf16_f32 v34, v36, v37\n"
    "v_cvt_pk_bf16_f32 v35, v38, v39\n"
    "v_mfma_f32_16x16x32_bf16 v[176:179], a[216:219], a[244:247], v[176:179]\n"
    "s_nop 1\n"
    "s_waitcnt lgkmcnt(2)\n"
    "v_mfma_f32_16x16x32_bf16 v[20:23], a[220:223], a[224:227], v[20:23]\n"
    "v_permlane16_swap_b32 v32, v34\n"
    "v_mfma_f32_16x16x32_bf16 v[52:55], a[220:223], a[228:231], v[52:55]\n"
    "v_permlane16_swap_b32 v33, v35\n"
    "v_mfma_f32_16x16x32_bf16 v[84:87], a[220:223], a[232:235], v[84:87]\n"
    "v_mul_f32 v8, s76, v8\n"
    "v_mul_f32 v9, s76, v9\n"
    "v_mul_f32 v10, s76, v10\n"
    "v_mfma_f32_16x16x32_bf16 v[116:119], a[220:223], a[236:239], v[116:119]\n"
    "v_mul_f32 v11, s76, v11\n"
    "v_mul_f32 v12, s76, v12\n"
    "v_mul_f32 v13, s76, v13\n"
    "v_mfma_f32_16x16x32_bf16 v[148:151], a[220:223], a[240:243], v[148:151]\n"
    "v_mul_f32 v14, s76, v14\n"
    "v_mul_f32 v15, s76, v15\n"
    "v_mfma_f32_16x16x32_bf16 v[180:183], a[220:223], a[244:247], v[180:183]\n"
    "v_cvt_pk_bf16_f32 v4, v8, v9\n"
    "v_cvt_pk_bf16_f32 v5, v10, v11\n"
    "v_mfma_f32_16x16x32_bf16 v[24:27], a[248:251], a[224:227], v[24:27]\n"
    "v_cvt_pk_bf16_f32 v6, v12, v13\n"
    "v_cvt_pk_bf16_f32 v7, v14, v15\n"
    "v_mfma_f32_16x16x32_bf16 v[56:59], a[248:251], a[228:231], v[56:59]\n"
    "v_mul_f32 v40, s76, v40\n"
    "v_mul_f32 v41, s76, v41\n"
    "v_mfma_f32_16x16x32_bf16 v[88:91], a[248:251], a[232:235], v[88:91]\n"
    "v_permlane16_swap_b32 v4, v6\n"
    "v_mfma_f32_16x16x32_bf16 v[120:123], a[248:251], a[236:239], v[120:123]\n"
    "v_permlane16_swap_b32 v5, v7\n"
    "v_mfma_f32_16x16x32_bf16 v[152:155], a[248:251], a[240:243], v[152:155]\n"
    "v_mul_f32 v42, s76, v42\n"
    "v_mul_f32 v43, s76, v43\n"
    "v_mfma_f32_16x16x32_bf16 v[184:187], a[248:251], a[244:247], v[184:187]\n"
    "v_mul_f32 v44, s76, v44\n"
    "v_mul_f32 v45, s76, v45\n"
    "s_waitcnt lgkmcnt(0)\n"
    "v_mfma_f32_16x16x32_bf16 v[28:31], a[252:255], a[224:227], v[28:31]\n"
    "v_mul_f32 v46, s76, v46\n"
    "v_mul_f32 v47, s76, v47\n"
    "v_mfma_f32_16x16x32_bf16 v[60:63], a[252:255], a[228:231], v[60:63]\n"
    "v_cvt_pk_bf16_f32 v36, v40, v41\n"
    "v_cvt_pk_bf16_f32 v37, v42, v43\n"
    "v_mfma_f32_16x16x32_bf16 v[92:95], a[252:255], a[232:235], v[92:95]\n"
    "v_cvt_pk_bf16_f32 v38, v44, v45\n"
    "v_cvt_pk_bf16_f32 v39, v46, v47\n"
    "v_mfma_f32_16x16x32_bf16 v[124:127], a[252:255], a[236:239], v[124:127]\n"
    "s_nop 1\n"
    "v_mfma_f32_16x16x32_bf16 v[156:159], a[252:255], a[240:243], v[156:159]\n"
    "v_permlane16_swap_b32 v36, v38\n"
    "v_mfma_f32_16x16x32_bf16 v[188:191], a[252:255], a[244:247], v[188:191]\n"
    "v_permlane16_swap_b32 v37, v39\n"
    "v_pk_mul_f32 v[16:17], v[16:17], v[240:241] op_sel_hi:[1,0]\n"
    "v_pk_mul_f32 v[18:19], v[18:19], v[240:241] op_sel_hi:[1,0]\n"
    "v_pk_mul_f32 v[20:21], v[20:21], v[240:241] op_sel_hi:[1,0]\n"
    "v_pk_mul_f32 v[22:23], v[22:23], v[240:241] op_sel_hi:[1,0]\n"
    "v_cvt_pk_bf16_f32 v8, v16, v17\n"
    "v_cvt_pk_bf16_f32 v9, v18, v19\n"
    "v_cvt_pk_bf16_f32 v10, v20, v21\n"
    "v_cvt_pk_bf16_f32 v11, v22, v23\n"
    "v_pk_mul_f32 v[48:49], v[48:49], v[240:241] op_sel_hi:[1,0]\n"
    "v_pk_mul_f32 v[50:51], v[50:51], v[240:241] op_sel_hi:[1,0]\n"
    "v_permlane16_swap_b32 v8, v10\n"
    "v_permlane16_swap_b32 v9, v11\n"
    "v_pk_mul_f32 v[52:53], v[52:53], v[240:241] op_sel_hi:[1,0]\n"
    "v_pk_mul_f32 v[54:55], v[54:55], v[240:241] op_sel_hi:[1,0]\n"
    "v_cvt_pk_bf16_f32 v40, v48, v49\n"
    "v_cvt_pk_bf16_f32 v41, v50, v51\n"
    "v_cvt_pk_bf16_f32 v42, v52, v53\n"
    "v_cvt_pk_bf16_f32 v43, v54, v55\n"
    "v_pk_mul_f32 v[64:65], v[64:65], v[240:241] op_sel_hi:[1,0]\n"
    "v_pk_mul_f32 v[66:67], v[66:67], v[240:241] op_sel_hi:[1,0]\n"
    "v_permlane16_swap_b32 v40, v42\n"
    "v_permlane16_swap_b32 v41, v43\n"
    "v_pk_mul_f32 v[68:69], v[68:69], v[240:241] op_sel_hi:[1,0]\n"
    "v_pk_mul_f32 v[70:71], v[70:71], v[240:241] op_sel_hi:[1,0]\n"
    "v_cvt_pk_bf16_f32 v64, v64, v65\n"
    "v_cvt_pk_bf16_f32 v65, v66, v67\n"
    "v_cvt_pk_bf16_f32 v66, v68, v69\n"
    "v_cvt_pk_bf16_f32 v67, v70, v71\n"
    "v_pk_mul_f32 v[96:97], v[96:97], v[240:241] op_sel_hi:[1,0]\n"
    "v_pk_mul_f32 v[98:99], v[98:99], v[240:241] op_sel_hi:[1,0]\n"
    "v_permlane16_swap_b32 v64, v66\n"
    "v_permlane16_swap_b32 v65, v67\n"
    "v_pk_mul_f32 v[100:101], v[100:101], v[240:241] op_sel_hi:[1,0]\n"
    "v_pk_mul_f32 v[102:103], v[102:103], v[240:241] op_sel_hi:[1,0]\n"
    "v_cvt_pk_bf16_f32 v96, v96, v97\n"
    "v_cvt_pk_bf16_f32 v97, v98, v99\n"
    "v_cvt_pk_bf16_f32 v98, v100, v101\n"
    "v_cvt_pk_bf16_f32 v99, v102, v103\n"
    "v_pk_mul_f32 v[72:73], v[72:73], v[240:241] op_sel_hi:[1,0]\n"
    "v_pk_mul_f32 v[74:75], v[74:75], v[240:241] op_sel_hi:[1,0]\n"
    "v_permlane16_swap_b32 v96, v98\n"
    "v_permlane16_swap_b32 v97, v99\n"
    "v_pk_mul_f32 v[76:77], v[76:77], v[240:241] op_sel_hi:[1,0]\n"
    "v_pk_mul_f32 v[78:79], v[78:79], v[240:241] op_sel_hi:[1,0]\n"
    "v_cvt_pk_bf16_f32 v68, v72, v73\n"
    "v_cvt_pk_bf16_f32 v69, v74, v75\n"
    "v_cvt_pk_bf16_f32 v70, v76, v77\n"
    "v_cvt_pk_bf16_f32 v71, v78, v79\n"
    "v_pk_mul_f32 v[104:105], v[104:105], v[240:241] op_sel_hi:[1,0]\n"
    "v_pk_mul_f32 v[106:107], v[106:107], v[240:241] op_sel_hi:[1,0]\n"
    "v_permlane16_swap_b32 v68, v70\n"
    "v_permlane16_swap_b32 v69, v71\n"
    "v_pk_mul_f32 v[108:109], v[108:109], v[240:241] op_sel_hi:[1,0]\n"
    "v_pk_mul_f32 v[110:111], v[110:111], v[240:241] op_sel_hi:[1,0]\n"
    "v_cvt_pk_bf16_f32 v100, v104, v105\n"
    "v_cvt_pk_bf16_f32 v101, v106, v107\n"
    "v_cvt_pk_bf16_f32 v102, v108, v109\n"
    "v_cvt_pk_bf16_f32 v103, v110, v111\n"
    "v_pk_mul_f32 v[80:81], v[80:81], v[240:241] op_sel_hi:[1,0]\n"
    "v_pk_mul_f32 v[82:83], v[82:83], v[240:241] op_sel_hi:[1,0]\n"
    "v_permlane16_swap_b32 v100, v102\n"
    "v_permlane16_swap_b32 v101, v103\n"
    "v_pk_mul_f32 v[84:85], v[84:85], v[240:241] op_sel_hi:[1,0]\n"
    "v_pk_mul_f32 v[86:87], v[86:87], v[240:241] op_sel_hi:[1,0]\n"
    "v_cvt_pk_bf16_f32 v72, v80, v81\n"
    "v_cvt_pk_bf16_f32 v73, v82, v83\n"
    "v_cvt_pk_bf16_f32 v74, v84, v85\n"
    "v_cvt_pk_bf16_f32 v75, v86, v87\n"
    "v_pk_mul_f32 v[112:113], v[112:113], v[240:241] op_sel_hi:[1,0]\n"
    "v_pk_mul_f32 v[114:115], v[114:115], v[240:241] op_sel_hi:[1,0]\n"
    "v_permlane16_swap_b32 v72, v74\n"
    "v_permlane16_swap_b32 v73, v75\n"
    "v_pk_mul_f32 v[116:117], v[116:117], v[240:241] op_sel_hi:[1,0]\n"
    "v_pk_mul_f32 v[118:119], v[118:119], v[240:241] op_sel_hi:[1,0]\n"
    "v_cvt_pk_bf16_f32 v104, v112, v113\n"
    "v_cvt_pk_bf16_f32 v105, v114, v115\n"
    "v_cvt_pk_bf16_f32 v106, v116, v117\n"
    "v_cvt_pk_bf16_f32 v107, v118, v119\n"
    "v_pk_mul_f32 v[128:129], v[128:129], v[240:241] op_sel_hi:[1,0]\n"
    "v_pk_mul_f32 v[130:131], v[130:131], v[240:241] op_sel_hi:[1,0]\n"
    "v_permlane16_swap_b32 v104, v106\n"
    "v_permlane16_swap_b32 v105, v107\n"
    "v_pk_mul_f32 v[132:133], v[132:133], v[240:241] op_sel_hi:[1,0]\n"
    "v_pk_mul_f32 v[134:135], v[134:135], v[240:241] op_sel_hi:[1,0]\n"
    "v_cvt_pk_bf16_f32 v128, v128, v129\n"
    "v_cvt_pk_bf16_f32 v129, v130, v131\n"
    "v_cvt_pk_bf16_f32 v130, v132, v133\n"
    "v_cvt_pk_bf16_f32 v131, v134, v135\n"
    "v_pk_mul_f32 v[160:161], v[160:161], v[240:241] op_sel_hi:[1,0]\n"
    "v_pk_mul_f32 v[162:163], v[162:163], v[240:241] op_sel_hi:[1,0]\n"
    "v_permlane16_swap_b32 v128, v130\n"
    "v_permlane16_swap_b32 v129, v131\n"
    "v_pk_mul_f32 v[164:165], v[164:165], v[240:241] op_sel_hi:[1,0]\n"
    "v_pk_mul_f32 v[166:167], v[166:167], v[240:241] op_sel_hi:[1,0]\n"
    "v_cvt_pk_bf16_f32 v160, v160, v161\n"
    "v_cvt_pk_bf16_f32 v161, v162, v163\n"
    "v_cvt_pk_bf16_f32 v162, v164, v165\n"
    "v_cvt_pk_bf16_f32 v163, v166, v167\n"
    "v_pk_mul_f32 v[136:137], v[136:137], v[240:241] op_sel_hi:[1,0]\n"
    "v_pk_mul_f32 v[138:139], v[138:139], v[240:241] op_sel_hi:[1,0]\n"
    "v_permlane16_swap_b32 v160, v162\n"
    "v_permlane16_swap_b32 v161, v163\n"
    "v_pk_mul_f32 v[140:141], v[140:141], v[240:241] op_sel_hi:[1,0]\n"
    "v_pk_mul_f32 v[142:143], v[142:143], v[240:241] op_sel_hi:[1,0]\n"
    "v_cvt_pk_bf16_f32 v132, v136, v137\n"
    "v_cvt_pk_bf16_f32 v133, v138, v139\n"
    "v_cvt_pk_bf16_f32 v134, v140, v141\n"
    "v_cvt_pk_bf16_f32 v135, v142, v143\n"
    "v_pk_mul_f32 v[168:169], v[168:169], v[240:241] op_sel_hi:[1,0]\n"
    "v_pk_mul_f32 v[170:171], v[170:171], v[240:241] op_sel_hi:[1,0]\n"
    "v_permlane16_swap_b32 v132, v134\n"
    "v_permlane16_swap_b32 v133, v135\n"
    "v_pk_mul_f32 v[172:173], v[172:173], v[240:241] op_sel_hi:[1,0]\n"
    "v_pk_mul_f32 v[174:175], v[174:175], v[240:241] op_sel_hi:[1,0]\n"
    "v_cvt_pk_bf16_f32 v164, v168, v169\n"
    "v_cvt_pk_bf16_f32 v165, v170, v171\n"
    "v_cvt_pk_bf16_f32 v166, v172, v173\n"
    "v_cvt_pk_bf16_f32 v167, v174, v175\n"
    "v_pk_mul_f32 v[144:145], v[144:145], v[240:241] op_sel_hi:[1,0]\n"
    "v_pk_mul_f32 v[146:147], v[146:147], v[240:241] op_sel_hi:[1,0]\n"
    "s_waitcnt vmcnt(20) lgkmcnt(0)\n"
    "s_barrier\n"
    "ds_read_b128 a[192:195], v192 offset:0\n"
    "ds_read_b128 a[196:199], v192 offset:512\n"
    "ds_read_b128 a[200:203], v192 offset:1024\n"
    "ds_read_b128 a[204:207], v192 offset:1536\n"
    "ds_read_b128 a[208:211], v192 offset:4096\n"
    "ds_read_b128 a[212:215], v192 offset:4608\n"
    "ds_read_b128 a[216:219], v192 offset:5120\n"
    "ds_read_b128 a[220:223], v192 offset:5632\n"
    "v_lshl_add_u64 v[248:249], v[248:249], 0, 1\n"
    "v_permlane16_swap_b32 v164, v166\n"
    "v_permlane16_swap_b32 v165, v167\n"
    "v_pk_mul_f32 v[148:149], v[148:149], v[240:241] op_sel_hi:[1,0]\n"
    "v_pk_mul_f32 v[150:151], v[150:151], v[240:241] op_sel_hi:[1,0]\n"
    "v_cvt_pk_bf16_f32 v136, v144, v145\n"
    "v_cvt_pk_bf16_f32 v137, v146, v147\n"
    "v_cvt_pk_bf16_f32 v138, v148, v149\n"
    "v_cvt_pk_bf16_f32 v139, v150, v151\n"
    "v_pk_mul_f32 v[176:177], v[176:177], v[240:241] op_sel_hi:[1,0]\n"
    "v_pk_mul_f32 v[178:179], v[178:179], v[240:241] op_sel_hi:[1,0]\n"
    "v_permlane16_swap_b32 v136, v138\n"
    "v_permlane16_swap_b32 v137, v139\n"
    "v_pk_mul_f32 v[180:181], v[180:181], v[240:241] op_sel_hi:[1,0]\n"
    "v_pk_mul_f32 v[182:183], v[182:183], v[240:241] op_sel_hi:[1,0]\n"
    "v_cvt_pk_bf16_f32 v168, v176, v177\n"
    "v_cvt_pk_bf16_f32 v169, v178, v179\n"
    "v_cvt_pk_bf16_f32 v170, v180, v181\n"
    "v_cvt_pk_bf16_f32 v171, v182, v183\n"
    "v_pk_mul_f32 v[24:25], v[24:25], v[240:241] op_sel_hi:[1,0]\n"
    "v_pk_mul_f32 v[26:27], v[26:27], v[240:241] op_sel_hi:[1,0]\n"
    "v_permlane16_swap_b32 v168, v170\n"
    "v_permlane16_swap_b32 v169, v171\n"
    "v_pk_mul_f32 v[28:29], v[28:29], v[240:241] op_sel_hi:[1,0]\n"
    "v_pk_mul_f32 v[30:31], v[30:31], v[240:241] op_sel_hi:[1,0]\n"
    "v_cvt_pk_bf16_f32 v12, v24, v25\n"
    "v_cvt_pk_bf16_f32 v13, v26, v27\n"
    "v_cvt_pk_bf16_f32 v14, v28, v29\n"
    "v_cvt_pk_bf16_f32 v15, v30, v31\n"
    "v_pk_mul_f32 v[56:57], v[56:57], v[240:241] op_sel_hi:[1,0]\n"
    "v_pk_mul_f32 v[58:59], v[58:59], v[240:241] op_sel_hi:[1,0]\n"
    "v_permlane16_swap_b32 v12, v14\n"
    "v_permlane16_swap_b32 v13, v15\n"
    "v_pk_mul_f32 v[60:61], v[60:61], v[240:241] op_sel_hi:[1,0]\n"
    "v_pk_mul_f32 v[62:63], v[62:63], v[240:241] op_sel_hi:[1,0]\n"
    "v_cvt_pk_bf16_f32 v44, v56, v57\n"
    "v_cvt_pk_bf16_f32 v45, v58, v59\n"
    "v_cvt_pk_bf16_f32 v46, v60, v61\n"
    "v_cvt_pk_bf16_f32 v47, v62, v63\n"
    "v_pk_mul_f32 v[88:89], v[88:89], v[240:241] op_sel_hi:[1,0]\n"
    "v_pk_mul_f32 v[90:91], v[90:91], v[240:241] op_sel_hi:[1,0]\n"
    "v_permlane16_swap_b32 v44, v46\n"
    "v_permlane16_swap_b32 v45, v47\n"
    "v_pk_mul_f32 v[92:93], v[92:93], v[240:241] op_sel_hi:[1,0]\n"
    "v_pk_mul_f32 v[94:95], v[94:95], v[240:241] op_sel_hi:[1,0]\n"
    "v_cvt_pk_bf16_f32 v76, v88, v89\n"
    "v_cvt_pk_bf16_f32 v77, v90, v91\n"
    "v_cvt_pk_bf16_f32 v78, v92, v93\n"
    "v_cvt_pk_bf16_f32 v79, v94, v95\n"
    "v_pk_mul_f32 v[120:121], v[120:121], v[240:241] op_sel_hi:[1,0]\n"
    "v_pk_mul_f32 v[122:123], v[122:123], v[240:241] op_sel_hi:[1,0]\n"
    "v_permlane16_swap_b32 v76, v78\n"
    "v_permlane16_swap_b32 v77, v79\n"
    "v_pk_mul_f32 v[124:125], v[124:125], v[240:241] op_sel_hi:[1,0]\n"
    "v_pk_mul_f32 v[126:127], v[126:127], v[240:241] op_sel_hi:[1,0]\n"
    "v_cvt_pk_bf16_f32 v108, v120, v121\n"
    "v_cvt_pk_bf16_f32 v109, v122, v123\n"
    "v_cvt_pk_bf16_f32 v110, v124, v125\n"
    "v_cvt_pk_bf16_f32 v111, v126, v127\n"
    "v_pk_mul_f32 v[152:153], v[152:153], v[240:241] op_sel_hi:[1,0]\n"
    "v_pk_mul_f32 v[154:155], v[154:155], v[240:241] op_sel_hi:[1,0]\n"
    "v_permlane16_swap_b32 v108, v110\n"
    "v_permlane16_swap_b32 v109, v111\n"
    "v_pk_mul_f32 v[156:157], v[156:157], v[240:241] op_sel_hi:[1,0]\n"
    "v_pk_mul_f32 v[158:159], v[158:159], v[240:241] op_sel_hi:[1,0]\n"
    "v_cvt_pk_bf16_f32 v140, v152, v153\n"
    "v_cvt_pk_bf16_f32 v141, v154, v155\n"
    "v_cvt_pk_bf16_f32 v142, v156, v157\n"
    "v_cvt_pk_bf16_f32 v143, v158, v159\n"
    "v_pk_mul_f32 v[184:185], v[184:185], v[240:241] op_sel_hi:[1,0]\n"
    "v_pk_mul_f32 v[186:187], v[186:187], v[240:241] op_sel_hi:[1,0]\n"
    "v_permlane16_swap_b32 v140, v142\n"
    "v_permlane16_swap_b32 v141, v143\n"
    "v_pk_mul_f32 v[188:189], v[188:189], v[240:241] op_sel_hi:[1,0]\n"
    "v_pk_mul_f32 v[190:191], v[190:191], v[240:241] op_sel_hi:[1,0]\n"
    "v_cvt_pk_bf16_f32 v172, v184, v185\n"
    "v_cvt_pk_bf16_f32 v173, v186, v187\n"
    "v_cvt_pk_bf16_f32 v174, v188, v189\n"
    "v_cvt_pk_bf16_f32 v175, v190, v191\n"
    "s_nop 1\n"
    "v_permlane16_swap_b32 v172, v174\n"
    "v_permlane16_swap_b32 v173, v175\n"
    "s_waitcnt lgkmcnt(0)\n"
    "L_p0%=:\n"
    "v_mfma_f32_16x16x32_bf16 v[16:19], a[192:195], v[0:3], 0\n"
    "s_mov_b32 s62, 0\n"
    "v_mfma_f32_16x16x32_bf16 v[16:19], a[196:199], v[4:7], v[16:19]\n"
    "s_mov_b32 s63, 0x4000\n"
    "v_mfma_f32_16x16x32_bf16 v[16:19], a[200:203], v[8:11], v[16:19]\n"
    "s_mov_b32 s64, 0x8000\n"
    "v_mfma_f32_16x16x32_bf16 v[16:19], a[204:207], v[12:15], v[16:19]\n"
    "s_mov_b32 s70, 13\n"
    "v_mfma_f32_16x16x32_bf16 v[20:23], a[208:211], v[0:3], 0\n"
    "s_cmp_eq_u32 s71, 1\n"
    "v_mfma_f32_16x16x32_bf16 v[20:23], a[212:215], v[4:7], v[20:23]\n"
    "s_cselect_b32 s46, 0, s46\n"
    "v_mfma_f32_16x16x32_bf16 v[20:23], a[216:219], v[8:11], v[20:23]\n"
    "s_cselect_b32 s50, 0, s50\n"
    "v_mfma_f32_16x16x32_bf16 v[20:23], a[220:223], v[12:15], v[20:23]\n"
    "s_mov_b32 s58, 0x100\n"
    "v_mfma_f32_16x16x32_bf16 v[48:51], a[192:195], v[32:35], 0\n"
    "s_waitcnt vmcnt(12) lgkmcnt(0)\n"
    "s_barrier\n"
    "v_mfma_f32_16x16x32_bf16 v[48:51], a[196:199], v[36:39], v[48:51]\n"
    "s_add_u32 m0, s65, 98304\n"
    "v_mfma_f32_16x16x32_bf16 v[48:51], a[200:203], v[40:43], v[48:51]\n"
    "buffer_load_dwordx4 v203, s[40:43], s57 offen nt lds\n"
    "v_mfma_f32_16x16x32_bf16 v[48:51], a[204:207], v[44:47], v[48:51]\n"
    "v_mfma_f32_16x16x32_bf16 v[52:55], a[208:211], v[32:35], 0\n"
    "buffer_load_dwordx4 v203, s[40:43], s57 offen offset:2048 nt lds\n"
    "v_mfma_f32_16x16x32_bf16 v[52:55], a[212:215], v[36:39], v[52:55]\n"
    "s_add_u32 s93, s93, 0x1000\n"
    "s_xor_b32 s57, s93, %[rot]\n"
    "s_add_u32 m0, s65, 102400\n"
    "v_mfma_f32_16x16x32_bf16 v[52:55], a[216:219], v[40:43], v[52:55]\n"
    "buffer_load_dwordx4 v203, s[40:43], s57 offen nt lds\n"
    "v_mfma_f32_16x16x32_bf16 v[52:55], a[220:223], v[44:47], v[52:55]\n"
    "v_mfma_f32_16x16x32_bf16 v[80:83], a[192:195], v[64:67], 0\n"
    "buffer_load_dwordx4 v203, s[40:43], s57 offen offset:2048 nt lds\n"
    "v_mfma_f32_16x16x32_bf16 v[80:83], a[196:199], v[68:71], v[80:83]\n"
    "ds_read_b64_tr_b16 a[224:225], v193 offset:0\n"
    "s_add_u32 s93, s93, 0x1000\n"
    "s_xor_b32 s57, s93, %[rot]\n"
    "s_add_u32 m0, s65, 0\n"
    "v_mfma_f32_16x16x32_bf16 v[80:83], a[200:203], v[72:75], v[80:83]\n"
    "buffer_load_dwordx4 v201, s[36:39], s56 offen nt lds\n"
    "v_mfma_f32_16x16x32_bf16 v[80:83], a[204:207], v[76:79], v[80:83]\n"
    "ds_read_b64_tr_b16 a[226:227], v193 offset:4096\n"
    "v_mfma_f32_16x16x32_bf16 v[84:87], a[208:211], v[64:67], 0\n"
    "buffer_load_dwordx4 v202, s[36:39], s56 offen offset:2048 nt lds\n"
    "v_max3_f32 v29, v16, v17, v18\n"
    "v_max3_f32 v61, v48, v49, v50\n"
    "v_max3_f32 v30, v19, v20, v21\n"
    "v_max3_f32 v62, v51, v52, v53\n"
    "v_max3_f32 v29, v29, v22, v23\n"
    "v_max3_f32 v61, v61, v54, v55\n"
    "v_mfma_f32_16x16x32_bf16 v[84:87], a[212:215], v[68:71], v[84:87]\n"
    "ds_read_b64_tr_b16 a[228:229], v194 offset:0\n"
    "s_add_u32 s92, s92, 0x1000\n"
    "s_xor_b32 s56, s92, %[rot]\n"
    "s_add_u32 m0, s65, 4096\n"
    "v_max_f32 v29, v29, v30\n"
    "v_max_f32 v61, v61, v62\n"
    "v_mov_b32 v30, v29\n"
    "v_mov_b32 v62, v61\n"
    "s_nop 0\n"
    "v_permlane16_swap_b32 v29, v30\n"
    "v_mfma_f32_16x16x32_bf16 v[84:87], a[216:219], v[72:75], v[84:87]\n"
    "buffer_load_dwordx4 v201, s[36:39], s56 offen nt lds\n"
    "v_permlane16_swap_b32 v61, v62\n"
    "v_max_f32 v29, v29, v30\n"
    "v_max_f32 v61, v61, v62\n"
    "v_mov_b32 v30, v29\n"
    "v_mov_b32 v62, v61\n"
    "v_mfma_f32_16x16x32_bf16 v[84:87], a[220:223], v[76:79], v[84:87]\n"
    "ds_read_b64_tr_b16 a[230:231], v194 offset:4096\n"
    "s_nop 0\n"
    "v_permlane32_swap_b32 v29, v30\n"
    "v_permlane32_swap_b32 v61, v62\n"
    "v_max_f32 v29, v29, v30\n"
    "v_mfma_f32_16x16x32_bf16 v[112:115], a[192:195], v[96:99], 0\n"
    "buffer_load_dwordx4 v202, s[36:39], s56 offen offset:2048 nt lds\n"
    "v_max_f32 v61, v61, v62\n"
    "v_mov_b32 v28, 0\n"
    "v_mov_b32 v60, 0\n"
    "v_sub_f32 v16, v16, v29\n"
    "v_sub_f32 v48, v48, v61\n"
    "v_sub_f32 v17, v17, v29\n"
    "v_mfma_f32_16x16x32_bf16 v[112:115], a[196:199], v[100:103], v[112:115]\n"
    "ds_read_b64_tr_b16 a[232:233], v193 offset:512\n"
    "s_add_u32 s92, s92, 0x1000\n"
    "s_xor_b32 s56, s92, %[rot]\n"
    "s_add_u32 m0, s65, 114688\n"
    "v_sub_f32 v49, v49, v61\n"
    "v_sub_f32 v18, v18, v29\n"
    "v_sub_f32 v50, v50, v61\n"
    "v_sub_f32 v19, v19, v29\n"
    "v_sub_f32 v51, v51, v61\n"
    "v_sub_f32 v20, v20, v29\n"
    "v_mfma_f32_16x16x32_bf16 v[112:115], a[200:203], v[104:107], v[112:115]\n"
    "buffer_load_dwordx4 v203, s[40:43], s57 offen nt lds\n"
    "v_sub_f32 v52, v52, v61\n"
    "v_sub_f32 v21, v21, v29\n"
    "v_sub_f32 v53, v53, v61\n"
    "v_sub_f32 v22, v22, v29\n"
    "v_sub_f32 v54, v54, v61\n"
    "v_sub_f32 v23, v23, v29\n"
    "v_mfma_f32_16x16x32_bf16 v[112:115], a[204:207], v[108:111], v[112:115]\n"
    "ds_read_b64_tr_b16 a[234:235], v193 offset:4608\n"
    "v_sub_f32 v55, v55, v61\n"
    "v_sub_f32 v24, 0, v29\n"
    "v_sub_f32 v56, 0, v61\n"
    "v_sub_f32 v25, 0, v29\n"
    "v_sub_f32 v57, 0, v61\n"
    "v_sub_f32 v26, 0, v29\n"
    "v_mfma_f32_16x16x32_bf16 v[116:119], a[208:211], v[96:99], 0\n"
    "buffer_load_dwordx4 v203, s[40:43], s57 offen offset:2048 nt lds\n"
    "v_sub_f32 v58, 0, v61\n"
    "v_sub_f32 v27, 0, v29\n"
    "v_sub_f32 v59, 0, v61\n"
    "v_exp_f32 v16, v16\n"
    "v_exp_f32 v48, v48\n"
    "v_mfma_f32_16x16x32_bf16 v[116:119], a[212:215], v[100:103], v[116:119]\n"
    "ds_read_b64_tr_b16 a[236:237], v194 offset:512\n"
    "s_add_u32 s93, s93, 0x1000\n"
    "s_xor_b32 s57, s93, %[rot]\n"
    "s_add_u32 m0, s65, 118784\n"
    "v_exp_f32 v17, v17\n"
    "v_exp_f32 v49, v49\n"
    "v_add_f32 v28, v28, v16\n"
    "v_add_f32 v60, v60, v48\n"
    "v_mfma_f32_16x16x32_bf16 v[116:119], a[216:219], v[104:107], v[116:119]\n"
    "buffer_load_dwordx4 v203, s[40:43], s57 offen nt lds\n"
    "v_exp_f32 v18, v18\n"
    "v_exp_f32 v50, v50\n"
    "v_add_f32 v28, v28, v17\n"
    "v_add_f32 v60, v60, v49\n"
    "v_mfma_f32_16x16x32_bf16 v[116:119], a[220:223], v[108:111], v[116:119]\n"
    "ds_read_b64_tr_b16 a[238:239], v194 offset:4608\n"
    "v_exp_f32 v19, v19\n"
    "v_exp_f32 v51, v51\n"
    "v_cvt_pk_bf16_f32 v16, v16, v17\n"
    "v_cvt_pk_bf16_f32 v48, v48, v49\n"
    "v_mfma_f32_16x16x32_bf16 v[144:147], a[192:195], v[128:131], 0\n"
    "buffer_load_dwordx4 v203, s[40:43], s57 offen offset:2048 nt lds\n"
    "v_add_f32 v28, v28, v18\n"
    "v_add_f32 v60, v60, v50\n"
    "v_exp_f32 v20, v20\n"
    "v_exp_f32 v52, v52\n"
    "v_mfma_f32_16x16x32_bf16 v[144:147], a[196:199], v[132:135], v[144:147]\n"
    "ds_read_b64_tr_b16 a[240:241], v193 offset:1024\n"
    "s_add_u32 s93, s93, 0x1000\n"
    "s_xor_b32 s57, s93, %[rot]\n"
    "s_add_u32 m0, s77, 0\n"
    "v_add_f32 v28, v28, v19\n"
    "v_add_f32 v60, v60, v51\n"
    "v_exp_f32 v21, v21\n"
    "v_exp_f32 v53, v53\n"
    "v_mfma_f32_16x16x32_bf16 v[144:147], a[200:203], v[136:139], v[144:147]\n"
    "buffer_load_dwordx4 v208, s[84:87], s74 offen lds\n"
    "v_cvt_pk_bf16_f32 v17, v18, v19\n"
    "v_cvt_pk_bf16_f32 v49, v50, v51\n"
    "v_add_f32 v28, v28, v20\n"
    "v_add_f32 v60, v60, v52\n"
    "v_exp_f32 v22, v22\n"
    "v_mfma_f32_16x16x32_bf16 v[144:147], a[204:207], v[140:143], v[144:147]\n"
    "ds_read_b64_tr_b16 a[242:243], v193 offset:5120\n"
    "s_add_u32 m0, s77, 992\n"
    "v_exp_f32 v54, v54\n"
    "v_add_f32 v28, v28, v21\n"
    "v_add_f32 v60, v60, v53\n"
    "v_exp_f32 v23, v23\n"
    "v_mfma_f32_16x16x32_bf16 v[148:151], a[208:211], v[128:131], 0\n"
    "buffer_load_dwordx4 v208, s[84:87], s74 offen offset:32 lds\n"
    "v_exp_f32 v55, v55\n"
    "v_cvt_pk_bf16_f32 v18, v20, v21\n"
    "v_cvt_pk_bf16_f32 v50, v52, v53\n"
    "v_add_f32 v28, v28, v22\n"
    "v_add_f32 v60, v60, v54\n"
    "v_mfma_f32_16x16x32_bf16 v[148:151], a[212:215], v[132:135], v[148:151]\n"
    "ds_read_b64_tr_b16 a[244:245], v194 offset:1024\n"
    "s_add_u32 m0, s77, 1984\n"
    "v_add_f32 v28, v28, v23\n"
    "v_add_f32 v60, v60, v55\n"
    "v_cvt_pk_bf16_f32 v19, v22, v23\n"
    "v_cvt_pk_bf16_f32 v51, v54, v55\n"
    "v_max3_f32 v93, v80, v81, v82\n"
    "v_max3_f32 v125, v112, v113, v114\n"
    "v_mfma_f32_16x16x32_bf16 v[148:151], a[216:219], v[136:139], v[148:151]\n"
    "buffer_load_dwordx4 v208, s[84:87], s74 offen offset:64 lds\n"
    "v_max3_f32 v94, v83, v84, v85\n"
    "v_max3_f32 v126, v115, v116, v117\n"
    "v_max3_f32 v93, v93, v86, v87\n"
    "v_max3_f32 v125, v125, v118, v119\n"
    "v_max_f32 v93, v93, v94\n"
    "v_mfma_f32_16x16x32_bf16 v[148:151], a[220:223], v[140:143], v[148:151]\n"
    "ds_read_b64_tr_b16 a[246:247], v194 offset:5120\n"
    "s_add_u32 m0, s77, 2976\n"
    "v_max_f32 v125, v125, v126\n"
    "v_mov_b32 v94, v93\n"
    "v_mov_b32 v126, v125\n"
    "s_nop 0\n"
    "v_permlane16_swap_b32 v93, v94\n"
    "v_mfma_f32_16x16x32_bf16 v[176:179], a[192:195], v[160:163], 0\n"
    "buffer_load_dwordx4 v208, s[84:87], s74 offen offset:96 lds\n"
    "v_permlane16_swap_b32 v125, v126\n"
    "v_max_f32 v93, v93, v94\n"
    "v_max_f32 v125, v125, v126\n"
    "v_mov_b32 v94, v93\n"
    "v_mfma_f32_16x16x32_bf16 v[176:179], a[196:199], v[164:167], v[176:179]\n"
    "ds_read_b64_tr_b16 a[248:249], v193 offset:1536\n"
    "v_mov_b32 v126, v125\n"
    "s_nop 0\n"
    "v_permlane32_swap_b32 v93, v94\n"
    "v_permlane32_swap_b32 v125, v126\n"
    "v_mfma_f32_16x16x32_bf16 v[176:179], a[200:203], v[168:171], v[176:179]\n"
    "ds_read_b128 a[192:195], v192 offset:16384\n"
    "v_max_f32 v93, v93, v94\n"
    "v_max_f32 v125, v125, v126\n"
    "v_mov_b32 v92, 0\n"
    "v_mov_b32 v124, 0\n"
    "v_sub_f32 v80, v80, v93\n"
    "v_mfma_f32_16x16x32_bf16 v[176:179], a[204:207], v[172:175], v[176:179]\n"
    "ds_read_b64_tr_b16 a[250:251], v193 offset:5632\n"
    "v_sub_f32 v112, v112, v125\n"
    "v_sub_f32 v81, v81, v93\n"
    "v_sub_f32 v113, v113, v125\n"
    "v_sub_f32 v82, v82, v93\n"
    "v_sub_f32 v114, v114, v125\n"
    "v_mfma_f32_16x16x32_bf16 v[180:183], a[208:211], v[160:163], 0\n"
    "ds_read_b128 a[196:199], v192 offset:16896\n"
    "v_sub_f32 v83, v83, v93\n"
    "v_sub_f32 v115, v115, v125\n"
    "v_sub_f32 v84, v84, v93\n"
    "v_sub_f32 v116, v116, v125\n"
    "v_sub_f32 v85, v85, v93\n"
    "v_mfma_f32_16x16x32_bf16 v[180:183], a[212:215], v[164:167], v[180:183]\n"
    "ds_read_b64_tr_b16 a[252:253], v194 offset:1536\n"
    "v_sub_f32 v117, v117, v125\n"
    "v_sub_f32 v86, v86, v93\n"
    "v_sub_f32 v118, v118, v125\n"
    "v_sub_f32 v87, v87, v93\n"
    "v_sub_f32 v119, v119, v125\n"
    "v_mfma_f32_16x16x32_bf16 v[180:183], a[216:219], v[168:171], v[180:183]\n"
    "ds_read_b128 a[200:203], v192 offset:17408\n"
    "v_sub_f32 v88, 0, v93\n"
    "v_sub_f32 v120, 0, v125\n"
    "v_sub_f32 v89, 0, v93\n"
    "v_sub_f32 v121, 0, v125\n"
    "v_mfma_f32_16x16x32_bf16 v[180:183], a[220:223], v[172:175], v[180:183]\n"
    "ds_read_b64_tr_b16 a[254:255], v194 offset:5632\n"
    "v_sub_f32 v90, 0, v93\n"
    "v_sub_f32 v122, 0, v125\n"
    "v_sub_f32 v91, 0, v93\n"
    "v_sub_f32 v123, 0, v125\n"
    "s_waitcnt lgkmcnt(7)\n"
    "v_mfma_f32_16x16x32_bf16 a[0:3], a[224:227], v[16:19], a[0:3]\n"
    "s_add_u32 m0, s77, 3968\n"
    "v_exp_f32 v80, v80\n"
    "v_exp_f32 v112, v112\n"
    "v_exp_f32 v81, v81\n"
    "v_mfma_f32_16x16x32_bf16 a[32:35], a[224:227], v[48:51], a[32:35]\n"
    "buffer_load_dwordx4 v208, s[84:87], s74 offen offset:128 lds\n"
    "v_exp_f32 v113, v113\n"
    "v_add_f32 v92, v92, v80\n"
    "v_add_f32 v124, v124, v112\n"
    "v_exp_f32 v82, v82\n"
    "v_mfma_f32_16x16x32_bf16 a[4:7], a[228:231], v[16:19], a[4:7]\n"
    "v_exp_f32 v114, v114\n"
    "v_add_f32 v92, v92, v81\n"
    "v_add_f32 v124, v124, v113\n"
    "v_mfma_f32_16x16x32_bf16 a[36:39], a[228:231], v[48:51], a[36:39]\n"
    "v_exp_f32 v83, v83\n"
    "v_exp_f32 v115, v115\n"
    "v_mfma_f32_16x16x32_bf16 a[8:11], a[232:235], v[16:19], a[8:11]\n"
    "v_cvt_pk_bf16_f32 v80, v80, v81\n"
    "v_cvt_pk_bf16_f32 v112, v112, v113\n"
    "v_add_f32 v92, v92, v82\n"
    "v_add_f32 v124, v124, v114\n"
    "v_exp_f32 v84, v84\n"
    "v_exp_f32 v116, v116\n"
    "v_add_f32 v92, v92, v83\n"
    "v_add_f32 v124, v124, v115\n"
    "v_mfma_f32_16x16x32_bf16 a[40:43], a[232:235], v[48:51], a[40:43]\n"
    "v_exp_f32 v85, v85\n"
    "v_exp_f32 v117, v117\n"
    "v_cvt_pk_bf16_f32 v81, v82, v83\n"
    "v_cvt_pk_bf16_f32 v113, v114, v115\n"
    "v_add_f32 v92, v92, v84\n"
    "v_add_f32 v124, v124, v116\n"
    "v_exp_f32 v86, v86\n"
    "v_mfma_f32_16x16x32_bf16 a[12:15], a[236:239], v[16:19], a[12:15]\n"
    "ds_read_b128 a[204:207], v192 offset:17920\n"
    "v_exp_f32 v118, v118\n"
    "v_add_f32 v92, v92, v85\n"
    "v_add_f32 v124, v124, v117\n"
    "v_exp_f32 v87, v87\n"
    "v_exp_f32 v119, v119\n"
    "v_cvt_pk_bf16_f32 v82, v84, v85\n"
    "v_cvt_pk_bf16_f32 v114, v116, v117\n"
    "v_mfma_f32_16x16x32_bf16 a[44:47], a[236:239], v[48:51], a[44:47]\n"
    "ds_read_b128 a[208:211], v192 offset:20480\n"
    "v_add_f32 v92, v92, v86\n"
    "v_add_f32 v124, v124, v118\n"
    "v_add_f32 v92, v92, v87\n"
    "v_add_f32 v124, v124, v119\n"
    "v_cvt_pk_bf16_f32 v83, v86, v87\n"
    "v_cvt_pk_bf16_f32 v115, v118, v119\n"
    "v_max3_f32 v157, v144, v145, v146\n"
    "v_max3_f32 v189, v176, v177, v178\n"
    "v_max3_f32 v158, v147, v148, v149\n"
    "v_max3_f32 v190, v179, v180, v181\n"
    "v_mfma_f32_16x16x32_bf16 a[16:19], a[240:243], v[16:19], a[16:19]\n"
    "ds_read_b128 a[212:215], v192 offset:20992\n"
    "v_max3_f32 v157, v157, v150, v151\n"
    "v_max3_f32 v189, v189, v182, v183\n"
    "v_max_f32 v157, v157, v158\n"
    "v_max_f32 v189, v189, v190\n"
    "v_mov_b32 v158, v157\n"
    "v_mov_b32 v190, v189\n"
    "s_nop 0\n"
    "v_permlane16_swap_b32 v157, v158\n"
    "v_mfma_f32_16x16x32_bf16 a[48:51], a[240:243], v[48:51], a[48:51]\n"
    "ds_read_b128 a[216:219], v192 offset:21504\n"
    "v_permlane16_swap_b32 v189, v190\n"
    "v_max_f32 v157, v157, v158\n"
    "v_max_f32 v189, v189, v190\n"
    "v_mov_b32 v158, v157\n"
    "v_mov_b32 v190, v189\n"
    "s_nop 0\n"
    "v_permlane32_swap_b32 v157, v158\n"
    "v_mfma_f32_16x16x32_bf16 a[20:23], a[244:247], v[16:19], a[20:23]\n"
    "ds_read_b128 a[220:223], v192 offset:22016\n"
    "v_permlane32_swap_b32 v189, v190\n"
    "v_max_f32 v157, v157, v158\n"
    "v_max_f32 v189, v189, v190\n"
    "v_mov_b32 v156, 0\n"
    "v_mov_b32 v188, 0\n"
    "v_sub_f32 v144, v144, v157\n"
    "v_sub_f32 v176, v176, v189\n"
    "v_sub_f32 v145, v145, v157\n"
    "v_mfma_f32_16x16x32_bf16 a[52:55], a[244:247], v[48:51], a[52:55]\n"
    "v_sub_f32 v177, v177, v189\n"
    "v_sub_f32 v146, v146, v157\n"
    "v_sub_f32 v178, v178, v189\n"
    "v_sub_f32 v147, v147, v157\n"
    "v_sub_f32 v179, v179, v189\n"
    "v_sub_f32 v148, v148, v157\n"
    "v_sub_f32 v180, v180, v189\n"
    "v_sub_f32 v149, v149, v157\n"
    "v_sub_f32 v181, v181, v189\n"
    "s_waitcnt lgkmcnt(5)\n"
    "v_mfma_f32_16x16x32_bf16 a[24:27], a[248:251], v[16:19], a[24:27]\n"
    "v_sub_f32 v150, v150, v157\n"
    "v_sub_f32 v182, v182, v189\n"
    "v_sub_f32 v151, v151, v157\n"
    "v_sub_f32 v183, v183, v189\n"
    "v_sub_f32 v152, 0, v157\n"
    "v_sub_f32 v184, 0, v189\n"
    "v_sub_f32 v153, 0, v157\n"
    "v_sub_f32 v185, 0, v189\n"
    "v_sub_f32 v154, 0, v157\n"
    "v_mfma_f32_16x16x32_bf16 a[56:59], a[248:251], v[48:51], a[56:59]\n"
    "v_sub_f32 v186, 0, v189\n"
    "v_sub_f32 v155, 0, v157\n"
    "v_sub_f32 v187, 0, v189\n"
    "v_exp_f32 v144, v144\n"
    "v_exp_f32 v176, v176\n"
    "v_exp_f32 v145, v145\n"
    "v_mfma_f32_16x16x32_bf16 a[28:31], a[252:255], v[16:19], a[28:31]\n"
    "v_exp_f32 v177, v177\n"
    "v_add_f32 v156, v156, v144\n"
    "v_exp_f32 v146, v146\n"
    "v_add_f32 v156, v156, v145\n"
    "v_exp_f32 v147, v147\n"
    "v_cvt_pk_bf16_f32 v144, v144, v145\n"
    "v_mfma_f32_16x16x32_bf16 a[60:63], a[252:255], v[48:51], a[60:63]\n"
    "v_add_f32 v156, v156, v146\n"
    "v_exp_f32 v148, v148\n"
    "v_exp_f32 v149, v149\n"
    "v_exp_f32 v150, v150\n"
    "v_exp_f32 v151, v151\n"
    "L_loop%=:\n"
    "v_mfma_f32_16x16x32_bf16 v[16:19], a[192:195], v[0:3], v[24:27]\n"
    "v_exp_f32 v178, v178\n"
    "v_mfma_f32_16x16x32_bf16 v[16:19], a[196:199], v[4:7], v[16:19]\n"
    "v_add_u32 v212, s63, v193\n"
    "v_add_u32 v213, s63, v194\n"
    "v_mfma_f32_16x16x32_bf16 v[16:19], a[200:203], v[8:11], v[16:19]\n"
    "s_waitcnt lgkmcnt(0)\n"
    "v_exp_f32 v179, v179\n"
    "v_mfma_f32_16x16x32_bf16 v[16:19], a[204:207], v[12:15], v[16:19]\n"
    "v_exp_f32 v180, v180\n"
    "v_mfma_f32_16x16x32_bf16 v[20:23], a[208:211], v[0:3], v[24:27]\n"
    "v_add_u32 v211, s64, v192\n"
    "v_add_f32 v156, v156, v147\n"
    "v_mfma_f32_16x16x32_bf16 v[20:23], a[212:215], v[4:7], v[20:23]\n"
    "v_exp_f32 v181, v181\n"
    "v_cvt_pk_bf16_f32 v145, v146, v147\n"
    "v_mfma_f32_16x16x32_bf16 v[20:23], a[216:219], v[8:11], v[20:23]\n"
    "v_exp_f32 v182, v182\n"
    "v_mfma_f32_16x16x32_bf16 v[20:23], a[220:223], v[12:15], v[20:23]\n"
    "v_exp_f32 v183, v183\n"
    "v_mfma_f32_16x16x32_bf16 v[48:51], a[192:195], v[32:35], v[56:59]\n"
    "s_waitcnt vmcnt(16) lgkmcnt(0)\n"
    "s_barrier\n"
    "v_mfma_f32_16x16x32_bf16 v[48:51], a[196:199], v[36:39], v[48:51]\n"
    "v_add_f32 v156, v156, v148\n"
    "v_add_f32 v156, v156, v149\n"
    "v_mfma_f32_16x16x32_bf16 v[48:51], a[200:203], v[40:43], v[48:51]\n"
    "s_add_u32 m0, s63, s65\n"
    "v_cvt_pk_bf16_f32 v146, v148, v149\n"
    "v_add_f32 v156, v156, v150\n"
    "v_mfma_f32_16x16x32_bf16 v[48:51], a[204:207], v[44:47], v[48:51]\n"
    "buffer_load_dwordx4 v201, s[36:39], s56 offen nt lds\n"
    "v_add_f32 v156, v156, v151\n"
    "v_cvt_pk_bf16_f32 v147, v150, v151\n"
    "v_mfma_f32_16x16x32_bf16 v[52:55], a[208:211], v[32:35], v[56:59]\n"
    "v_add_f32 v188, v188, v176\n"
    "v_add_f32 v188, v188, v177\n"
    "v_mfma_f32_16x16x32_bf16 v[52:55], a[212:215], v[36:39], v[52:55]\n"
    "v_cvt_pk_bf16_f32 v176, v176, v177\n"
    "v_add_f32 v188, v188, v178\n"
    "v_mfma_f32_16x16x32_bf16 v[52:55], a[216:219], v[40:43], v[52:55]\n"
    "v_add_f32 v188, v188, v179\n"
    "v_cvt_pk_bf16_f32 v177, v178, v179\n"
    "v_mfma_f32_16x16x32_bf16 v[52:55], a[220:223], v[44:47], v[52:55]\n"
    "v_add_f32 v188, v188, v180\n"
    "v_add_f32 v188, v188, v181\n"
    "v_mfma_f32_16x16x32_bf16 a[64:67], a[224:227], v[80:83], a[64:67]\n"
    "buffer_load_dwordx4 v202, s[36:39], s56 offen offset:2048 nt lds\n"
    "v_cvt_pk_bf16_f32 v178, v180, v181\n"
    "v_mfma_f32_16x16x32_bf16 a[96:99], a[224:227], v[112:115], a[96:99]\n"
    "s_add_u32 s92, s92, 0x1000\n"
    "v_add_f32 v188, v188, v182\n"
    "v_add_f32 v188, v188, v183\n"
    "v_cvt_pk_bf16_f32 v179, v182, v183\n"
    "v_mfma_f32_16x16x32_bf16 a[68:71], a[228:231], v[80:83], a[68:71]\n"
    "s_xor_b32 s56, s92, %[rot]\n"
    "v_exp_f32 v16, v16\n"
    "v_mfma_f32_16x16x32_bf16 a[100:103], a[228:231], v[112:115], a[100:103]\n"
    "v_exp_f32 v17, v17\n"
    "v_mfma_f32_16x16x32_bf16 a[72:75], a[232:235], v[80:83], a[72:75]\n"
    "s_add_u32 m0, s63, s66\n"
    "v_exp_f32 v18, v18\n"
    "v_mfma_f32_16x16x32_bf16 a[104:107], a[232:235], v[112:115], a[104:107]\n"
    "buffer_load_dwordx4 v201, s[36:39], s56 offen nt lds\n"
    "v_add_f32 v28, v28, v16\n"
    "v_mfma_f32_16x16x32_bf16 a[76:79], a[236:239], v[80:83], a[76:79]\n"
    "v_exp_f32 v19, v19\n"
    "v_mfma_f32_16x16x32_bf16 a[108:111], a[236:239], v[112:115], a[108:111]\n"
    "v_exp_f32 v20, v20\n"
    "v_mfma_f32_16x16x32_bf16 a[80:83], a[240:243], v[80:83], a[80:83]\n"
    "v_exp_f32 v21, v21\n"
    "v_add_f32 v28, v28, v17\n"
    "v_mfma_f32_16x16x32_bf16 a[112:115], a[240:243], v[112:115], a[112:115]\n"
    "v_exp_f32 v22, v22\n"
    "v_mfma_f32_16x16x32_bf16 a[84:87], a[244:247], v[80:83], a[84:87]\n"
    "buffer_load_dwordx4 v202, s[36:39], s56 offen offset:2048 nt lds\n"
    "v_cvt_pk_bf16_f32 v16, v16, v17\n"
    "v_mfma_f32_16x16x32_bf16 a[116:119], a[244:247], v[112:115], a[116:119]\n"
    "s_add_u32 s92, s92, 0x1000\n"
    "v_exp_f32 v23, v23\n"
    "v_mfma_f32_16x16x32_bf16 a[88:91], a[248:251], v[80:83], a[88:91]\n"
    "s_xor_b32 s56, s92, %[rot]\n"
    "v_exp_f32 v48, v48\n"
    "v_mfma_f32_16x16x32_bf16 a[120:123], a[248:251], v[112:115], a[120:123]\n"
    "v_exp_f32 v49, v49\n"
    "v_mfma_f32_16x16x32_bf16 a[92:95], a[252:255], v[80:83], a[92:95]\n"
    "s_add_u32 m0, s62, s67\n"
    "v_exp_f32 v50, v50\n"
    "v_add_f32 v28, v28, v18\n"
    "v_mfma_f32_16x16x32_bf16 a[124:127], a[252:255], v[112:115], a[124:127]\n"
    "buffer_load_dwordx4 v203, s[40:43], s57 offen nt lds\n"
    "v_add_f32 v28, v28, v19\n"
    "v_mfma_f32_16x16x32_bf16 v[80:83], a[192:195], v[64:67], v[88:91]\n"
    "v_exp_f32 v51, v51\n"
    "v_mfma_f32_16x16x32_bf16 v[80:83], a[196:199], v[68:71], v[80:83]\n"
    "v_exp_f32 v52, v52\n"
    "v_mfma_f32_16x16x32_bf16 v[80:83], a[200:203], v[72:75], v[80:83]\n"
    "v_exp_f32 v53, v53\n"
    "v_mfma_f32_16x16x32_bf16 v[80:83], a[204:207], v[76:79], v[80:83]\n"
    "v_exp_f32 v54, v54\n"
    "v_mfma_f32_16x16x32_bf16 v[84:87], a[208:211], v[64:67], v[88:91]\n"
    "buffer_load_dwordx4 v203, s[40:43], s57 offen offset:2048 nt lds\n"
    "v_cvt_pk_bf16_f32 v17, v18, v19\n"
    "v_mfma_f32_16x16x32_bf16 v[84:87], a[212:215], v[68:71], v[84:87]\n"
    "s_add_u32 s93, s93, 0x1000\n"
    "v_exp_f32 v55, v55\n"
    "v_add_f32 v28, v28, v20\n"
    "v_mfma_f32_16x16x32_bf16 v[84:87], a[216:219], v[72:75], v[84:87]\n"
    "s_xor_b32 s57, s93, %[rot]\n"
    "v_add_f32 v28, v28, v21\n"
    "v_cvt_pk_bf16_f32 v18, v20, v21\n"
    "v_mfma_f32_16x16x32_bf16 v[84:87], a[220:223], v[76:79], v[84:87]\n"
    "v_add_f32 v28, v28, v22\n"
    "v_add_f32 v28, v28, v23\n"
    "v_mfma_f32_16x16x32_bf16 v[112:115], a[192:195], v[96:99], v[120:123]\n"
    "s_add_u32 m0, s62, s68\n"
    "v_cvt_pk_bf16_f32 v19, v22, v23\n"
    "v_add_f32 v60, v60, v48\n"
    "v_mfma_f32_16x16x32_bf16 v[112:115], a[196:199], v[100:103], v[112:115]\n"
    "buffer_load_dwordx4 v203, s[40:43], s57 offen nt lds\n"
    "v_add_f32 v60, v60, v49\n"
    "v_mfma_f32_16x16x32_bf16 v[112:115], a[200:203], v[104:107], v[112:115]\n"
    "v_cvt_pk_bf16_f32 v48, v48, v49\n"
    "v_add_f32 v60, v60, v50\n"
    "v_mfma_f32_16x16x32_bf16 v[112:115], a[204:207], v[108:111], v[112:115]\n"
    "v_add_f32 v60, v60, v51\n"
    "v_cvt_pk_bf16_f32 v49, v50, v51\n"
    "v_add_f32 v60, v60, v52\n"
    "v_mfma_f32_16x16x32_bf16 v[116:119], a[208:211], v[96:99], v[120:123]\n"
    "v_exp_f32 v80, v80\n"
    "v_mfma_f32_16x16x32_bf16 v[116:119], a[212:215], v[100:103], v[116:119]\n"
    "v_exp_f32 v81, v81\n"
    "v_mfma_f32_16x16x32_bf16 v[116:119], a[216:219], v[104:107], v[116:119]\n"
    "buffer_load_dwordx4 v203, s[40:43], s57 offen offset:2048 nt lds\n"
    "v_add_f32 v60, v60, v53\n"
    "v_mfma_f32_16x16x32_bf16 v[116:119], a[220:223], v[108:111], v[116:119]\n"
    "s_add_u32 s93, s93, 0x1000\n"
    "v_exp_f32 v82, v82\n"
    "v_mfma_f32_16x16x32_bf16 a[128:131], a[224:227], v[144:147], a[128:131]\n"
    "s_xor_b32 s57, s93, %[rot]\n"
    "v_exp_f32 v83, v83\n"
    "v_mfma_f32_16x16x32_bf16 a[160:163], a[224:227], v[176:179], a[160:163]\n"
    "ds_read_b64_tr_b16 a[224:225], v212 offset:0\n"
    "v_cvt_pk_bf16_f32 v50, v52, v53\n"
    "v_add_f32 v60, v60, v54\n"
    "v_mfma_f32_16x16x32_bf16 a[132:135], a[228:231], v[144:147], a[132:135]\n"
    "s_mov_b32 s62, s63\n"
    "v_exp_f32 v84, v84\n"
    "v_mfma_f32_16x16x32_bf16 a[164:167], a[228:231], v[176:179], a[164:167]\n"
    "ds_read_b64_tr_b16 a[226:227], v212 offset:4096\n"
    "v_add_f32 v60, v60, v55\n"
    "v_mfma_f32_16x16x32_bf16 a[136:139], a[232:235], v[144:147], a[136:139]\n"
    "s_mov_b32 s63, s64\n"
    "v_exp_f32 v85, v85\n"
    "v_mfma_f32_16x16x32_bf16 a[168:171], a[232:235], v[176:179], a[168:171]\n"
    "ds_read_b64_tr_b16 a[228:229], v213 offset:0\n"
    "v_cvt_pk_bf16_f32 v51, v54, v55\n"
    "v_mfma_f32_16x16x32_bf16 a[140:143], a[236:239], v[144:147], a[140:143]\n"
    "s_add_u32 s64, s64, 0x4000\n"
    "v_exp_f32 v86, v86\n"
    "v_mfma_f32_16x16x32_bf16 a[172:175], a[236:239], v[176:179], a[172:175]\n"
    "ds_read_b64_tr_b16 a[230:231], v213 offset:4096\n"
    "v_add_f32 v92, v92, v80\n"
    "v_mfma_f32_16x16x32_bf16 a[144:147], a[240:243], v[144:147], a[144:147]\n"
    "s_and_b32 s64, s64, 0xffff\n"
    "v_exp_f32 v87, v87\n"
    "v_add_f32 v92, v92, v81\n"
    "v_mfma_f32_16x16x32_bf16 a[176:179], a[240:243], v[176:179], a[176:179]\n"
    "ds_read_b64_tr_b16 a[232:233], v212 offset:512\n"
    "v_cvt_pk_bf16_f32 v80, v80, v81\n"
    "v_mfma_f32_16x16x32_bf16 a[148:151], a[244:247], v[144:147], a[148:151]\n"
    "s_sub_u32 s70, s70, 1\n"
    "v_exp_f32 v112, v112\n"
    "v_mfma_f32_16x16x32_bf16 a[180:183], a[244:247], v[176:179], a[180:183]\n"
    "ds_read_b64_tr_b16 a[234:235], v212 offset:4608\n"
    "v_add_f32 v92, v92, v82\n"
    "v_mfma_f32_16x16x32_bf16 a[152:155], a[248:251], v[144:147], a[152:155]\n"
    "s_cmp_lg_u32 s70, 0\n"
    "v_exp_f32 v113, v113\n"
    "v_mfma_f32_16x16x32_bf16 a[184:187], a[248:251], v[176:179], a[184:187]\n"
    "ds_read_b64_tr_b16 a[236:237], v213 offset:512\n"
    "v_add_f32 v92, v92, v83\n"
    "v_mfma_f32_16x16x32_bf16 a[156:159], a[252:255], v[144:147], a[156:159]\n"
    "v_exp_f32 v114, v114\n"
    "v_cvt_pk_bf16_f32 v81, v82, v83\n"
    "v_mfma_f32_16x16x32_bf16 a[188:191], a[252:255], v[176:179], a[188:191]\n"
    "ds_read_b64_tr_b16 a[238:239], v213 offset:4608\n"
    "v_add_f32 v92, v92, v84\n"
    "v_mfma_f32_16x16x32_bf16 v[144:147], a[192:195], v[128:131], v[152:155]\n"
    "v_exp_f32 v115, v115\n"
    "v_mfma_f32_16x16x32_bf16 v[144:147], a[196:199], v[132:135], v[144:147]\n"
    "ds_read_b64_tr_b16 a[240:241], v212 offset:1024\n"
    "v_add_f32 v92, v92, v85\n"
    "v_mfma_f32_16x16x32_bf16 v[144:147], a[200:203], v[136:139], v[144:147]\n"
    "v_exp_f32 v116, v116\n"
    "v_mfma_f32_16x16x32_bf16 v[144:147], a[204:207], v[140:143], v[144:147]\n"
    "ds_read_b64_tr_b16 a[242:243], v212 offset:5120\n"
    "v_cvt_pk_bf16_f32 v82, v84, v85\n"
    "v_mfma_f32_16x16x32_bf16 v[148:151], a[208:211], v[128:131], v[152:155]\n"
    "v_exp_f32 v117, v117\n"
    "v_mfma_f32_16x16x32_bf16 v[148:151], a[212:215], v[132:135], v[148:151]\n"
    "ds_read_b64_tr_b16 a[244:245], v213 offset:1024\n"
    "v_add_f32 v92, v92, v86\n"
    "v_add_f32 v92, v92, v87\n"
    "v_mfma_f32_16x16x32_bf16 v[148:151], a[216:219], v[136:139], v[148:151]\n"
    "v_exp_f32 v118, v118\n"
    "v_mfma_f32_16x16x32_bf16 v[148:151], a[220:223], v[140:143], v[148:151]\n"
    "ds_read_b64_tr_b16 a[246:247], v213 offset:5120\n"
    "v_cvt_pk_bf16_f32 v83, v86, v87\n"
    "v_mfma_f32_16x16x32_bf16 v[176:179], a[192:195], v[160:163], v[184:187]\n"
    "v_exp_f32 v119, v119\n"
    "v_mfma_f32_16x16x32_bf16 v[176:179], a[196:199], v[164:167], v[176:179]\n"
    "ds_read_b64_tr_b16 a[248:249], v212 offset:1536\n"
    "v_add_f32 v124, v124, v112\n"
    "v_mfma_f32_16x16x32_bf16 v[176:179], a[200:203], v[168:171], v[176:179]\n"
    "ds_read_b128 a[192:195], v211 offset:0\n"
    "v_add_f32 v124, v124, v113\n"
    "v_mfma_f32_16x16x32_bf16 v[176:179], a[204:207], v[172:175], v[176:179]\n"
    "ds_read_b64_tr_b16 a[250:251], v212 offset:5632\n"
    "v_cvt_pk_bf16_f32 v112, v112, v113\n"
    "v_add_f32 v124, v124, v114\n"
    "v_mfma_f32_16x16x32_bf16 v[180:183], a[208:211], v[160:163], v[184:187]\n"
    "ds_read_b128 a[196:199], v211 offset:512\n"
    "v_add_f32 v124, v124, v115\n"
    "v_mfma_f32_16x16x32_bf16 v[180:183], a[212:215], v[164:167], v[180:183]\n"
    "ds_read_b64_tr_b16 a[252:253], v213 offset:1536\n"
    "v_cvt_pk_bf16_f32 v113, v114, v115\n"
    "v_mfma_f32_16x16x32_bf16 v[180:183], a[216:219], v[168:171], v[180:183]\n"
    "ds_read_b128 a[200:203], v211 offset:1024\n"
    "v_add_f32 v124, v124, v116\n"
    "v_mfma_f32_16x16x32_bf16 v[180:183], a[220:223], v[172:175], v[180:183]\n"
    "ds_read_b64_tr_b16 a[254:255], v213 offset:5632\n"
    "s_waitcnt lgkmcnt(7)\n"
    "v_mfma_f32_16x16x32_bf16 a[0:3], a[224:227], v[16:19], a[0:3]\n"
    "v_exp_f32 v144, v144\n"
    "v_mfma_f32_16x16x32_bf16 a[32:35], a[224:227], v[48:51], a[32:35]\n"
    "v_exp_f32 v145, v145\n"
    "v_add_f32 v124, v124, v117\n"
    "v_mfma_f32_16x16x32_bf16 a[4:7], a[228:231], v[16:19], a[4:7]\n"
    "v_exp_f32 v146, v146\n"
    "v_mfma_f32_16x16x32_bf16 a[36:39], a[228:231], v[48:51], a[36:39]\n"
    "v_exp_f32 v147, v147\n"
    "v_mfma_f32_16x16x32_bf16 a[8:11], a[232:235], v[16:19], a[8:11]\n"
    "v_exp_f32 v148, v148\n"
    "v_mfma_f32_16x16x32_bf16 a[40:43], a[232:235], v[48:51], a[40:43]\n"
    "v_exp_f32 v149, v149\n"
    "v_mfma_f32_16x16x32_bf16 a[12:15], a[236:239], v[16:19], a[12:15]\n"
    "ds_read_b128 a[204:207], v211 offset:1536\n"
    "v_cvt_pk_bf16_f32 v114, v116, v117\n"
    "v_mfma_f32_16x16x32_bf16 a[44:47], a[236:239], v[48:51], a[44:47]\n"
    "ds_read_b128 a[208:211], v211 offset:4096\n"
    "v_add_f32 v124, v124, v118\n"
    "v_mfma_f32_16x16x32_bf16 a[16:19], a[240:243], v[16:19], a[16:19]\n"
    "ds_read_b128 a[212:215], v211 offset:4608\n"
    "v_add_f32 v124, v124, v119\n"
    "v_cvt_pk_bf16_f32 v115, v118, v119\n"
    "v_mfma_f32_16x16x32_bf16 a[48:51], a[240:243], v[48:51], a[48:51]\n"
    "ds_read_b128 a[216:219], v211 offset:5120\n"
    "v_add_f32 v156, v156, v144\n"
    "v_mfma_f32_16x16x32_bf16 a[20:23], a[244:247], v[16:19], a[20:23]\n"
    "ds_read_b128 a[220:223], v211 offset:5632\n"
    "v_add_f32 v156, v156, v145\n"
    "v_mfma_f32_16x16x32_bf16 a[52:55], a[244:247], v[48:51], a[52:55]\n"
    "s_waitcnt lgkmcnt(5)\n"
    "v_exp_f32 v150, v150\n"
    "v_mfma_f32_16x16x32_bf16 a[24:27], a[248:251], v[16:19], a[24:27]\n"
    "v_exp_f32 v151, v151\n"
    "v_mfma_f32_16x16x32_bf16 a[56:59], a[248:251], v[48:51], a[56:59]\n"
    "v_exp_f32 v176, v176\n"
    "v_mfma_f32_16x16x32_bf16 a[28:31], a[252:255], v[16:19], a[28:31]\n"
    "v_exp_f32 v177, v177\n"
    "v_cvt_pk_bf16_f32 v144, v144, v145\n"
    "v_mfma_f32_16x16x32_bf16 a[60:63], a[252:255], v[48:51], a[60:63]\n"
    "v_add_f32 v156, v156, v146\n"
    "s_sleep 10\n"
    "s_cbranch_scc1 L_loop%=\n"
    "v_mfma_f32_16x16x32_bf16 v[16:19], a[192:195], v[0:3], v[24:27]\n"
    "ds_read_b64_tr_b16 v[220:221], v209 offset:0\n"
    "ds_read_b64_tr_b16 v[222:223], v209 offset:512\n"
    "v_exp_f32 v178, v178\n"
    "v_mfma_f32_16x16x32_bf16 v[16:19], a[196:199], v[4:7], v[16:19]\n"
    "ds_read_b64_tr_b16 v[224:225], v209 offset:1024\n"
    "ds_read_b64_tr_b16 v[226:227], v209 offset:1536\n"
    "v_add_u32 v212, s63, v193\n"
    "v_add_u32 v213, s63, v194\n"
    "v_mfma_f32_16x16x32_bf16 v[16:19], a[200:203], v[8:11], v[16:19]\n"
    "ds_read_b64_tr_b16 v[228:229], v209 offset:2048\n"
    "ds_read_b64_tr_b16 v[230:231], v209 offset:2560\n"
    "v_exp_f32 v179, v179\n"
    "s_waitcnt lgkmcnt(6)\n"
    "v_mfma_f32_16x16x32_bf16 v[16:19], a[204:207], v[12:15], v[16:19]\n"
    "ds_read_b64_tr_b16 v[232:233], v209 offset:3072\n"
    "ds_read_b64_tr_b16 v[234:235], v209 offset:3584\n"
    "v_exp_f32 v180, v180\n"
    "v_mfma_f32_16x16x32_bf16 v[20:23], a[208:211], v[0:3], v[24:27]\n"
    "ds_read_b64_tr_b16 v[236:237], v209 offset:4096\n"
    "ds_read_b64_tr_b16 v[238:239], v209 offset:4608\n"
    "v_add_u32 v211, s64, v192\n"
    "v_add_f32 v156, v156, v147\n"
    "v_mfma_f32_16x16x32_bf16 v[20:23], a[212:215], v[4:7], v[20:23]\n"
    "v_exp_f32 v181, v181\n"
    "v_cvt_pk_bf16_f32 v145, v146, v147\n"
    "v_mfma_f32_16x16x32_bf16 v[20:23], a[216:219], v[8:11], v[20:23]\n"
    "v_exp_f32 v182, v182\n"
    "v_mfma_f32_16x16x32_bf16 v[20:23], a[220:223], v[12:15], v[20:23]\n"
    "v_exp_f32 v183, v183\n"
    "v_mfma_f32_16x16x32_bf16 v[48:51], a[192:195], v[32:35], v[56:59]\n"
    "s_waitcnt vmcnt(16) lgkmcnt(0)\n"
    "s_barrier\n"
    "v_mfma_f32_16x16x32_bf16 v[48:51], a[196:199], v[36:39], v[48:51]\n"
    "v_add_f32 v156, v156, v148\n"
    "v_add_f32 v156, v156, v149\n"
    "v_mfma_f32_16x16x32_bf16 v[48:51], a[200:203], v[40:43], v[48:51]\n"
    "s_add_u32 m0, s62, s67\n"
    "v_cvt_pk_bf16_f32 v146, v148, v149\n"
    "v_add_f32 v156, v156, v150\n"
    "v_mfma_f32_16x16x32_bf16 v[48:51], a[204:207], v[44:47], v[48:51]\n"
    "buffer_load_dwordx4 v203, s[40:43], s57 offen nt lds\n"
    "v_add_f32 v156, v156, v151\n"
    "v_cvt_pk_bf16_f32 v147, v150, v151\n"
    "v_mfma_f32_16x16x32_bf16 v[52:55], a[208:211], v[32:35], v[56:59]\n"
    "v_add_f32 v188, v188, v176\n"
    "v_add_f32 v188, v188, v177\n"
    "v_mfma_f32_16x16x32_bf16 v[52:55], a[212:215], v[36:39], v[52:55]\n"
    "buffer_store_dwordx4 v[220:223], v242, s[88:91], s74 offen sc1\n"
    "v_cvt_pk_bf16_f32 v176, v176, v177\n"
    "v_add_f32 v188, v188, v178\n"
    "v_mfma_f32_16x16x32_bf16 v[52:55], a[216:219], v[40:43], v[52:55]\n"
    "v_add_f32 v188, v188, v179\n"
    "v_cvt_pk_bf16_f32 v177, v178, v179\n"
    "v_mfma_f32_16x16x32_bf16 v[52:55], a[220:223], v[44:47], v[52:55]\n"
    "v_add_f32 v188, v188, v180\n"
    "v_add_f32 v188, v188, v181\n"
    "v_mfma_f32_16x16x32_bf16 a[64:67], a[224:227], v[80:83], a[64:67]\n"
    "buffer_load_dwordx4 v203, s[40:43], s57 offen offset:2048 nt lds\n"
    "v_cvt_pk_bf16_f32 v178, v180, v181\n"
    "v_mfma_f32_16x16x32_bf16 a[96:99], a[224:227], v[112:115], a[96:99]\n"
    "s_add_u32 s93, s93, 0x1000\n"
    "v_add_f32 v188, v188, v182\n"
    "v_add_f32 v188, v188, v183\n"
    "v_cvt_pk_bf16_f32 v179, v182, v183\n"
    "v_mfma_f32_16x16x32_bf16 a[68:71], a[228:231], v[80:83], a[68:71]\n"
    "buffer_store_dwordx4 v[224:227], v242, s[88:91], s74 offen offset:1024 sc1\n"
    "s_xor_b32 s57, s93, %[rot]\n"
    "v_exp_f32 v16, v16\n"
    "v_mfma_f32_16x16x32_bf16 a[100:103], a[228:231], v[112:115], a[100:103]\n"
    "v_exp_f32 v17, v17\n"
    "v_mfma_f32_16x16x32_bf16 a[72:75], a[232:235], v[80:83], a[72:75]\n"
    "s_add_u32 m0, s62, s68\n"
    "v_exp_f32 v18, v18\n"
    "v_mfma_f32_16x16x32_bf16 a[104:107], a[232:235], v[112:115], a[104:107]\n"
    "buffer_load_dwordx4 v203, s[40:43], s57 offen nt lds\n"
    "v_add_f32 v28, v28, v16\n"
    "v_mfma_f32_16x16x32_bf16 a[76:79], a[236:239], v[80:83], a[76:79]\n"
    "v_exp_f32 v19, v19\n"
    "v_mfma_f32_16x16x32_bf16 a[108:111], a[236:239], v[112:115], a[108:111]\n"
    "buffer_store_dwordx4 v[228:231], v242, s[88:91], s74 offen offset:2048 sc1\n"
    "v_exp_f32 v20, v20\n"
    "v_mfma_f32_16x16x32_bf16 a[80:83], a[240:243], v[80:83], a[80:83]\n"
    "v_exp_f32 v21, v21\n"
    "v_add_f32 v28, v28, v17\n"
    "v_mfma_f32_16x16x32_bf16 a[112:115], a[240:243], v[112:115], a[112:115]\n"
    "v_exp_f32 v22, v22\n"
    "v_mfma_f32_16x16x32_bf16 a[84:87], a[244:247], v[80:83], a[84:87]\n"
    "buffer_load_dwordx4 v203, s[40:43], s57 offen offset:2048 nt lds\n"
    "v_cvt_pk_bf16_f32 v16, v16, v17\n"
    "v_mfma_f32_16x16x32_bf16 a[116:119], a[244:247], v[112:115], a[116:119]\n"
    "s_add_u32 s93, s93, 0x1000\n"
    "v_exp_f32 v23, v23\n"
    "v_mfma_f32_16x16x32_bf16 a[88:91], a[248:251], v[80:83], a[88:91]\n"
    "buffer_store_dwordx4 v[232:235], v242, s[88:91], s74 offen offset:3072 sc1\n"
    "s_xor_b32 s57, s93, %[rot]\n"
    "v_exp_f32 v48, v48\n"
    "v_mfma_f32_16x16x32_bf16 a[120:123], a[248:251], v[112:115], a[120:123]\n"
    "v_exp_f32 v49, v49\n"
    "v_mfma_f32_16x16x32_bf16 a[92:95], a[252:255], v[80:83], a[92:95]\n"
    "s_add_u32 m0, s65, 32768\n"
    "v_exp_f32 v50, v50\n"
    "v_add_f32 v28, v28, v18\n"
    "v_mfma_f32_16x16x32_bf16 a[124:127], a[252:255], v[112:115], a[124:127]\n"
    "buffer_load_dwordx4 v204, s[44:47], s58 offen lds\n"
    "v_add_f32 v28, v28, v19\n"
    "v_mfma_f32_16x16x32_bf16 v[80:83], a[192:195], v[64:67], v[88:91]\n"
    "s_add_u32 s60, s74, 0x1000\n"
    "s_add_u32 s58, s58, 0x10000\n"
    "v_exp_f32 v51, v51\n"
    "v_mfma_f32_16x16x32_bf16 v[80:83], a[196:199], v[68:71], v[80:83]\n"
    "buffer_store_dwordx4 v[236:239], v242, s[88:91], s60 offen sc1\n"
    "v_exp_f32 v52, v52\n"
    "v_mfma_f32_16x16x32_bf16 v[80:83], a[200:203], v[72:75], v[80:83]\n"
    "v_exp_f32 v53, v53\n"
    "v_mfma_f32_16x16x32_bf16 v[80:83], a[204:207], v[76:79], v[80:83]\n"
    "s_add_u32 m0, s65, 34816\n"
    "v_exp_f32 v54, v54\n"
    "v_mfma_f32_16x16x32_bf16 v[84:87], a[208:211], v[64:67], v[88:91]\n"
    "buffer_load_dwordx4 v204, s[44:47], s58 offen lds\n"
    "v_cvt_pk_bf16_f32 v17, v18, v19\n"
    "v_mfma_f32_16x16x32_bf16 v[84:87], a[212:215], v[68:71], v[84:87]\n"
    "s_add_u32 s58, s58, 0x10000\n"
    "v_exp_f32 v55, v55\n"
    "v_add_f32 v28, v28, v20\n"
    "v_mfma_f32_16x16x32_bf16 v[84:87], a[216:219], v[72:75], v[84:87]\n"
    "v_add_f32 v28, v28, v21\n"
    "v_cvt_pk_bf16_f32 v18, v20, v21\n"
    "v_mfma_f32_16x16x32_bf16 v[84:87], a[220:223], v[76:79], v[84:87]\n"
    "v_add_f32 v28, v28, v22\n"
    "v_add_f32 v28, v28, v23\n"
    "v_mfma_f32_16x16x32_bf16 v[112:115], a[192:195], v[96:99], v[120:123]\n"
    "s_add_u32 m0, s65, 36864\n"
    "v_cvt_pk_bf16_f32 v19, v22, v23\n"
    "v_add_f32 v60, v60, v48\n"
    "v_mfma_f32_16x16x32_bf16 v[112:115], a[196:199], v[100:103], v[112:115]\n"
    "buffer_load_dwordx4 v205, s[44:47], s58 offen lds\n"
    "v_add_f32 v60, v60, v49\n"
    "v_mfma_f32_16x16x32_bf16 v[112:115], a[200:203], v[104:107], v[112:115]\n"
    "s_add_u32 s58, s58, 0x10000\n"
    "v_cvt_pk_bf16_f32 v48, v48, v49\n"
    "v_add_f32 v60, v60, v50\n"
    "v_mfma_f32_16x16x32_bf16 v[112:115], a[204:207], v[108:111], v[112:115]\n"
    "v_add_f32 v60, v60, v51\n"
    "v_cvt_pk_bf16_f32 v49, v50, v51\n"
    "v_add_f32 v60, v60, v52\n"
    "v_mfma_f32_16x16x32_bf16 v[116:119], a[208:211], v[96:99], v[120:123]\n"
    "v_exp_f32 v80, v80\n"
    "v_mfma_f32_16x16x32_bf16 v[116:119], a[212:215], v[100:103], v[116:119]\n"
    "s_add_u32 m0, s65, 38912\n"
    "v_exp_f32 v81, v81\n"
    "v_mfma_f32_16x16x32_bf16 v[116:119], a[216:219], v[104:107], v[116:119]\n"
    "buffer_load_dwordx4 v205, s[44:47], s58 offen lds\n"
    "v_add_f32 v60, v60, v53\n"
    "v_mfma_f32_16x16x32_bf16 v[116:119], a[220:223], v[108:111], v[116:119]\n"
    "s_add_u32 s58, s58, 0x10000\n"
    "v_exp_f32 v82, v82\n"
    "v_mfma_f32_16x16x32_bf16 a[128:131], a[224:227], v[144:147], a[128:131]\n"
    "v_exp_f32 v83, v83\n"
    "v_mfma_f32_16x16x32_bf16 a[160:163], a[224:227], v[176:179], a[160:163]\n"
    "ds_read_b64_tr_b16 a[224:225], v212 offset:0\n"
    "v_cvt_pk_bf16_f32 v50, v52, v53\n"
    "v_add_f32 v60, v60, v54\n"
    "v_mfma_f32_16x16x32_bf16 a[132:135], a[228:231], v[144:147], a[132:135]\n"
    "s_mov_b32 s62, s63\n"
    "v_exp_f32 v84, v84\n"
    "v_mfma_f32_16x16x32_bf16 a[164:167], a[228:231], v[176:179], a[164:167]\n"
    "ds_read_b64_tr_b16 a[226:227], v212 offset:4096\n"
    "v_add_f32 v60, v60, v55\n"
    "v_mfma_f32_16x16x32_bf16 a[136:139], a[232:235], v[144:147], a[136:139]\n"
    "s_mov_b32 s63, s64\n"
    "v_exp_f32 v85, v85\n"
    "v_mfma_f32_16x16x32_bf16 a[168:171], a[232:235], v[176:179], a[168:171]\n"
    "ds_read_b64_tr_b16 a[228:229], v213 offset:0\n"
    "v_cvt_pk_bf16_f32 v51, v54, v55\n"
    "v_mfma_f32_16x16x32_bf16 a[140:143], a[236:239], v[144:147], a[140:143]\n"
    "s_add_u32 s64, s64, 0x4000\n"
    "v_exp_f32 v86, v86\n"
    "v_mfma_f32_16x16x32_bf16 a[172:175], a[236:239], v[176:179], a[172:175]\n"
    "ds_read_b64_tr_b16 a[230:231], v213 offset:4096\n"
    "v_add_f32 v92, v92, v80\n"
    "v_mfma_f32_16x16x32_bf16 a[144:147], a[240:243], v[144:147], a[144:147]\n"
    "s_and_b32 s64, s64, 0xffff\n"
    "v_exp_f32 v87, v87\n"
    "v_add_f32 v92, v92, v81\n"
    "v_mfma_f32_16x16x32_bf16 a[176:179], a[240:243], v[176:179], a[176:179]\n"
    "ds_read_b64_tr_b16 a[232:233], v212 offset:512\n"
    "v_cvt_pk_bf16_f32 v80, v80, v81\n"
    "v_mfma_f32_16x16x32_bf16 a[148:151], a[244:247], v[144:147], a[148:151]\n"
    "v_exp_f32 v112, v112\n"
    "v_mfma_f32_16x16x32_bf16 a[180:183], a[244:247], v[176:179], a[180:183]\n"
    "ds_read_b64_tr_b16 a[234:235], v212 offset:4608\n"
    "v_add_f32 v92, v92, v82\n"
    "v_mfma_f32_16x16x32_bf16 a[152:155], a[248:251], v[144:147], a[152:155]\n"
    "v_exp_f32 v113, v113\n"
    "v_mfma_f32_16x16x32_bf16 a[184:187], a[248:251], v[176:179], a[184:187]\n"
    "ds_read_b64_tr_b16 a[236:237], v213 offset:512\n"
    "v_add_f32 v92, v92, v83\n"
    "v_mfma_f32_16x16x32_bf16 a[156:159], a[252:255], v[144:147], a[156:159]\n"
    "s_add_u32 s74, s74, 147456\n"
    "v_exp_f32 v114, v114\n"
    "v_cvt_pk_bf16_f32 v81, v82, v83\n"
    "v_mfma_f32_16x16x32_bf16 a[188:191], a[252:255], v[176:179], a[188:191]\n"
    "ds_read_b64_tr_b16 a[238:239], v213 offset:4608\n"
    "v_add_f32 v92, v92, v84\n"
    "v_mfma_f32_16x16x32_bf16 v[144:147], a[192:195], v[128:131], v[152:155]\n"
    "v_exp_f32 v115, v115\n"
    "v_mfma_f32_16x16x32_bf16 v[144:147], a[196:199], v[132:135], v[144:147]\n"
    "ds_read_b64_tr_b16 a[240:241], v212 offset:1024\n"
    "v_add_f32 v92, v92, v85\n"
    "v_mfma_f32_16x16x32_bf16 v[144:147], a[200:203], v[136:139], v[144:147]\n"
    "v_exp_f32 v116, v116\n"
    "v_mfma_f32_16x16x32_bf16 v[144:147], a[204:207], v[140:143], v[144:147]\n"
    "ds_read_b64_tr_b16 a[242:243], v212 offset:5120\n"
    "v_cvt_pk_bf16_f32 v82, v84, v85\n"
    "v_mfma_f32_16x16x32_bf16 v[148:151], a[208:211], v[128:131], v[152:155]\n"
    "v_exp_f32 v117, v117\n"
    "v_mfma_f32_16x16x32_bf16 v[148:151], a[212:215], v[132:135], v[148:151]\n"
    "ds_read_b64_tr_b16 a[244:245], v213 offset:1024\n"
    "v_add_f32 v92, v92, v86\n"
    "v_add_f32 v92, v92, v87\n"
    "v_mfma_f32_16x16x32_bf16 v[148:151], a[216:219], v[136:139], v[148:151]\n"
    "v_exp_f32 v118, v118\n"
    "v_mfma_f32_16x16x32_bf16 v[148:151], a[220:223], v[140:143], v[148:151]\n"
    "ds_read_b64_tr_b16 a[246:247], v213 offset:5120\n"
    "v_cvt_pk_bf16_f32 v83, v86, v87\n"
    "v_mfma_f32_16x16x32_bf16 v[176:179], a[192:195], v[160:163], v[184:187]\n"
    "v_exp_f32 v119, v119\n"
    "v_mfma_f32_16x16x32_bf16 v[176:179], a[196:199], v[164:167], v[176:179]\n"
    "ds_read_b64_tr_b16 a[248:249], v212 offset:1536\n"
    "v_add_f32 v124, v124, v112\n"
    "v_mfma_f32_16x16x32_bf16 v[176:179], a[200:203], v[168:171], v[176:179]\n"
    "ds_read_b128 a[192:195], v211 offset:0\n"
    "v_add_f32 v124, v124, v113\n"
    "v_mfma_f32_16x16x32_bf16 v[176:179], a[204:207], v[172:175], v[176:179]\n"
    "ds_read_b64_tr_b16 a[250:251], v212 offset:5632\n"
    "v_cvt_pk_bf16_f32 v112, v112, v113\n"
    "v_add_f32 v124, v124, v114\n"
    "v_mfma_f32_16x16x32_bf16 v[180:183], a[208:211], v[160:163], v[184:187]\n"
    "ds_read_b128 a[196:199], v211 offset:512\n"
    "v_add_f32 v124, v124, v115\n"
    "v_mfma_f32_16x16x32_bf16 v[180:183], a[212:215], v[164:167], v[180:183]\n"
    "ds_read_b64_tr_b16 a[252:253], v213 offset:1536\n"
    "v_cvt_pk_bf16_f32 v113, v114, v115\n"
    "v_mfma_f32_16x16x32_bf16 v[180:183], a[216:219], v[168:171], v[180:183]\n"
    "ds_read_b128 a[200:203], v211 offset:1024\n"
    "s_add_u32 m0, s69, 139264\n"
    "v_add_f32 v124, v124, v116\n"
    "v_mfma_f32_16x16x32_bf16 v[180:183], a[220:223], v[172:175], v[180:183]\n"
    "ds_read_b64_tr_b16 a[254:255], v213 offset:5632\n"
    "s_add_u32 s59, s61, 0\n"
    "s_waitcnt lgkmcnt(7)\n"
    "v_mfma_f32_16x16x32_bf16 a[0:3], a[224:227], v[16:19], a[0:3]\n"
    "buffer_load_dwordx4 v206, s[48:51], s59 offen lds\n"
    "v_exp_f32 v144, v144\n"
    "v_mfma_f32_16x16x32_bf16 a[32:35], a[224:227], v[48:51], a[32:35]\n"
    "s_add_u32 m0, s69, 140288\n"
    "v_exp_f32 v145, v145\n"
    "v_add_f32 v124, v124, v117\n"
    "v_mfma_f32_16x16x32_bf16 a[4:7], a[228:231], v[16:19], a[4:7]\n"
    "s_add_u32 s59, s61, 9216\n"
    "v_exp_f32 v146, v146\n"
    "v_mfma_f32_16x16x32_bf16 a[36:39], a[228:231], v[48:51], a[36:39]\n"
    "buffer_load_dwordx4 v207, s[48:51], s59 offen lds\n"
    "v_exp_f32 v147, v147\n"
    "v_mfma_f32_16x16x32_bf16 a[8:11], a[232:235], v[16:19], a[8:11]\n"
    "v_exp_f32 v148, v148\n"
    "v_mfma_f32_16x16x32_bf16 a[40:43], a[232:235], v[48:51], a[40:43]\n"
    "v_exp_f32 v149, v149\n"
    "v_mfma_f32_16x16x32_bf16 a[12:15], a[236:239], v[16:19], a[12:15]\n"
    "ds_read_b128 a[204:207], v211 offset:1536\n"
    "v_cvt_pk_bf16_f32 v114, v116, v117\n"
    "v_mfma_f32_16x16x32_bf16 a[44:47], a[236:239], v[48:51], a[44:47]\n"
    "ds_read_b128 a[208:211], v211 offset:4096\n"
    "v_add_f32 v124, v124, v118\n"
    "v_mfma_f32_16x16x32_bf16 a[16:19], a[240:243], v[16:19], a[16:19]\n"
    "ds_read_b128 a[212:215], v211 offset:4608\n"
    "v_add_f32 v124, v124, v119\n"
    "v_cvt_pk_bf16_f32 v115, v118, v119\n"
    "v_mfma_f32_16x16x32_bf16 a[48:51], a[240:243], v[48:51], a[48:51]\n"
    "ds_read_b128 a[216:219], v211 offset:5120\n"
    "v_add_f32 v156, v156, v144\n"
    "v_mfma_f32_16x16x32_bf16 a[20:23], a[244:247], v[16:19], a[20:23]\n"
    "ds_read_b128 a[220:223], v211 offset:5632\n"
    "s_add_u32 m0, s69, 141312\n"
    "v_add_f32 v156, v156, v145\n"
    "v_mfma_f32_16x16x32_bf16 a[52:55], a[244:247], v[48:51], a[52:55]\n"
    "s_add_u32 s59, s61, 18432\n"
    "v_exp_f32 v150, v150\n"
    "s_waitcnt lgkmcnt(5)\n"
    "v_mfma_f32_16x16x32_bf16 a[24:27], a[248:251], v[16:19], a[24:27]\n"
    "buffer_load_dwordx4 v206, s[48:51], s59 offen lds\n"
    "v_exp_f32 v151, v151\n"
    "v_mfma_f32_16x16x32_bf16 a[56:59], a[248:251], v[48:51], a[56:59]\n"
    "v_exp_f32 v176, v176\n"
    "v_mfma_f32_16x16x32_bf16 a[28:31], a[252:255], v[16:19], a[28:31]\n"
    "v_exp_f32 v177, v177\n"
    "v_cvt_pk_bf16_f32 v144, v144, v145\n"
    "v_mfma_f32_16x16x32_bf16 a[60:63], a[252:255], v[48:51], a[60:63]\n"
    "v_add_f32 v156, v156, v146\n"
    "v_mfma_f32_16x16x32_bf16 v[16:19], a[192:195], v[0:3], v[24:27]\n"
    "v_exp_f32 v178, v178\n"
    "v_mfma_f32_16x16x32_bf16 v[16:19], a[196:199], v[4:7], v[16:19]\n"
    "v_add_u32 v212, s63, v193\n"
    "v_add_u32 v213, s63, v194\n"
    "v_mfma_f32_16x16x32_bf16 v[16:19], a[200:203], v[8:11], v[16:19]\n"
    "s_waitcnt lgkmcnt(0)\n"
    "v_exp_f32 v179, v179\n"
    "v_mfma_f32_16x16x32_bf16 v[16:19], a[204:207], v[12:15], v[16:19]\n"
    "v_exp_f32 v180, v180\n"
    "v_mfma_f32_16x16x32_bf16 v[20:23], a[208:211], v[0:3], v[24:27]\n"
    "v_add_u32 v211, s64, v192\n"
    "v_add_f32 v156, v156, v147\n"
    "v_mfma_f32_16x16x32_bf16 v[20:23], a[212:215], v[4:7], v[20:23]\n"
    "global_load_dwordx2 v[246:247], v241, %[pcnt] offset:0 sc1\n"
    "v_exp_f32 v181, v181\n"
    "v_cvt_pk_bf16_f32 v145, v146, v147\n"
    "v_mfma_f32_16x16x32_bf16 v[20:23], a[216:219], v[8:11], v[20:23]\n"
    "v_exp_f32 v182, v182\n"
    "v_mfma_f32_16x16x32_bf16 v[20:23], a[220:223], v[12:15], v[20:23]\n"
    "v_exp_f32 v183, v183\n"
    "v_mfma_f32_16x16x32_bf16 v[48:51], a[192:195], v[32:35], v[56:59]\n"
    "s_waitcnt vmcnt(25) lgkmcnt(0)\n"
    "s_barrier\n"
    "v_mfma_f32_16x16x32_bf16 v[48:51], a[196:199], v[36:39], v[48:51]\n"
    "s_add_u32 m0, s69, 142336\n"
    "v_add_f32 v156, v156, v148\n"
    "v_add_f32 v156, v156, v149\n"
    "v_mfma_f32_16x16x32_bf16 v[48:51], a[200:203], v[40:43], v[48:51]\n"
    "s_add_u32 s59, s61, 27648\n"
    "v_cvt_pk_bf16_f32 v146, v148, v149\n"
    "v_add_f32 v156, v156, v150\n"
    "v_mfma_f32_16x16x32_bf16 v[48:51], a[204:207], v[44:47], v[48:51]\n"
    "buffer_load_dwordx4 v207, s[48:51], s59 offen lds\n"
    "v_add_f32 v156, v156, v151\n"
    "v_cvt_pk_bf16_f32 v147, v150, v151\n"
    "v_mfma_f32_16x16x32_bf16 v[52:55], a[208:211], v[32:35], v[56:59]\n"
    "v_add_f32 v188, v188, v176\n"
    "v_add_f32 v188, v188, v177\n"
    "v_mfma_f32_16x16x32_bf16 v[52:55], a[212:215], v[36:39], v[52:55]\n"
    "v_cvt_pk_bf16_f32 v176, v176, v177\n"
    "v_add_f32 v188, v188, v178\n"
    "v_mfma_f32_16x16x32_bf16 v[52:55], a[216:219], v[40:43], v[52:55]\n"
    "s_add_u32 m0, s69, 143360\n"
    "v_add_f32 v188, v188, v179\n"
    "v_cvt_pk_bf16_f32 v177, v178, v179\n"
    "v_mfma_f32_16x16x32_bf16 v[52:55], a[220:223], v[44:47], v[52:55]\n"
    "s_add_u32 s59, s61, 36864\n"
    "v_add_f32 v188, v188, v180\n"
    "v_add_f32 v188, v188, v181\n"
    "v_mfma_f32_16x16x32_bf16 a[64:67], a[224:227], v[80:83], a[64:67]\n"
    "buffer_load_dwordx4 v206, s[48:51], s59 offen lds\n"
    "v_cvt_pk_bf16_f32 v178, v180, v181\n"
    "v_mfma_f32_16x16x32_bf16 a[96:99], a[224:227], v[112:115], a[96:99]\n"
    "v_add_f32 v188, v188, v182\n"
    "v_add_f32 v188, v188, v183\n"
    "v_cvt_pk_bf16_f32 v179, v182, v183\n"
    "v_mfma_f32_16x16x32_bf16 a[68:71], a[228:231], v[80:83], a[68:71]\n"
    "v_exp_f32 v16, v16\n"
    "v_mfma_f32_16x16x32_bf16 a[100:103], a[228:231], v[112:115], a[100:103]\n"
    "s_add_u32 m0, s69, 144384\n"
    "v_exp_f32 v17, v17\n"
    "v_mfma_f32_16x16x32_bf16 a[72:75], a[232:235], v[80:83], a[72:75]\n"
    "s_add_u32 s59, s61, 46080\n"
    "v_exp_f32 v18, v18\n"
    "v_mfma_f32_16x16x32_bf16 a[104:107], a[232:235], v[112:115], a[104:107]\n"
    "buffer_load_dwordx4 v207, s[48:51], s59 offen lds\n"
    "v_add_f32 v28, v28, v16\n"
    "v_mfma_f32_16x16x32_bf16 a[76:79], a[236:239], v[80:83], a[76:79]\n"
    "v_exp_f32 v19, v19\n"
    "v_mfma_f32_16x16x32_bf16 a[108:111], a[236:239], v[112:115], a[108:111]\n"
    "v_exp_f32 v20, v20\n"
    "v_mfma_f32_16x16x32_bf16 a[80:83], a[240:243], v[80:83], a[80:83]\n"
    "v_exp_f32 v21, v21\n"
    "v_add_f32 v28, v28, v17\n"
    "v_mfma_f32_16x16x32_bf16 a[112:115], a[240:243], v[112:115], a[112:115]\n"
    "s_add_u32 m0, s65, 49152\n"
    "v_exp_f32 v22, v22\n"
    "v_mfma_f32_16x16x32_bf16 a[84:87], a[244:247], v[80:83], a[84:87]\n"
    "buffer_load_dwordx4 v204, s[44:47], s58 offen lds\n"
    "v_cvt_pk_bf16_f32 v16, v16, v17\n"
    "v_mfma_f32_16x16x32_bf16 a[116:119], a[244:247], v[112:115], a[116:119]\n"
    "s_add_u32 s58, s58, 0x10000\n"
    "v_exp_f32 v23, v23\n"
    "v_mfma_f32_16x16x32_bf16 a[88:91], a[248:251], v[80:83], a[88:91]\n"
    "v_exp_f32 v48, v48\n"
    "v_mfma_f32_16x16x32_bf16 a[120:123], a[248:251], v[112:115], a[120:123]\n"
    "v_exp_f32 v49, v49\n"
    "v_mfma_f32_16x16x32_bf16 a[92:95], a[252:255], v[80:83], a[92:95]\n"
    "s_add_u32 m0, s65, 51200\n"
    "v_exp_f32 v50, v50\n"
    "v_add_f32 v28, v28, v18\n"
    "v_mfma_f32_16x16x32_bf16 a[124:127], a[252:255], v[112:115], a[124:127]\n"
    "buffer_load_dwordx4 v204, s[44:47], s58 offen lds\n"
    "v_add_f32 v28, v28, v19\n"
    "v_mfma_f32_16x16x32_bf16 v[80:83], a[192:195], v[64:67], v[88:91]\n"
    "s_add_u32 s58, s58, 0x10000\n"
    "v_exp_f32 v51, v51\n"
    "v_mfma_f32_16x16x32_bf16 v[80:83], a[196:199], v[68:71], v[80:83]\n"
    "v_exp_f32 v52, v52\n"
    "v_mfma_f32_16x16x32_bf16 v[80:83], a[200:203], v[72:75], v[80:83]\n"
    "v_exp_f32 v53, v53\n"
    "v_mfma_f32_16x16x32_bf16 v[80:83], a[204:207], v[76:79], v[80:83]\n"
    "s_add_u32 m0, s65, 53248\n"
    "v_exp_f32 v54, v54\n"
    "v_mfma_f32_16x16x32_bf16 v[84:87], a[208:211], v[64:67], v[88:91]\n"
    "buffer_load_dwordx4 v205, s[44:47], s58 offen lds\n"
    "v_cvt_pk_bf16_f32 v17, v18, v19\n"
    "v_mfma_f32_16x16x32_bf16 v[84:87], a[212:215], v[68:71], v[84:87]\n"
    "s_add_u32 s58, s58, 0x10000\n"
    "v_exp_f32 v55, v55\n"
    "v_add_f32 v28, v28, v20\n"
    "v_mfma_f32_16x16x32_bf16 v[84:87], a[216:219], v[72:75], v[84:87]\n"
    "v_add_f32 v28, v28, v21\n"
    "v_cvt_pk_bf16_f32 v18, v20, v21\n"
    "v_mfma_f32_16x16x32_bf16 v[84:87], a[220:223], v[76:79], v[84:87]\n"
    "v_add_f32 v28, v28, v22\n"
    "v_add_f32 v28, v28, v23\n"
    "v_mfma_f32_16x16x32_bf16 v[112:115], a[192:195], v[96:99], v[120:123]\n"
    "s_add_u32 m0, s65, 55296\n"
    "v_cvt_pk_bf16_f32 v19, v22, v23\n"
    "v_add_f32 v60, v60, v48\n"
    "v_mfma_f32_16x16x32_bf16 v[112:115], a[196:199], v[100:103], v[112:115]\n"
    "buffer_load_dwordx4 v205, s[44:47], s58 offen lds\n"
    "v_add_f32 v60, v60, v49\n"
    "v_mfma_f32_16x16x32_bf16 v[112:115], a[200:203], v[104:107], v[112:115]\n"
    "s_add_u32 s58, s58, 0x10000\n"
    "v_cvt_pk_bf16_f32 v48, v48, v49\n"
    "v_add_f32 v60, v60, v50\n"
    "v_mfma_f32_16x16x32_bf16 v[112:115], a[204:207], v[108:111], v[112:115]\n"
    "v_add_f32 v60, v60, v51\n"
    "v_cvt_pk_bf16_f32 v49, v50, v51\n"
    "v_add_f32 v60, v60, v52\n"
    "v_mfma_f32_16x16x32_bf16 v[116:119], a[208:211], v[96:99], v[120:123]\n"
    "v_exp_f32 v80, v80\n"
    "v_mfma_f32_16x16x32_bf16 v[116:119], a[212:215], v[100:103], v[116:119]\n"
    "v_exp_f32 v81, v81\n"
    "v_mfma_f32_16x16x32_bf16 v[116:119], a[216:219], v[104:107], v[116:119]\n"
    "v_add_f32 v60, v60, v53\n"
    "v_mfma_f32_16x16x32_bf16 v[116:119], a[220:223], v[108:111], v[116:119]\n"
    "v_exp_f32 v82, v82\n"
    "v_mfma_f32_16x16x32_bf16 a[128:131], a[224:227], v[144:147], a[128:131]\n"
    "v_exp_f32 v83, v83\n"
    "v_mfma_f32_16x16x32_bf16 a[160:163], a[224:227], v[176:179], a[160:163]\n"
    "ds_read_b64_tr_b16 a[224:225], v212 offset:0\n"
    "v_cvt_pk_bf16_f32 v50, v52, v53\n"
    "v_add_f32 v60, v60, v54\n"
    "v_mfma_f32_16x16x32_bf16 a[132:135], a[228:231], v[144:147], a[132:135]\n"
    "s_mov_b32 s62, s63\n"
    "v_exp_f32 v84, v84\n"
    "v_mfma_f32_16x16x32_bf16 a[164:167], a[228:231], v[176:179], a[164:167]\n"
    "ds_read_b64_tr_b16 a[226:227], v212 offset:4096\n"
    "v_add_f32 v60, v60, v55\n"
    "v_mfma_f32_16x16x32_bf16 a[136:139], a[232:235], v[144:147], a[136:139]\n"
    "s_mov_b32 s63, s64\n"
    "v_exp_f32 v85, v85\n"
    "v_mfma_f32_16x16x32_bf16 a[168:171], a[232:235], v[176:179], a[168:171]\n"
    "ds_read_b64_tr_b16 a[228:229], v213 offset:0\n"
    "v_cvt_pk_bf16_f32 v51, v54, v55\n"
    "v_mfma_f32_16x16x32_bf16 a[140:143], a[236:239], v[144:147], a[140:143]\n"
    "s_add_u32 s64, s64, 0x4000\n"
    "v_exp_f32 v86, v86\n"
    "v_mfma_f32_16x16x32_bf16 a[172:175], a[236:239], v[176:179], a[172:175]\n"
    "ds_read_b64_tr_b16 a[230:231], v213 offset:4096\n"
    "v_add_f32 v92, v92, v80\n"
    "v_mfma_f32_16x16x32_bf16 a[144:147], a[240:243], v[144:147], a[144:147]\n"
    "s_and_b32 s64, s64, 0xffff\n"
    "v_exp_f32 v87, v87\n"
    "v_add_f32 v92, v92, v81\n"
    "v_mfma_f32_16x16x32_bf16 a[176:179], a[240:243], v[176:179], a[176:179]\n"
    "ds_read_b64_tr_b16 a[232:233], v212 offset:512\n"
    "v_cvt_pk_bf16_f32 v80, v80, v81\n"
    "v_mfma_f32_16x16x32_bf16 a[148:151], a[244:247], v[144:147], a[148:151]\n"
    "v_exp_f32 v112, v112\n"
    "v_mfma_f32_16x16x32_bf16 a[180:183], a[244:247], v[176:179], a[180:183]\n"
    "ds_read_b64_tr_b16 a[234:235], v212 offset:4608\n"
    "v_add_f32 v92, v92, v82\n"
    "v_mfma_f32_16x16x32_bf16 a[152:155], a[248:251], v[144:147], a[152:155]\n"
    "v_exp_f32 v113, v113\n"
    "v_mfma_f32_16x16x32_bf16 a[184:187], a[248:251], v[176:179], a[184:187]\n"
    "ds_read_b64_tr_b16 a[236:237], v213 offset:512\n"
    "v_add_f32 v92, v92, v83\n"
    "v_mfma_f32_16x16x32_bf16 a[156:159], a[252:255], v[144:147], a[156:159]\n"
    "v_exp_f32 v114, v114\n"
    "v_cvt_pk_bf16_f32 v81, v82, v83\n"
    "v_mfma_f32_16x16x32_bf16 a[188:191], a[252:255], v[176:179], a[188:191]\n"
    "ds_read_b64_tr_b16 a[238:239], v213 offset:4608\n"
    "v_add_f32 v92, v92, v84\n"
    "v_mfma_f32_16x16x32_bf16 v[144:147], a[192:195], v[128:131], v[152:155]\n"
    "v_exp_f32 v115, v115\n"
    "v_mfma_f32_16x16x32_bf16 v[144:147], a[196:199], v[132:135], v[144:147]\n"
    "ds_read_b64_tr_b16 a[240:241], v212 offset:1024\n"
    "v_add_f32 v92, v92, v85\n"
    "v_mfma_f32_16x16x32_bf16 v[144:147], a[200:203], v[136:139], v[144:147]\n"
    "v_exp_f32 v116, v116\n"
    "v_mfma_f32_16x16x32_bf16 v[144:147], a[204:207], v[140:143], v[144:147]\n"
    "ds_read_b64_tr_b16 a[242:243], v212 offset:5120\n"
    "v_cvt_pk_bf16_f32 v82, v84, v85\n"
    "v_mfma_f32_16x16x32_bf16 v[148:151], a[208:211], v[128:131], v[152:155]\n"
    "v_exp_f32 v117, v117\n"
    "v_mfma_f32_16x16x32_bf16 v[148:151], a[212:215], v[132:135], v[148:151]\n"
    "ds_read_b64_tr_b16 a[244:245], v213 offset:1024\n"
    "v_add_f32 v92, v92, v86\n"
    "v_add_f32 v92, v92, v87\n"
    "v_mfma_f32_16x16x32_bf16 v[148:151], a[216:219], v[136:139], v[148:151]\n"
    "v_exp_f32 v118, v118\n"
    "v_mfma_f32_16x16x32_bf16 v[148:151], a[220:223], v[140:143], v[148:151]\n"
    "ds_read_b64_tr_b16 a[246:247], v213 offset:5120\n"
    "v_cvt_pk_bf16_f32 v83, v86, v87\n"
    "v_mfma_f32_16x16x32_bf16 v[176:179], a[192:195], v[160:163], v[184:187]\n"
    "v_exp_f32 v119, v119\n"
    "v_mfma_f32_16x16x32_bf16 v[176:179], a[196:199], v[164:167], v[176:179]\n"
    "ds_read_b64_tr_b16 a[248:249], v212 offset:1536\n"
    "v_add_f32 v124, v124, v112\n"
    "v_mfma_f32_16x16x32_bf16 v[176:179], a[200:203], v[168:171], v[176:179]\n"
    "v_add_f32 v124, v124, v113\n"
    "v_mfma_f32_16x16x32_bf16 v[176:179], a[204:207], v[172:175], v[176:179]\n"
    "ds_read_b64_tr_b16 a[250:251], v212 offset:5632\n"
    "v_cvt_pk_bf16_f32 v112, v112, v113\n"
    "v_add_f32 v124, v124, v114\n"
    "v_mfma_f32_16x16x32_bf16 v[180:183], a[208:211], v[160:163], v[184:187]\n"
    "v_add_f32 v124, v124, v115\n"
    "v_mfma_f32_16x16x32_bf16 v[180:183], a[212:215], v[164:167], v[180:183]\n"
    "ds_read_b64_tr_b16 a[252:253], v213 offset:1536\n"
    "v_cvt_pk_bf16_f32 v113, v114, v115\n"
    "v_mfma_f32_16x16x32_bf16 v[180:183], a[216:219], v[168:171], v[180:183]\n"
    "v_add_f32 v124, v124, v116\n"
    "v_mfma_f32_16x16x32_bf16 v[180:183], a[220:223], v[172:175], v[180:183]\n"
    "ds_read_b64_tr_b16 a[254:255], v213 offset:5632\n"
    "s_waitcnt lgkmcnt(4)\n"
    "v_mfma_f32_16x16x32_bf16 a[0:3], a[224:227], v[16:19], a[0:3]\n"
    "v_exp_f32 v144, v144\n"
    "v_mfma_f32_16x16x32_bf16 a[32:35], a[224:227], v[48:51], a[32:35]\n"
    "v_exp_f32 v145, v145\n"
    "v_add_f32 v124, v124, v117\n"
    "v_mfma_f32_16x16x32_bf16 a[4:7], a[228:231], v[16:19], a[4:7]\n"
    "v_exp_f32 v146, v146\n"
    "v_mfma_f32_16x16x32_bf16 a[36:39], a[228:231], v[48:51], a[36:39]\n"
    "v_exp_f32 v147, v147\n"
    "v_mfma_f32_16x16x32_bf16 a[8:11], a[232:235], v[16:19], a[8:11]\n"
    "v_exp_f32 v148, v148\n"
    "v_mfma_f32_16x16x32_bf16 a[40:43], a[232:235], v[48:51], a[40:43]\n"
    "v_exp_f32 v149, v149\n"
    "v_mfma_f32_16x16x32_bf16 a[12:15], a[236:239], v[16:19], a[12:15]\n"
    "v_cvt_pk_bf16_f32 v114, v116, v117\n"
    "v_mfma_f32_16x16x32_bf16 a[44:47], a[236:239], v[48:51], a[44:47]\n"
    "v_add_f32 v124, v124, v118\n"
    "v_mfma_f32_16x16x32_bf16 a[16:19], a[240:243], v[16:19], a[16:19]\n"
    "v_add_f32 v124, v124, v119\n"
    "v_cvt_pk_bf16_f32 v115, v118, v119\n"
    "v_mfma_f32_16x16x32_bf16 a[48:51], a[240:243], v[48:51], a[48:51]\n"
    "v_add_f32 v156, v156, v144\n"
    "v_mfma_f32_16x16x32_bf16 a[20:23], a[244:247], v[16:19], a[20:23]\n"
    "v_add_f32 v156, v156, v145\n"
    "v_mfma_f32_16x16x32_bf16 a[52:55], a[244:247], v[48:51], a[52:55]\n"
    "s_waitcnt lgkmcnt(0)\n"
    "v_exp_f32 v150, v150\n"
    "v_mfma_f32_16x16x32_bf16 a[24:27], a[248:251], v[16:19], a[24:27]\n"
    "v_exp_f32 v151, v151\n"
    "v_mfma_f32_16x16x32_bf16 a[56:59], a[248:251], v[48:51], a[56:59]\n"
    "v_exp_f32 v176, v176\n"
    "v_mfma_f32_16x16x32_bf16 a[28:31], a[252:255], v[16:19], a[28:31]\n"
    "v_exp_f32 v177, v177\n"
    "v_cvt_pk_bf16_f32 v144, v144, v145\n"
    "v_mfma_f32_16x16x32_bf16 a[60:63], a[252:255], v[48:51], a[60:63]\n"
    "v_add_f32 v156, v156, v146\n"
    "v_mfma_f32_16x16x32_bf16 a[64:67], a[224:227], v[80:83], a[64:67]\n"
    "v_exp_f32 v178, v178\n"
    "v_exp_f32 v179, v179\n"
    "v_mfma_f32_16x16x32_bf16 a[96:99], a[224:227], v[112:115], a[96:99]\n"
    "v_exp_f32 v180, v180\n"
    "v_mfma_f32_16x16x32_bf16 a[68:71], a[228:231], v[80:83], a[68:71]\n"
    "v_add_f32 v156, v156, v147\n"
    "v_exp_f32 v181, v181\n"
    "v_mfma_f32_16x16x32_bf16 a[100:103], a[228:231], v[112:115], a[100:103]\n"
    "v_cvt_pk_bf16_f32 v145, v146, v147\n"
    "v_exp_f32 v182, v182\n"
    "v_mfma_f32_16x16x32_bf16 a[72:75], a[232:235], v[80:83], a[72:75]\n"
    "v_exp_f32 v183, v183\n"
    "v_mfma_f32_16x16x32_bf16 a[104:107], a[232:235], v[112:115], a[104:107]\n"
    "v_add_f32 v156, v156, v148\n"
    "v_add_f32 v156, v156, v149\n"
    "v_mfma_f32_16x16x32_bf16 a[76:79], a[236:239], v[80:83], a[76:79]\n"
    "v_cvt_pk_bf16_f32 v146, v148, v149\n"
    "v_add_f32 v156, v156, v150\n"
    "v_mfma_f32_16x16x32_bf16 a[108:111], a[236:239], v[112:115], a[108:111]\n"
    "v_add_f32 v156, v156, v151\n"
    "v_cvt_pk_bf16_f32 v147, v150, v151\n"
    "v_mfma_f32_16x16x32_bf16 a[80:83], a[240:243], v[80:83], a[80:83]\n"
    "v_add_f32 v188, v188, v176\n"
    "v_add_f32 v188, v188, v177\n"
    "v_mfma_f32_16x16x32_bf16 a[112:115], a[240:243], v[112:115], a[112:115]\n"
    "s_waitcnt vmcnt(4) lgkmcnt(0)\n"
    "s_barrier\n"
    "v_cvt_pk_bf16_f32 v176, v176, v177\n"
    "v_add_f32 v188, v188, v178\n"
    "v_mfma_f32_16x16x32_bf16 a[84:87], a[244:247], v[80:83], a[84:87]\n"
    "ds_read_b128 a[192:195], v199 offset:24576\n"
    "v_add_f32 v188, v188, v179\n"
    "v_cvt_pk_bf16_f32 v177, v178, v179\n"
    "v_mfma_f32_16x16x32_bf16 a[116:119], a[244:247], v[112:115], a[116:119]\n"
    "v_add_f32 v188, v188, v180\n"
    "v_add_f32 v188, v188, v181\n"
    "v_mfma_f32_16x16x32_bf16 a[88:91], a[248:251], v[80:83], a[88:91]\n"
    "ds_read_b64_tr_b16 a[216:217], v195 offset:0\n"
    "v_cvt_pk_bf16_f32 v178, v180, v181\n"
    "v_add_f32 v188, v188, v182\n"
    "v_mfma_f32_16x16x32_bf16 a[120:123], a[248:251], v[112:115], a[120:123]\n"
    "v_add_f32 v188, v188, v183\n"
    "v_mfma_f32_16x16x32_bf16 a[92:95], a[252:255], v[80:83], a[92:95]\n"
    "ds_read_b64_tr_b16 a[218:219], v195 offset:256\n"
    "v_cvt_pk_bf16_f32 v179, v182, v183\n"
    "v_mfma_f32_16x16x32_bf16 a[124:127], a[252:255], v[112:115], a[124:127]\n"
    "v_mov_b32 v29, v28\n"
    "v_mov_b32 v61, v60\n"
    "s_nop 0\n"
    "v_permlane16_swap_b32 v28, v29\n"
    "v_mfma_f32_16x16x32_bf16 a[128:131], a[224:227], v[144:147], a[128:131]\n"
    "ds_read_b128 a[196:199], v199 offset:26624\n"
    "v_permlane16_swap_b32 v60, v61\n"
    "v_add_f32 v28, v28, v29\n"
    "v_add_f32 v60, v60, v61\n"
    "v_mov_b32 v29, v28\n"
    "v_mfma_f32_16x16x32_bf16 a[160:163], a[224:227], v[176:179], a[160:163]\n"
    "v_mov_b32 v61, v60\n"
    "s_nop 0\n"
    "v_permlane32_swap_b32 v28, v29\n"
    "v_permlane32_swap_b32 v60, v61\n"
    "v_mfma_f32_16x16x32_bf16 a[132:135], a[228:231], v[144:147], a[132:135]\n"
    "ds_read_b128 a[200:203], v199 offset:28672\n"
    "v_add_f32 v28, v28, v29\n"
    "v_add_f32 v60, v60, v61\n"
    "v_cmp_nge_f32 vcc, s75, v28\n"
    "v_rcp_f32 v214, v28\n"
    "v_mfma_f32_16x16x32_bf16 a[164:167], a[228:231], v[176:179], a[164:167]\n"
    "s_or_b64 s[78:79], s[78:79], vcc\n"
    "v_cmp_nge_f32 vcc, s75, v60\n"
    "v_rcp_f32 v215, v60\n"
    "s_or_b64 s[78:79], s[78:79], vcc\n"
    "v_mfma_f32_16x16x32_bf16 a[136:139], a[232:235], v[144:147], a[136:139]\n"
    "ds_read_b128 a[204:207], v199 offset:30720\n"
    "v_mov_b32 v93, v92\n"
    "v_mov_b32 v125, v124\n"
    "s_nop 0\n"
    "v_permlane16_swap_b32 v92, v93\n"
    "v_mfma_f32_16x16x32_bf16 a[168:171], a[232:235], v[176:179], a[168:171]\n"
    "v_permlane16_swap_b32 v124, v125\n"
    "v_add_f32 v92, v92, v93\n"
    "v_add_f32 v124, v124, v125\n"
    "v_mov_b32 v93, v92\n"
    "v_mfma_f32_16x16x32_bf16 a[140:143], a[236:239], v[144:147], a[140:143]\n"
    "ds_read_b128 a[208:211], v199 offset:32768\n"
    "v_mov_b32 v125, v124\n"
    "s_nop 0\n"
    "v_permlane32_swap_b32 v92, v93\n"
    "v_permlane32_swap_b32 v124, v125\n"
    "v_mfma_f32_16x16x32_bf16 a[172:175], a[236:239], v[176:179], a[172:175]\n"
    "v_add_f32 v92, v92, v93\n"
    "v_add_f32 v124, v124, v125\n"
    "v_cmp_nge_f32 vcc, s75, v92\n"
    "v_rcp_f32 v216, v92\n"
    "v_mfma_f32_16x16x32_bf16 a[144:147], a[240:243], v[144:147], a[144:147]\n"
    "ds_read_b128 a[212:215], v199 offset:34816\n"
    "s_or_b64 s[78:79], s[78:79], vcc\n"
    "v_cmp_nge_f32 vcc, s75, v124\n"
    "v_rcp_f32 v217, v124\n"
    "v_mfma_f32_16x16x32_bf16 a[176:179], a[240:243], v[176:179], a[176:179]\n"
    "s_or_b64 s[78:79], s[78:79], vcc\n"
    "v_mov_b32 v157, v156\n"
    "v_mov_b32 v189, v188\n"
    "s_nop 0\n"
    "v_mfma_f32_16x16x32_bf16 a[148:151], a[244:247], v[144:147], a[148:151]\n"
    "ds_read_b64_tr_b16 a[220:221], v196 offset:0\n"
    "v_permlane16_swap_b32 v156, v157\n"
    "v_permlane16_swap_b32 v188, v189\n"
    "v_mfma_f32_16x16x32_bf16 a[180:183], a[244:247], v[176:179], a[180:183]\n"
    "v_add_f32 v156, v156, v157\n"
    "v_add_f32 v188, v188, v189\n"
    "v_mov_b32 v157, v156\n"
    "v_mov_b32 v189, v188\n"
    "v_mfma_f32_16x16x32_bf16 a[152:155], a[248:251], v[144:147], a[152:155]\n"
    "ds_read_b64_tr_b16 a[222:223], v196 offset:256\n"
    "s_nop 0\n"
    "v_permlane32_swap_b32 v156, v157\n"
    "v_permlane32_swap_b32 v188, v189\n"
    "v_mfma_f32_16x16x32_bf16 a[184:187], a[248:251], v[176:179], a[184:187]\n"
    "v_add_f32 v156, v156, v157\n"
    "v_add_f32 v188, v188, v189\n"
    "v_cmp_nge_f32 vcc, s75, v156\n"
    "v_rcp_f32 v218, v156\n"
    "v_mfma_f32_16x16x32_bf16 a[156:159], a[252:255], v[144:147], a[156:159]\n"
    "s_or_b64 s[78:79], s[78:79], vcc\n"
    "v_cmp_nge_f32 vcc, s75, v188\n"
    "v_rcp_f32 v219, v188\n"
    "v_mfma_f32_16x16x32_bf16 a[188:191], a[252:255], v[176:179], a[188:191]\n"
    "s_or_b64 s[78:79], s[78:79], vcc\n"
    "s_add_u32 s71, s71, 1\n"
    "s_cmp_eq_u32 s71, 2\n"
    "s_cbranch_scc1 L_final%=\n"
    "s_waitcnt lgkmcnt(2)\n"
    "v_mfma_f32_16x16x32_bf16 v[0:3], a[216:219], a[192:195], 0\n"
    "ds_read_b64_tr_b16 a[252:253], v196 offset:512\n"
    "v_mfma_f32_16x16x32_bf16 v[32:35], a[216:219], a[196:199], 0\n"
    "ds_read_b64_tr_b16 a[248:249], v195 offset:512\n"
    "v_mfma_f32_16x16x32_bf16 v[64:67], a[216:219], a[200:203], 0\n"
    "ds_read_b64_tr_b16 a[254:255], v196 offset:768\n"
    "v_mfma_f32_16x16x32_bf16 v[96:99], a[216:219], a[204:207], 0\n"
    "ds_read_b64_tr_b16 a[250:251], v195 offset:768\n"
    "v_mfma_f32_16x16x32_bf16 v[128:131], a[216:219], a[208:211], 0\n"
    "s_add_u32 m0, s65, 98304\n"
    "v_mfma_f32_16x16x32_bf16 v[160:163], a[216:219], a[212:215], 0\n"
    "buffer_load_dwordx4 v204, s[44:47], s58 offen lds\n"
    "s_waitcnt lgkmcnt(4)\n"
    "v_mfma_f32_16x16x32_bf16 v[4:7], a[220:223], a[192:195], 0\n"
    "ds_read_b64_tr_b16 a[216:217], v195 offset:1024\n"
    "s_add_u32 s58, s58, 0x10000\n"
    "s_add_u32 m0, s65, 100352\n"
    "v_mfma_f32_16x16x32_bf16 v[36:39], a[220:223], a[196:199], 0\n"
    "buffer_load_dwordx4 v204, s[44:47], s58 offen lds\n"
    "v_mfma_f32_16x16x32_bf16 v[68:71], a[220:223], a[200:203], 0\n"
    "ds_read_b64_tr_b16 a[218:219], v195 offset:1280\n"
    "s_add_u32 s58, s58, 0x10000\n"
    "v_mfma_f32_16x16x32_bf16 v[100:103], a[220:223], a[204:207], 0\n"
    "s_add_u32 m0, s65, 102400\n"
    "v_mfma_f32_16x16x32_bf16 v[132:135], a[220:223], a[208:211], 0\n"
    "buffer_load_dwordx4 v205, s[44:47], s58 offen lds\n"
    "v_mfma_f32_16x16x32_bf16 v[164:167], a[220:223], a[212:215], 0\n"
    "s_add_u32 s58, s58, 0x10000\n"
    "s_waitcnt lgkmcnt(2)\n"
    "v_mfma_f32_16x16x32_bf16 v[8:11], a[248:251], a[192:195], 0\n"
    "ds_read_b64_tr_b16 a[220:221], v196 offset:1024\n"
    "s_add_u32 m0, s65, 104448\n"
    "v_mfma_f32_16x16x32_bf16 v[40:43], a[248:251], a[196:199], 0\n"
    "buffer_load_dwordx4 v205, s[44:47], s58 offen lds\n"
    "v_mfma_f32_16x16x32_bf16 v[72:75], a[248:251], a[200:203], 0\n"
    "ds_read_b64_tr_b16 a[222:223], v196 offset:1280\n"
    "s_add_u32 s58, s58, 0x10000\n"
    "s_add_u32 m0, s69, 114688\n"
    "v_mfma_f32_16x16x32_bf16 v[104:107], a[248:251], a[204:207], 0\n"
    "s_add_u32 s59, s61, 128\n"
    "v_mfma_f32_16x16x32_bf16 v[136:139], a[248:251], a[208:211], 0\n"
    "buffer_load_dwordx4 v206, s[48:51], s59 offen lds\n"
    "v_mfma_f32_16x16x32_bf16 v[168:171], a[248:251], a[212:215], 0\n"
    "s_add_u32 m0, s69, 115712\n"
    "v_mfma_f32_16x16x32_bf16 v[12:15], a[252:255], a[192:195], 0\n"
    "ds_read_b64_tr_b16 a[248:249], v195 offset:1536\n"
    "s_add_u32 s59, s61, 9344\n"
    "v_mfma_f32_16x16x32_bf16 v[44:47], a[252:255], a[196:199], 0\n"
    "buffer_load_dwordx4 v207, s[48:51], s59 offen lds\n"
    "v_mfma_f32_16x16x32_bf16 v[76:79], a[252:255], a[200:203], 0\n"
    "ds_read_b64_tr_b16 a[250:251], v195 offset:1792\n"
    "s_add_u32 m0, s69, 116736\n"
    "v_mfma_f32_16x16x32_bf16 v[108:111], a[252:255], a[204:207], 0\n"
    "s_add_u32 s59, s61, 18560\n"
    "v_mfma_f32_16x16x32_bf16 v[140:143], a[252:255], a[208:211], 0\n"
    "buffer_load_dwordx4 v206, s[48:51], s59 offen lds\n"
    "v_mfma_f32_16x16x32_bf16 v[172:175], a[252:255], a[212:215], 0\n"
    "s_add_u32 m0, s69, 117760\n"
    "s_waitcnt lgkmcnt(2)\n"
    "v_mfma_f32_16x16x32_bf16 v[16:19], a[216:219], a[192:195], 0\n"
    "ds_read_b64_tr_b16 a[252:253], v196 offset:1536\n"
    "s_add_u32 s59, s61, 27776\n"
    "v_mfma_f32_16x16x32_bf16 v[48:51], a[216:219], a[196:199], 0\n"
    "buffer_load_dwordx4 v207, s[48:51], s59 offen lds\n"
    "v_mfma_f32_16x16x32_bf16 v[80:83], a[216:219], a[200:203], 0\n"
    "ds_read_b64_tr_b16 a[254:255], v196 offset:1792\n"
    "s_add_u32 m0, s69, 118784\n"
    "v_mfma_f32_16x16x32_bf16 v[112:115], a[216:219], a[204:207], 0\n"
    "s_add_u32 s59, s61, 36992\n"
    "v_mfma_f32_16x16x32_bf16 v[144:147], a[216:219], a[208:211], 0\n"
    "buffer_load_dwordx4 v206, s[48:51], s59 offen lds\n"
    "v_mfma_f32_16x16x32_bf16 v[176:179], a[216:219], a[212:215], 0\n"
    "s_add_u32 m0, s69, 119808\n"
    "v_mfma_f32_16x16x32_bf16 v[20:23], a[220:223], a[192:195], 0\n"
    "s_add_u32 s59, s61, 46208\n"
    "v_mfma_f32_16x16x32_bf16 v[52:55], a[220:223], a[196:199], 0\n"
    "buffer_load_dwordx4 v207, s[48:51], s59 offen lds\n"
    "v_mfma_f32_16x16x32_bf16 v[84:87], a[220:223], a[200:203], 0\n"
    "s_waitcnt vmcnt(10) lgkmcnt(0)\n"
    "s_barrier\n"
    "v_mfma_f32_16x16x32_bf16 v[116:119], a[220:223], a[204:207], 0\n"
    "ds_read_b64_tr_b16 a[216:217], v195 offset:16384\n"
    "v_mfma_f32_16x16x32_bf16 v[148:151], a[220:223], a[208:211], 0\n"
    "ds_read_b64_tr_b16 a[218:219], v195 offset:16640\n"
    "v_mfma_f32_16x16x32_bf16 v[180:183], a[220:223], a[212:215], 0\n"
    "ds_read_b128 a[224:227], v199 offset:25088\n"
    "v_mfma_f32_16x16x32_bf16 v[24:27], a[248:251], a[192:195], 0\n"
    "ds_read_b128 a[228:231], v199 offset:27136\n"
    "v_mfma_f32_16x16x32_bf16 v[56:59], a[248:251], a[196:199], 0\n"
    "ds_read_b128 a[232:235], v199 offset:29184\n"
    "v_mfma_f32_16x16x32_bf16 v[88:91], a[248:251], a[200:203], 0\n"
    "ds_read_b128 a[236:239], v199 offset:31232\n"
    "v_mfma_f32_16x16x32_bf16 v[120:123], a[248:251], a[204:207], 0\n"
    "ds_read_b128 a[240:243], v199 offset:33280\n"
    "v_mfma_f32_16x16x32_bf16 v[152:155], a[248:251], a[208:211], 0\n"
    "ds_read_b128 a[244:247], v199 offset:35328\n"
    "s_add_u32 m0, s65, 32768\n"
    "v_mfma_f32_16x16x32_bf16 v[184:187], a[248:251], a[212:215], 0\n"
    "buffer_load_dwordx4 v204, s[44:47], s58 offen lds\n"
    "v_mfma_f32_16x16x32_bf16 v[28:31], a[252:255], a[192:195], 0\n"
    "ds_read_b64_tr_b16 a[220:221], v196 offset:16384\n"
    "s_add_u32 s58, s58, 0x10000\n"
    "v_mfma_f32_16x16x32_bf16 v[60:63], a[252:255], a[196:199], 0\n"
    "ds_read_b64_tr_b16 a[222:223], v196 offset:16640\n"
    "s_add_u32 m0, s65, 34816\n"
    "v_mfma_f32_16x16x32_bf16 v[92:95], a[252:255], a[200:203], 0\n"
    "buffer_load_dwordx4 v204, s[44:47], s58 offen lds\n"
    "v_mfma_f32_16x16x32_bf16 v[124:127], a[252:255], a[204:207], 0\n"
    "ds_read_b64_tr_b16 a[248:249], v195 offset:16896\n"
    "s_add_u32 s58, s58, 0x10000\n"
    "v_mfma_f32_16x16x32_bf16 v[156:159], a[252:255], a[208:211], 0\n"
    "ds_read_b64_tr_b16 a[250:251], v195 offset:17152\n"
    "s_add_u32 m0, s65, 36864\n"
    "v_mfma_f32_16x16x32_bf16 v[188:191], a[252:255], a[212:215], 0\n"
    "buffer_load_dwordx4 v205, s[44:47], s58 offen lds\n"
    "s_waitcnt lgkmcnt(4)\n"
    "v_mfma_f32_16x16x32_bf16 v[0:3], a[216:219], a[224:227], v[0:3]\n"
    "ds_read_b64_tr_b16 a[252:253], v196 offset:16896\n"
    "s_add_u32 s58, s58, 0x10000\n"
    "v_mfma_f32_16x16x32_bf16 v[32:35], a[216:219], a[228:231], v[32:35]\n"
    "s_add_u32 s73, s72, 0\n"
    "v_accvgpr_read_b32 v228, a0\n"
    "v_accvgpr_read_b32 v229, a1\n"
    "v_mfma_f32_16x16x32_bf16 v[64:67], a[216:219], a[232:235], v[64:67]\n"
    "ds_read_b64_tr_b16 a[254:255], v196 offset:17152\n"
    "s_add_u32 m0, s65, 38912\n"
    "v_mfma_f32_16x16x32_bf16 v[96:99], a[216:219], a[236:239], v[96:99]\n"
    "buffer_load_dwordx4 v205, s[44:47], s58 offen lds\n"
    "v_accvgpr_read_b32 v230, a2\n"
    "v_mfma_f32_16x16x32_bf16 v[128:131], a[216:219], a[240:243], v[128:131]\n"
    "s_add_u32 s58, s58, 0x10000\n"
    "v_accvgpr_read_b32 v231, a3\n"
    "v_accvgpr_read_b32 v232, a4\n"
    "v_mfma_f32_16x16x32_bf16 v[160:163], a[216:219], a[244:247], v[160:163]\n"
    "v_accvgpr_read_b32 v233, a5\n"
    "v_accvgpr_read_b32 v234, a6\n"
    "s_waitcnt lgkmcnt(2)\n"
    "v_mfma_f32_16x16x32_bf16 v[4:7], a[220:223], a[224:227], v[4:7]\n"
    "ds_read_b64_tr_b16 a[216:217], v195 offset:17408\n"
    "v_accvgpr_read_b32 v235, a7\n"
    "v_mfma_f32_16x16x32_bf16 v[36:39], a[220:223], a[228:231], v[36:39]\n"
    "v_accvgpr_write_b32 a0, 0\n"
    "v_accvgpr_write_b32 a1, 0\n"
    "v_mfma_f32_16x16x32_bf16 v[68:71], a[220:223], a[232:235], v[68:71]\n"
    "ds_read_b64_tr_b16 a[218:219], v195 offset:17664\n"
    "v_accvgpr_write_b32 a2, 0\n"
    "v_mfma_f32_16x16x32_bf16 v[100:103], a[220:223], a[236:239], v[100:103]\n"
    "v_accvgpr_write_b32 a3, 0\n"
    "v_accvgpr_write_b32 a4, 0\n"
    "v_mfma_f32_16x16x32_bf16 v[132:135], a[220:223], a[240:243], v[132:135]\n"
    "v_accvgpr_write_b32 a5, 0\n"
    "v_accvgpr_write_b32 a6, 0\n"
    "v_mfma_f32_16x16x32_bf16 v[164:167], a[220:223], a[244:247], v[164:167]\n"
    "v_accvgpr_write_b32 a7, 0\n"
    "v_mul_f32 v228, v214, v228\n"
    "v_mfma_f32_16x16x32_bf16 v[8:11], a[248:251], a[224:227], v[8:11]\n"
    "ds_read_b64_tr_b16 a[220:221], v196 offset:17408\n"
    "v_mul_f32 v229, v214, v229\n"
    "v_mfma_f32_16x16x32_bf16 v[40:43], a[248:251], a[228:231], v[40:43]\n"
    "v_mul_f32 v230, v214, v230\n"
    "v_mul_f32 v231, v214, v231\n"
    "v_mfma_f32_16x16x32_bf16 v[72:75], a[248:251], a[232:235], v[72:75]\n"
    "ds_read_b64_tr_b16 a[222:223], v196 offset:17664\n"
    "v_mul_f32 v232, v214, v232\n"
    "v_mfma_f32_16x16x32_bf16 v[104:107], a[248:251], a[236:239], v[104:107]\n"
    "v_mul_f32 v233, v214, v233\n"
    "v_mul_f32 v234, v214, v234\n"
    "v_mfma_f32_16x16x32_bf16 v[136:139], a[248:251], a[240:243], v[136:139]\n"
    "v_mul_f32 v235, v214, v235\n"
    "v_cvt_pk_bf16_f32 v220, v228, v229\n"
    "v_mfma_f32_16x16x32_bf16 v[168:171], a[248:251], a[244:247], v[168:171]\n"
    "v_cvt_pk_bf16_f32 v221, v230, v231\n"
    "v_cvt_pk_bf16_f32 v222, v232, v233\n"
    "s_waitcnt lgkmcnt(2)\n"
    "v_mfma_f32_16x16x32_bf16 v[12:15], a[252:255], a[224:227], v[12:15]\n"
    "ds_read_b64_tr_b16 a[248:249], v195 offset:17920\n"
    "v_cvt_pk_bf16_f32 v223, v234, v235\n"
    "v_mfma_f32_16x16x32_bf16 v[44:47], a[252:255], a[228:231], v[44:47]\n"
    "buffer_store_dwordx4 v[220:223], v242, s[52:55], s73 offen sc1\n"
    "v_accvgpr_read_b32 v228, a32\n"
    "v_mfma_f32_16x16x32_bf16 v[76:79], a[252:255], a[232:235], v[76:79]\n"
    "ds_read_b64_tr_b16 a[250:251], v195 offset:18176\n"
    "v_accvgpr_read_b32 v229, a33\n"
    "v_mfma_f32_16x16x32_bf16 v[108:111], a[252:255], a[236:239], v[108:111]\n"
    "v_accvgpr_read_b32 v230, a34\n"
    "v_accvgpr_read_b32 v231, a35\n"
    "v_mfma_f32_16x16x32_bf16 v[140:143], a[252:255], a[240:243], v[140:143]\n"
    "v_accvgpr_read_b32 v232, a36\n"
    "v_accvgpr_read_b32 v233, a37\n"
    "v_mfma_f32_16x16x32_bf16 v[172:175], a[252:255], a[244:247], v[172:175]\n"
    "v_accvgpr_read_b32 v234, a38\n"
    "v_accvgpr_read_b32 v235, a39\n"
    "v_mfma_f32_16x16x32_bf16 v[16:19], a[216:219], a[224:227], v[16:19]\n"
    "ds_read_b64_tr_b16 a[252:253], v196 offset:17920\n"
    "v_accvgpr_write_b32 a32, 0\n"
    "v_mfma_f32_16x16x32_bf16 v[48:51], a[216:219], a[228:231], v[48:51]\n"
    "v_accvgpr_write_b32 a33, 0\n"
    "v_accvgpr_write_b32 a34, 0\n"
    "v_mfma_f32_16x16x32_bf16 v[80:83], a[216:219], a[232:235], v[80:83]\n"
    "ds_read_b64_tr_b16 a[254:255], v196 offset:18176\n"
    "v_accvgpr_write_b32 a35, 0\n"
    "v_mfma_f32_16x16x32_bf16 v[112:115], a[216:219], a[236:239], v[112:115]\n"
    "v_accvgpr_write_b32 a36, 0\n"
    "v_accvgpr_write_b32 a37, 0\n"
    "v_mfma_f32_16x16x32_bf16 v[144:147], a[216:219], a[240:243], v[144:147]\n"
    "v_accvgpr_write_b32 a38, 0\n"
    "v_accvgpr_write_b32 a39, 0\n"
    "v_mfma_f32_16x16x32_bf16 v[176:179], a[216:219], a[244:247], v[176:179]\n"
    "v_mul_f32 v228, v215, v228\n"
    "v_mul_f32 v229, v215, v229\n"
    "s_waitcnt lgkmcnt(2)\n"
    "v_mfma_f32_16x16x32_bf16 v[20:23], a[220:223], a[224:227], v[20:23]\n"
    "v_mul_f32 v230, v215, v230\n"
    "v_mul_f32 v231, v215, v231\n"
    "v_mfma_f32_16x16x32_bf16 v[52:55], a[220:223], a[228:231], v[52:55]\n"
    "v_mul_f32 v232, v215, v232\n"
    "v_mul_f32 v233, v215, v233\n"
    "v_mfma_f32_16x16x32_bf16 v[84:87], a[220:223], a[232:235], v[84:87]\n"
    "s_waitcnt vmcnt(5) lgkmcnt(0)\n"
    "s_barrier\n"
    "v_mfma_f32_16x16x32_bf16 v[116:119], a[220:223], a[236:239], v[116:119]\n"
    "ds_read_b64_tr_b16 a[216:217], v197 offset:0\n"
    "v_mul_f32 v234, v215, v234\n"
    "v_mfma_f32_16x16x32_bf16 v[148:151], a[220:223], a[240:243], v[148:151]\n"
    "ds_read_b64_tr_b16 a[218:219], v197 offset:256\n"
    "v_mul_f32 v235, v215, v235\n"
    "v_mfma_f32_16x16x32_bf16 v[180:183], a[220:223], a[244:247], v[180:183]\n"
    "ds_read_b128 a[192:195], v199 offset:0\n"
    "v_cvt_pk_bf16_f32 v224, v228, v229\n"
    "v_mfma_f32_16x16x32_bf16 v[24:27], a[248:251], a[224:227], v[24:27]\n"
    "ds_read_b128 a[196:199], v199 offset:2048\n"
    "v_cvt_pk_bf16_f32 v225, v230, v231\n"
    "v_mfma_f32_16x16x32_bf16 v[56:59], a[248:251], a[228:231], v[56:59]\n"
    "ds_read_b128 a[200:203], v199 offset:4096\n"
    "v_cvt_pk_bf16_f32 v226, v232, v233\n"
    "v_mfma_f32_16x16x32_bf16 v[88:91], a[248:251], a[232:235], v[88:91]\n"
    "ds_read_b128 a[204:207], v199 offset:6144\n"
    "v_cvt_pk_bf16_f32 v227, v234, v235\n"
    "v_mfma_f32_16x16x32_bf16 v[120:123], a[248:251], a[236:239], v[120:123]\n"
    "ds_read_b128 a[208:211], v199 offset:8192\n"
    "buffer_store_dwordx4 v[224:227], v242, s[52:55], s73 offen offset:1024 sc1\n"
    "v_mfma_f32_16x16x32_bf16 v[152:155], a[248:251], a[240:243], v[152:155]\n"
    "ds_read_b128 a[212:215], v199 offset:10240\n"
    "s_add_u32 m0, s65, 49152\n"
    "v_mfma_f32_16x16x32_bf16 v[184:187], a[248:251], a[244:247], v[184:187]\n"
    "buffer_load_dwordx4 v204, s[44:47], s58 offen lds\n"
    "v_accvgpr_read_b32 v228, a64\n"
    "v_mfma_f32_16x16x32_bf16 v[28:31], a[252:255], a[224:227], v[28:31]\n"
    "ds_read_b64_tr_b16 a[220:221], v198 offset:0\n"
    "s_add_u32 s58, s58, 0x10000\n"
    "v_mfma_f32_16x16x32_bf16 v[60:63], a[252:255], a[228:231], v[60:63]\n"
    "ds_read_b64_tr_b16 a[222:223], v198 offset:256\n"
    "s_add_u32 m0, s65, 51200\n"
    "v_mfma_f32_16x16x32_bf16 v[92:95], a[252:255], a[232:235], v[92:95]\n"
    "buffer_load_dwordx4 v204, s[44:47], s58 offen lds\n"
    "v_accvgpr_read_b32 v229, a65\n"
    "v_mfma_f32_16x16x32_bf16 v[124:127], a[252:255], a[236:239], v[124:127]\n"
    "ds_read_b64_tr_b16 a[248:249], v197 offset:512\n"
    "s_add_u32 s58, s58, 0x10000\n"
    "v_mfma_f32_16x16x32_bf16 v[156:159], a[252:255], a[240:243], v[156:159]\n"
    "ds_read_b64_tr_b16 a[250:251], v197 offset:768\n"
    "s_add_u32 m0, s65, 53248\n"
    "v_mfma_f32_16x16x32_bf16 v[188:191], a[252:255], a[244:247], v[188:191]\n"
    "buffer_load_dwordx4 v205, s[44:47], s58 offen lds\n"
    "v_accvgpr_read_b32 v230, a66\n"
    "s_waitcnt lgkmcnt(4)\n"
    "v_mfma_f32_16x16x32_bf16 v[0:3], a[216:219], a[192:195], v[0:3]\n"
    "ds_read_b64_tr_b16 a[252:253], v198 offset:512\n"
    "s_add_u32 s58, s58, 0x10000\n"
    "v_mfma_f32_16x16x32_bf16 v[32:35], a[216:219], a[196:199], v[32:35]\n"
    "v_accvgpr_read_b32 v231, a67\n"
    "v_accvgpr_read_b32 v232, a68\n"
    "v_mfma_f32_16x16x32_bf16 v[64:67], a[216:219], a[200:203], v[64:67]\n"
    "ds_read_b64_tr_b16 a[254:255], v198 offset:768\n"
    "s_add_u32 m0, s65, 55296\n"
    "v_mfma_f32_16x16x32_bf16 v[96:99], a[216:219], a[204:207], v[96:99]\n"
    "buffer_load_dwordx4 v205, s[44:47], s58 offen lds\n"
    "v_accvgpr_read_b32 v233, a69\n"
    "v_mfma_f32_16x16x32_bf16 v[128:131], a[216:219], a[208:211], v[128:131]\n"
    "s_add_u32 s58, s58, 0x10000\n"
    "v_accvgpr_read_b32 v234, a70\n"
    "v_accvgpr_read_b32 v235, a71\n"
    "v_mfma_f32_16x16x32_bf16 v[160:163], a[216:219], a[212:215], v[160:163]\n"
    "s_add_u32 m0, s69, 139264\n"
    "v_accvgpr_write_b32 a64, 0\n"
    "v_accvgpr_write_b32 a65, 0\n"
    "s_waitcnt lgkmcnt(2)\n"
    "v_mfma_f32_16x16x32_bf16 v[4:7], a[220:223], a[192:195], v[4:7]\n"
    "ds_read_b64_tr_b16 a[216:217], v197 offset:1024\n"
    "s_add_u32 s59, s61, 256\n"
    "v_mfma_f32_16x16x32_bf16 v[36:39], a[220:223], a[196:199], v[36:39]\n"
    "buffer_load_dwordx4 v206, s[48:51], s59 offen lds\n"
    "v_accvgpr_write_b32 a66, 0\n"
    "v_mfma_f32_16x16x32_bf16 v[68:71], a[220:223], a[200:203], v[68:71]\n"
    "ds_read_b64_tr_b16 a[218:219], v197 offset:1280\n"
    "v_accvgpr_write_b32 a67, 0\n"
    "v_mfma_f32_16x16x32_bf16 v[100:103], a[220:223], a[204:207], v[100:103]\n"
    "s_add_u32 m0, s69, 140288\n"
    "v_accvgpr_write_b32 a68, 0\n"
    "v_accvgpr_write_b32 a69, 0\n"
    "v_mfma_f32_16x16x32_bf16 v[132:135], a[220:223], a[208:211], v[132:135]\n"
    "s_add_u32 s59, s61, 9472\n"
    "v_accvgpr_write_b32 a70, 0\n"
    "v_accvgpr_write_b32 a71, 0\n"
    "v_mfma_f32_16x16x32_bf16 v[164:167], a[220:223], a[212:215], v[164:167]\n"
    "buffer_load_dwordx4 v207, s[48:51], s59 offen lds\n"
    "v_mul_f32 v228, v216, v228\n"
    "v_mfma_f32_16x16x32_bf16 v[8:11], a[248:251], a[192:195], v[8:11]\n"
    "ds_read_b64_tr_b16 a[220:221], v198 offset:1024\n"
    "v_mul_f32 v229, v216, v229\n"
    "v_mfma_f32_16x16x32_bf16 v[40:43], a[248:251], a[196:199], v[40:43]\n"
    "s_add_u32 m0, s69, 141312\n"
    "v_mul_f32 v230, v216, v230\n"
    "v_mul_f32 v231, v216, v231\n"
    "v_mfma_f32_16x16x32_bf16 v[72:75], a[248:251], a[200:203], v[72:75]\n"
    "ds_read_b64_tr_b16 a[222:223], v198 offset:1280\n"
    "s_add_u32 s59, s61, 18688\n"
    "v_mfma_f32_16x16x32_bf16 v[104:107], a[248:251], a[204:207], v[104:107]\n"
    "buffer_load_dwordx4 v206, s[48:51], s59 offen lds\n"
    "v_mul_f32 v232, v216, v232\n"
    "v_mfma_f32_16x16x32_bf16 v[136:139], a[248:251], a[208:211], v[136:139]\n"
    "v_mul_f32 v233, v216, v233\n"
    "v_mul_f32 v234, v216, v234\n"
    "v_mfma_f32_16x16x32_bf16 v[168:171], a[248:251], a[212:215], v[168:171]\n"
    "s_add_u32 m0, s69, 142336\n"
    "v_mul_f32 v235, v216, v235\n"
    "v_cvt_pk_bf16_f32 v220, v228, v229\n"
    "s_waitcnt lgkmcnt(2)\n"
    "v_mfma_f32_16x16x32_bf16 v[12:15], a[252:255], a[192:195], v[12:15]\n"
    "ds_read_b64_tr_b16 a[248:249], v197 offset:1536\n"
    "s_add_u32 s59, s61, 27904\n"
    "v_mfma_f32_16x16x32_bf16 v[44:47], a[252:255], a[196:199], v[44:47]\n"
    "buffer_load_dwordx4 v207, s[48:51], s59 offen lds\n"
    "v_cvt_pk_bf16_f32 v221, v230, v231\n"
    "v_mfma_f32_16x16x32_bf16 v[76:79], a[252:255], a[200:203], v[76:79]\n"
    "ds_read_b64_tr_b16 a[250:251], v197 offset:1792\n"
    "v_cvt_pk_bf16_f32 v222, v232, v233\n"
    "v_mfma_f32_16x16x32_bf16 v[108:111], a[252:255], a[204:207], v[108:111]\n"
    "s_add_u32 m0, s69, 143360\n"
    "v_cvt_pk_bf16_f32 v223, v234, v235\n"
    "v_mfma_f32_16x16x32_bf16 v[140:143], a[252:255], a[208:211], v[140:143]\n"
    "buffer_store_dwordx4 v[220:223], v242, s[52:55], s73 offen offset:2048 sc1\n"
    "s_add_u32 s59, s61, 37120\n"
    "v_mfma_f32_16x16x32_bf16 v[172:175], a[252:255], a[212:215], v[172:175]\n"
    "buffer_load_dwordx4 v206, s[48:51], s59 offen lds\n"
    "v_accvgpr_read_b32 v228, a96\n"
    "v_mfma_f32_16x16x32_bf16 v[16:19], a[216:219], a[192:195], v[16:19]\n"
    "ds_read_b64_tr_b16 a[252:253], v198 offset:1536\n"
    "v_accvgpr_read_b32 v229, a97\n"
    "v_mfma_f32_16x16x32_bf16 v[48:51], a[216:219], a[196:199], v[48:51]\n"
    "s_add_u32 m0, s69, 144384\n"
    "v_accvgpr_read_b32 v230, a98\n"
    "v_accvgpr_read_b32 v231, a99\n"
    "v_mfma_f32_16x16x32_bf16 v[80:83], a[216:219], a[200:203], v[80:83]\n"
    "ds_read_b64_tr_b16 a[254:255], v198 offset:1792\n"
    "s_add_u32 s59, s61, 46336\n"
    "v_mfma_f32_16x16x32_bf16 v[112:115], a[216:219], a[204:207], v[112:115]\n"
    "buffer_load_dwordx4 v207, s[48:51], s59 offen lds\n"
    "v_accvgpr_read_b32 v232, a100\n"
    "v_mfma_f32_16x16x32_bf16 v[144:147], a[216:219], a[208:211], v[144:147]\n"
    "v_accvgpr_read_b32 v233, a101\n"
    "v_accvgpr_read_b32 v234, a102\n"
    "v_mfma_f32_16x16x32_bf16 v[176:179], a[216:219], a[212:215], v[176:179]\n"
    "v_accvgpr_read_b32 v235, a103\n"
    "v_accvgpr_write_b32 a96, 0\n"
    "s_waitcnt lgkmcnt(2)\n"
    "v_mfma_f32_16x16x32_bf16 v[20:23], a[220:223], a[192:195], v[20:23]\n"
    "v_accvgpr_write_b32 a97, 0\n"
    "v_accvgpr_write_b32 a98, 0\n"
    "v_mfma_f32_16x16x32_bf16 v[52:55], a[220:223], a[196:199], v[52:55]\n"
    "v_accvgpr_write_b32 a99, 0\n"
    "v_accvgpr_write_b32 a100, 0\n"
    "v_mfma_f32_16x16x32_bf16 v[84:87], a[220:223], a[200:203], v[84:87]\n"
    "s_waitcnt vmcnt(13) lgkmcnt(0)\n"
    "s_barrier\n"
    "v_mfma_f32_16x16x32_bf16 v[116:119], a[220:223], a[204:207], v[116:119]\n"
    "ds_read_b64_tr_b16 a[216:217], v195 offset:0\n"
    "v_accvgpr_write_b32 a101, 0\n"
    "v_mfma_f32_16x16x32_bf16 v[148:151], a[220:223], a[208:211], v[148:151]\n"
    "ds_read_b64_tr_b16 a[218:219], v195 offset:256\n"
    "v_accvgpr_write_b32 a102, 0\n"
    "v_mfma_f32_16x16x32_bf16 v[180:183], a[220:223], a[212:215], v[180:183]\n"
    "ds_read_b128 a[224:227], v199 offset:512\n"
    "v_accvgpr_write_b32 a103, 0\n"
    "v_mfma_f32_16x16x32_bf16 v[24:27], a[248:251], a[192:195], v[24:27]\n"
    "ds_read_b128 a[228:231], v199 offset:2560\n"
    "v_mul_f32 v228, v217, v228\n"
    "v_mfma_f32_16x16x32_bf16 v[56:59], a[248:251], a[196:199], v[56:59]\n"
    "ds_read_b128 a[232:235], v199 offset:4608\n"
    "v_mul_f32 v229, v217, v229\n"
    "v_mfma_f32_16x16x32_bf16 v[88:91], a[248:251], a[200:203], v[88:91]\n"
    "ds_read_b128 a[236:239], v199 offset:6656\n"
    "v_mul_f32 v230, v217, v230\n"
    "v_mfma_f32_16x16x32_bf16 v[120:123], a[248:251], a[204:207], v[120:123]\n"
    "ds_read_b128 a[240:243], v199 offset:8704\n"
    "v_mul_f32 v231, v217, v231\n"
    "v_mfma_f32_16x16x32_bf16 v[152:155], a[248:251], a[208:211], v[152:155]\n"
    "ds_read_b128 a[244:247], v199 offset:10752\n"
    "s_add_u32 m0, s65, 98304\n"
    "v_mfma_f32_16x16x32_bf16 v[184:187], a[248:251], a[212:215], v[184:187]\n"
    "buffer_load_dwordx4 v204, s[44:47], s58 offen lds\n"
    "v_mul_f32 v232, v217, v232\n"
    "v_mfma_f32_16x16x32_bf16 v[28:31], a[252:255], a[192:195], v[28:31]\n"
    "ds_read_b64_tr_b16 a[220:221], v196 offset:0\n"
    "s_add_u32 s58, s58, 0x10000\n"
    "v_mfma_f32_16x16x32_bf16 v[60:63], a[252:255], a[196:199], v[60:63]\n"
    "ds_read_b64_tr_b16 a[222:223], v196 offset:256\n"
    "s_add_u32 m0, s65, 100352\n"
    "v_mfma_f32_16x16x32_bf16 v[92:95], a[252:255], a[200:203], v[92:95]\n"
    "buffer_load_dwordx4 v204, s[44:47], s58 offen lds\n"
    "v_mul_f32 v233, v217, v233\n"
    "v_mfma_f32_16x16x32_bf16 v[124:127], a[252:255], a[204:207], v[124:127]\n"
    "ds_read_b64_tr_b16 a[248:249], v195 offset:512\n"
    "s_add_u32 s58, s58, 0x10000\n"
    "v_mfma_f32_16x16x32_bf16 v[156:159], a[252:255], a[208:211], v[156:159]\n"
    "ds_read_b64_tr_b16 a[250:251], v195 offset:768\n"
    "s_add_u32 m0, s65, 102400\n"
    "v_mfma_f32_16x16x32_bf16 v[188:191], a[252:255], a[212:215], v[188:191]\n"
    "buffer_load_dwordx4 v205, s[44:47], s58 offen lds\n"
    "v_mul_f32 v234, v217, v234\n"
    "s_waitcnt lgkmcnt(4)\n"
    "v_mfma_f32_16x16x32_bf16 v[0:3], a[216:219], a[224:227], v[0:3]\n"
    "ds_read_b64_tr_b16 a[252:253], v196 offset:512\n"
    "s_add_u32 s58, s58, 0x10000\n"
    "v_mfma_f32_16x16x32_bf16 v[32:35], a[216:219], a[228:231], v[32:35]\n"
    "v_mul_f32 v235, v217, v235\n"
    "v_cvt_pk_bf16_f32 v224, v228, v229\n"
    "v_mfma_f32_16x16x32_bf16 v[64:67], a[216:219], a[232:235], v[64:67]\n"
    "ds_read_b64_tr_b16 a[254:255], v196 offset:768\n"
    "s_add_u32 m0, s65, 104448\n"
    "v_mfma_f32_16x16x32_bf16 v[96:99], a[216:219], a[236:239], v[96:99]\n"
    "buffer_load_dwordx4 v205, s[44:47], s58 offen lds\n"
    "v_cvt_pk_bf16_f32 v225, v230, v231\n"
    "v_mfma_f32_16x16x32_bf16 v[128:131], a[216:219], a[240:243], v[128:131]\n"
    "s_add_u32 s58, s58, 0x10000\n"
    "v_cvt_pk_bf16_f32 v226, v232, v233\n"
    "v_cvt_pk_bf16_f32 v227, v234, v235\n"
    "v_mfma_f32_16x16x32_bf16 v[160:163], a[216:219], a[244:247], v[160:163]\n"
    "buffer_store_dwordx4 v[224:227], v242, s[52:55], s73 offen offset:3072 sc1\n"
    "s_waitcnt lgkmcnt(2)\n"
    "v_mfma_f32_16x16x32_bf16 v[4:7], a[220:223], a[224:227], v[4:7]\n"
    "ds_read_b64_tr_b16 a[216:217], v195 offset:1024\n"
    "s_add_u32 s73, s72, 4096\n"
    "v_mfma_f32_16x16x32_bf16 v[36:39], a[220:223], a[228:231], v[36:39]\n"
    "v_accvgpr_read_b32 v228, a128\n"
    "v_accvgpr_read_b32 v229, a129\n"
    "v_mfma_f32_16x16x32_bf16 v[68:71], a[220:223], a[232:235], v[68:71]\n"
    "ds_read_b64_tr_b16 a[218:219], v195 offset:1280\n"
    "v_accvgpr_read_b32 v230, a130\n"
    "v_mfma_f32_16x16x32_bf16 v[100:103], a[220:223], a[236:239], v[100:103]\n"
    "v_accvgpr_read_b32 v231, a131\n"
    "v_accvgpr_read_b32 v232, a132\n"
    "v_mfma_f32_16x16x32_bf16 v[132:135], a[220:223], a[240:243], v[132:135]\n"
    "v_accvgpr_read_b32 v233, a133\n"
    "v_accvgpr_read_b32 v234, a134\n"
    "v_mfma_f32_16x16x32_bf16 v[164:167], a[220:223], a[244:247], v[164:167]\n"
    "v_accvgpr_read_b32 v235, a135\n"
    "v_accvgpr_write_b32 a128, 0\n"
    "v_mfma_f32_16x16x32_bf16 v[8:11], a[248:251], a[224:227], v[8:11]\n"
    "ds_read_b64_tr_b16 a[220:221], v196 offset:1024\n"
    "v_accvgpr_write_b32 a129, 0\n"
    "v_mfma_f32_16x16x32_bf16 v[40:43], a[248:251], a[228:231], v[40:43]\n"
    "v_accvgpr_write_b32 a130, 0\n"
    "v_accvgpr_write_b32 a131, 0\n"
    "v_mfma_f32_16x16x32_bf16 v[72:75], a[248:251], a[232:235], v[72:75]\n"
    "ds_read_b64_tr_b16 a[222:223], v196 offset:1280\n"
    "v_accvgpr_write_b32 a132, 0\n"
    "v_mfma_f32_16x16x32_bf16 v[104:107], a[248:251], a[236:239], v[104:107]\n"
    "v_accvgpr_write_b32 a133, 0\n"
    "v_accvgpr_write_b32 a134, 0\n"
    "v_mfma_f32_16x16x32_bf16 v[136:139], a[248:251], a[240:243], v[136:139]\n"
    "v_accvgpr_write_b32 a135, 0\n"
    "v_mul_f32 v228, v218, v228\n"
    "v_mfma_f32_16x16x32_bf16 v[168:171], a[248:251], a[244:247], v[168:171]\n"
    "v_mul_f32 v229, v218, v229\n"
    "v_mul_f32 v230, v218, v230\n"
    "s_waitcnt lgkmcnt(2)\n"
    "v_mfma_f32_16x16x32_bf16 v[12:15], a[252:255], a[224:227], v[12:15]\n"
    "ds_read_b64_tr_b16 a[248:249], v195 offset:1536\n"
    "v_mul_f32 v231, v218, v231\n"
    "v_mfma_f32_16x16x32_bf16 v[44:47], a[252:255], a[228:231], v[44:47]\n"
    "v_mul_f32 v232, v218, v232\n"
    "v_mul_f32 v233, v218, v233\n"
    "v_mfma_f32_16x16x32_bf16 v[76:79], a[252:255], a[232:235], v[76:79]\n"
    "ds_read_b64_tr_b16 a[250:251], v195 offset:1792\n"
    "v_mul_f32 v234, v218, v234\n"
    "v_mfma_f32_16x16x32_bf16 v[108:111], a[252:255], a[236:239], v[108:111]\n"
    "v_mul_f32 v235, v218, v235\n"
    "v_cvt_pk_bf16_f32 v220, v228, v229\n"
    "v_mfma_f32_16x16x32_bf16 v[140:143], a[252:255], a[240:243], v[140:143]\n"
    "v_cvt_pk_bf16_f32 v221, v230, v231\n"
    "v_cvt_pk_bf16_f32 v222, v232, v233\n"
    "v_mfma_f32_16x16x32_bf16 v[172:175], a[252:255], a[244:247], v[172:175]\n"
    "v_cvt_pk_bf16_f32 v223, v234, v235\n"
    "v_mfma_f32_16x16x32_bf16 v[16:19], a[216:219], a[224:227], v[16:19]\n"
    "ds_read_b64_tr_b16 a[252:253], v196 offset:1536\n"
    "buffer_store_dwordx4 v[220:223], v242, s[52:55], s73 offen sc1\n"
    "v_mfma_f32_16x16x32_bf16 v[48:51], a[216:219], a[228:231], v[48:51]\n"
    "v_accvgpr_read_b32 v228, a160\n"
    "v_accvgpr_read_b32 v229, a161\n"
    "v_mfma_f32_16x16x32_bf16 v[80:83], a[216:219], a[232:235], v[80:83]\n"
    "ds_read_b64_tr_b16 a[254:255], v196 offset:1792\n"
    "v_accvgpr_read_b32 v230, a162\n"
    "v_mfma_f32_16x16x32_bf16 v[112:115], a[216:219], a[236:239], v[112:115]\n"
    "v_accvgpr_read_b32 v231, a163\n"
    "v_accvgpr_read_b32 v232, a164\n"
    "v_mfma_f32_16x16x32_bf16 v[144:147], a[216:219], a[240:243], v[144:147]\n"
    "v_accvgpr_read_b32 v233, a165\n"
    "v_accvgpr_read_b32 v234, a166\n"
    "v_mfma_f32_16x16x32_bf16 v[176:179], a[216:219], a[244:247], v[176:179]\n"
    "v_accvgpr_read_b32 v235, a167\n"
    "v_accvgpr_write_b32 a160, 0\n"
    "s_waitcnt lgkmcnt(2)\n"
    "v_mfma_f32_16x16x32_bf16 v[20:23], a[220:223], a[224:227], v[20:23]\n"
    "v_accvgpr_write_b32 a161, 0\n"
    "v_accvgpr_write_b32 a162, 0\n"
    "v_mfma_f32_16x16x32_bf16 v[52:55], a[220:223], a[228:231], v[52:55]\n"
    "v_accvgpr_write_b32 a163, 0\n"
    "v_accvgpr_write_b32 a164, 0\n"
    "v_mfma_f32_16x16x32_bf16 v[84:87], a[220:223], a[232:235], v[84:87]\n"
    "s_waitcnt vmcnt(6) lgkmcnt(0)\n"
    "s_barrier\n"
    "v_mfma_f32_16x16x32_bf16 v[116:119], a[220:223], a[236:239], v[116:119]\n"
    "ds_read_b64_tr_b16 a[216:217], v195 offset:16384\n"
    "v_accvgpr_write_b32 a165, 0\n"
    "v_mfma_f32_16x16x32_bf16 v[148:151], a[220:223], a[240:243], v[148:151]\n"
    "ds_read_b64_tr_b16 a[218:219], v195 offset:16640\n"
    "v_accvgpr_write_b32 a166, 0\n"
    "v_mfma_f32_16x16x32_bf16 v[180:183], a[220:223], a[244:247], v[180:183]\n"
    "ds_read_b128 a[192:195], v199 offset:24576\n"
    "v_accvgpr_write_b32 a167, 0\n"
    "v_mfma_f32_16x16x32_bf16 v[24:27], a[248:251], a[224:227], v[24:27]\n"
    "ds_read_b128 a[196:199], v199 offset:26624\n"
    "v_mul_f32 v228, v219, v228\n"
    "v_mfma_f32_16x16x32_bf16 v[56:59], a[248:251], a[228:231], v[56:59]\n"
    "ds_read_b128 a[200:203], v199 offset:28672\n"
    "v_mul_f32 v229, v219, v229\n"
    "v_mfma_f32_16x16x32_bf16 v[88:91], a[248:251], a[232:235], v[88:91]\n"
    "ds_read_b128 a[204:207], v199 offset:30720\n"
    "v_mul_f32 v230, v219, v230\n"
    "v_mfma_f32_16x16x32_bf16 v[120:123], a[248:251], a[236:239], v[120:123]\n"
    "ds_read_b128 a[208:211], v199 offset:32768\n"
    "v_mul_f32 v231, v219, v231\n"
    "v_mfma_f32_16x16x32_bf16 v[152:155], a[248:251], a[240:243], v[152:155]\n"
    "ds_read_b128 a[212:215], v199 offset:34816\n"
    "s_add_u32 m0, s65, 32768\n"
    "v_mfma_f32_16x16x32_bf16 v[184:187], a[248:251], a[244:247], v[184:187]\n"
    "buffer_load_dwordx4 v204, s[44:47], s58 offen lds\n"
    "v_mul_f32 v232, v219, v232\n"
    "v_mfma_f32_16x16x32_bf16 v[28:31], a[252:255], a[224:227], v[28:31]\n"
    "ds_read_b64_tr_b16 a[220:221], v196 offset:16384\n"
    "s_add_u32 s58, s58, 0x10000\n"
    "v_mfma_f32_16x16x32_bf16 v[60:63], a[252:255], a[228:231], v[60:63]\n"
    "ds_read_b64_tr_b16 a[222:223], v196 offset:16640\n"
    "s_add_u32 m0, s65, 34816\n"
    "v_mfma_f32_16x16x32_bf16 v[92:95], a[252:255], a[232:235], v[92:95]\n"
    "buffer_load_dwordx4 v204, s[44:47], s58 offen lds\n"
    "v_mul_f32 v233, v219, v233\n"
    "v_mfma_f32_16x16x32_bf16 v[124:127], a[252:255], a[236:239], v[124:127]\n"
    "ds_read_b64_tr_b16 a[248:249], v195 offset:16896\n"
    "s_add_u32 s58, s58, 0x10000\n"
    "v_mfma_f32_16x16x32_bf16 v[156:159], a[252:255], a[240:243], v[156:159]\n"
    "ds_read_b64_tr_b16 a[250:251], v195 offset:17152\n"
    "s_add_u32 m0, s65, 36864\n"
    "v_mfma_f32_16x16x32_bf16 v[188:191], a[252:255], a[244:247], v[188:191]\n"
    "buffer_load_dwordx4 v205, s[44:47], s58 offen lds\n"
    "v_mul_f32 v234, v219, v234\n"
    "s_waitcnt lgkmcnt(4)\n"
    "v_mfma_f32_16x16x32_bf16 v[0:3], a[216:219], a[192:195], v[0:3]\n"
    "ds_read_b64_tr_b16 a[252:253], v196 offset:16896\n"
    "s_add_u32 s58, s58, 0x10000\n"
    "v_mfma_f32_16x16x32_bf16 v[32:35], a[216:219], a[196:199], v[32:35]\n"
    "v_mul_f32 v235, v219, v235\n"
    "v_cvt_pk_bf16_f32 v224, v228, v229\n"
    "v_mfma_f32_16x16x32_bf16 v[64:67], a[216:219], a[200:203], v[64:67]\n"
    "ds_read_b64_tr_b16 a[254:255], v196 offset:17152\n"
    "s_add_u32 m0, s65, 38912\n"
    "v_mfma_f32_16x16x32_bf16 v[96:99], a[216:219], a[204:207], v[96:99]\n"
    "buffer_load_dwordx4 v205, s[44:47], s58 offen lds\n"
    "v_cvt_pk_bf16_f32 v225, v230, v231\n"
    "v_mfma_f32_16x16x32_bf16 v[128:131], a[216:219], a[208:211], v[128:131]\n"
    "s_add_u32 s58, s58, 0x10000\n"
    "v_cvt_pk_bf16_f32 v226, v232, v233\n"
    "v_cvt_pk_bf16_f32 v227, v234, v235\n"
    "v_mfma_f32_16x16x32_bf16 v[160:163], a[216:219], a[212:215], v[160:163]\n"
    "buffer_store_dwordx4 v[224:227], v242, s[52:55], s73 offen offset:1024 sc1\n"
    "s_add_u32 m0, s69, 114688\n"
    "s_waitcnt lgkmcnt(2)\n"
    "v_mfma_f32_16x16x32_bf16 v[4:7], a[220:223], a[192:195], v[4:7]\n"
    "ds_read_b64_tr_b16 a[216:217], v195 offset:17408\n"
    "s_add_u32 s73, s72, 6144\n"
    "s_add_u32 s59, s61, 384\n"
    "v_mfma_f32_16x16x32_bf16 v[36:39], a[220:223], a[196:199], v[36:39]\n"
    "buffer_load_dwordx4 v206, s[48:51], s59 offen lds\n"
    "v_accvgpr_read_b32 v228, a8\n"
    "v_mfma_f32_16x16x32_bf16 v[68:71], a[220:223], a[200:203], v[68:71]\n"
    "ds_read_b64_tr_b16 a[218:219], v195 offset:17664\n"
    "v_accvgpr_read_b32 v229, a9\n"
    "v_mfma_f32_16x16x32_bf16 v[100:103], a[220:223], a[204:207], v[100:103]\n"
    "s_add_u32 m0, s69, 115712\n"
    "v_accvgpr_read_b32 v230, a10\n"
    "v_accvgpr_read_b32 v231, a11\n"
    "v_mfma_f32_16x16x32_bf16 v[132:135], a[220:223], a[208:211], v[132:135]\n"
    "s_add_u32 s59, s61, 9600\n"
    "v_accvgpr_read_b32 v232, a12\n"
    "v_accvgpr_read_b32 v233, a13\n"
    "v_mfma_f32_16x16x32_bf16 v[164:167], a[220:223], a[212:215], v[164:167]\n"
    "buffer_load_dwordx4 v207, s[48:51], s59 offen lds\n"
    "v_accvgpr_read_b32 v234, a14\n"
    "v_mfma_f32_16x16x32_bf16 v[8:11], a[248:251], a[192:195], v[8:11]\n"
    "ds_read_b64_tr_b16 a[220:221], v196 offset:17408\n"
    "v_accvgpr_read_b32 v235, a15\n"
    "v_mfma_f32_16x16x32_bf16 v[40:43], a[248:251], a[196:199], v[40:43]\n"
    "s_add_u32 m0, s69, 116736\n"
    "v_accvgpr_write_b32 a8, 0\n"
    "v_accvgpr_write_b32 a9, 0\n"
    "v_mfma_f32_16x16x32_bf16 v[72:75], a[248:251], a[200:203], v[72:75]\n"
    "ds_read_b64_tr_b16 a[222:223], v196 offset:17664\n"
    "s_add_u32 s59, s61, 18816\n"
    "v_mfma_f32_16x16x32_bf16 v[104:107], a[248:251], a[204:207], v[104:107]\n"
    "buffer_load_dwordx4 v206, s[48:51], s59 offen lds\n"
    "v_accvgpr_write_b32 a10, 0\n"
    "v_mfma_f32_16x16x32_bf16 v[136:139], a[248:251], a[208:211], v[136:139]\n"
    "v_accvgpr_write_b32 a11, 0\n"
    "v_accvgpr_write_b32 a12, 0\n"
    "v_mfma_f32_16x16x32_bf16 v[168:171], a[248:251], a[212:215], v[168:171]\n"
    "s_add_u32 m0, s69, 117760\n"
    "v_accvgpr_write_b32 a13, 0\n"
    "v_accvgpr_write_b32 a14, 0\n"
    "s_waitcnt lgkmcnt(2)\n"
    "v_mfma_f32_16x16x32_bf16 v[12:15], a[252:255], a[192:195], v[12:15]\n"
    "ds_read_b64_tr_b16 a[248:249], v195 offset:17920\n"
    "s_add_u32 s59, s61, 28032\n"
    "v_mfma_f32_16x16x32_bf16 v[44:47], a[252:255], a[196:199], v[44:47]\n"
    "buffer_load_dwordx4 v207, s[48:51], s59 offen lds\n"
    "v_accvgpr_write_b32 a15, 0\n"
    "v_mfma_f32_16x16x32_bf16 v[76:79], a[252:255], a[200:203], v[76:79]\n"
    "ds_read_b64_tr_b16 a[250:251], v195 offset:18176\n"
    "v_mul_f32 v228, v214, v228\n"
    "v_mfma_f32_16x16x32_bf16 v[108:111], a[252:255], a[204:207], v[108:111]\n"
    "s_add_u32 m0, s69, 118784\n"
    "v_mul_f32 v229, v214, v229\n"
    "v_mul_f32 v230, v214, v230\n"
    "v_mfma_f32_16x16x32_bf16 v[140:143], a[252:255], a[208:211], v[140:143]\n"
    "s_add_u32 s59, s61, 37248\n"
    "v_mul_f32 v231, v214, v231\n"
    "v_mul_f32 v232, v214, v232\n"
    "v_mfma_f32_16x16x32_bf16 v[172:175], a[252:255], a[212:215], v[172:175]\n"
    "buffer_load_dwordx4 v206, s[48:51], s59 offen lds\n"
    "v_mul_f32 v233, v214, v233\n"
    "v_mfma_f32_16x16x32_bf16 v[16:19], a[216:219], a[192:195], v[16:19]\n"
    "ds_read_b64_tr_b16 a[252:253], v196 offset:17920\n"
    "v_mul_f32 v234, v214, v234\n"
    "v_mfma_f32_16x16x32_bf16 v[48:51], a[216:219], a[196:199], v[48:51]\n"
    "s_add_u32 m0, s69, 119808\n"
    "v_mul_f32 v235, v214, v235\n"
    "v_cvt_pk_bf16_f32 v220, v228, v229\n"
    "v_mfma_f32_16x16x32_bf16 v[80:83], a[216:219], a[200:203], v[80:83]\n"
    "ds_read_b64_tr_b16 a[254:255], v196 offset:18176\n"
    "s_add_u32 s59, s61, 46464\n"
    "v_mfma_f32_16x16x32_bf16 v[112:115], a[216:219], a[204:207], v[112:115]\n"
    "buffer_load_dwordx4 v207, s[48:51], s59 offen lds\n"
    "v_cvt_pk_bf16_f32 v221, v230, v231\n"
    "v_mfma_f32_16x16x32_bf16 v[144:147], a[216:219], a[208:211], v[144:147]\n"
    "v_cvt_pk_bf16_f32 v222, v232, v233\n"
    "v_cvt_pk_bf16_f32 v223, v234, v235\n"
    "v_mfma_f32_16x16x32_bf16 v[176:179], a[216:219], a[212:215], v[176:179]\n"
    "buffer_store_dwordx4 v[220:223], v242, s[52:55], s73 offen sc1\n"
    "v_accvgpr_read_b32 v228, a40\n"
    "s_waitcnt lgkmcnt(2)\n"
    "v_mfma_f32_16x16x32_bf16 v[20:23], a[220:223], a[192:195], v[20:23]\n"
    "v_accvgpr_read_b32 v229, a41\n"
    "v_accvgpr_read_b32 v230, a42\n"
    "v_mfma_f32_16x16x32_bf16 v[52:55], a[220:223], a[196:199], v[52:55]\n"
    "v_accvgpr_read_b32 v231, a43\n"
    "v_accvgpr_read_b32 v232, a44\n"
    "v_mfma_f32_16x16x32_bf16 v[84:87], a[220:223], a[200:203], v[84:87]\n"
    "s_waitcnt vmcnt(14) lgkmcnt(0)\n"
    "s_barrier\n"
    "v_mfma_f32_16x16x32_bf16 v[116:119], a[220:223], a[204:207], v[116:119]\n"
    "ds_read_b64_tr_b16 a[216:217], v197 offset:0\n"
    "v_accvgpr_read_b32 v233, a45\n"
    "v_mfma_f32_16x16x32_bf16 v[148:151], a[220:223], a[208:211], v[148:151]\n"
    "ds_read_b64_tr_b16 a[218:219], v197 offset:256\n"
    "v_accvgpr_read_b32 v234, a46\n"
    "v_mfma_f32_16x16x32_bf16 v[180:183], a[220:223], a[212:215], v[180:183]\n"
    "ds_read_b128 a[224:227], v199 offset:25088\n"
    "v_accvgpr_read_b32 v235, a47\n"
    "v_mfma_f32_16x16x32_bf16 v[24:27], a[248:251], a[192:195], v[24:27]\n"
    "ds_read_b128 a[228:231], v199 offset:27136\n"
    "v_accvgpr_write_b32 a40, 0\n"
    "v_mfma_f32_16x16x32_bf16 v[56:59], a[248:251], a[196:199], v[56:59]\n"
    "ds_read_b128 a[232:235], v199 offset:29184\n"
    "v_accvgpr_write_b32 a41, 0\n"
    "v_mfma_f32_16x16x32_bf16 v[88:91], a[248:251], a[200:203], v[88:91]\n"
    "ds_read_b128 a[236:239], v199 offset:31232\n"
    "v_accvgpr_write_b32 a42, 0\n"
    "v_mfma_f32_16x16x32_bf16 v[120:123], a[248:251], a[204:207], v[120:123]\n"
    "ds_read_b128 a[240:243], v199 offset:33280\n"
    "v_accvgpr_write_b32 a43, 0\n"
    "v_mfma_f32_16x16x32_bf16 v[152:155], a[248:251], a[208:211], v[152:155]\n"
    "ds_read_b128 a[244:247], v199 offset:35328\n"
    "s_add_u32 m0, s65, 49152\n"
    "v_mfma_f32_16x16x32_bf16 v[184:187], a[248:251], a[212:215], v[184:187]\n"
    "buffer_load_dwordx4 v204, s[44:47], s58 offen lds\n"
    "v_accvgpr_write_b32 a44, 0\n"
    "v_mfma_f32_16x16x32_bf16 v[28:31], a[252:255], a[192:195], v[28:31]\n"
    "ds_read_b64_tr_b16 a[220:221], v198 offset:0\n"
    "s_add_u32 s58, s58, 0x10000\n"
    "v_mfma_f32_16x16x32_bf16 v[60:63], a[252:255], a[196:199], v[60:63]\n"
    "ds_read_b64_tr_b16 a[222:223], v198 offset:256\n"
    "s_add_u32 m0, s65, 51200\n"
    "v_mfma_f32_16x16x32_bf16 v[92:95], a[252:255], a[200:203], v[92:95]\n"
    "buffer_load_dwordx4 v204, s[44:47], s58 offen lds\n"
    "v_accvgpr_write_b32 a45, 0\n"
    "v_mfma_f32_16x16x32_bf16 v[124:127], a[252:255], a[204:207], v[124:127]\n"
    "ds_read_b64_tr_b16 a[248:249], v197 offset:512\n"
    "s_add_u32 s58, s58, 0x10000\n"
    "v_mfma_f32_16x16x32_bf16 v[156:159], a[252:255], a[208:211], v[156:159]\n"
    "ds_read_b64_tr_b16 a[250:251], v197 offset:768\n"
    "s_add_u32 m0, s65, 53248\n"
    "v_mfma_f32_16x16x32_bf16 v[188:191], a[252:255], a[212:215], v[188:191]\n"
    "buffer_load_dwordx4 v205, s[44:47], s58 offen lds\n"
    "v_accvgpr_write_b32 a46, 0\n"
    "s_waitcnt lgkmcnt(4)\n"
    "v_mfma_f32_16x16x32_bf16 v[0:3], a[216:219], a[224:227], v[0:3]\n"
    "ds_read_b64_tr_b16 a[252:253], v198 offset:512\n"
    "s_add_u32 s58, s58, 0x10000\n"
    "v_mfma_f32_16x16x32_bf16 v[32:35], a[216:219], a[228:231], v[32:35]\n"
    "v_accvgpr_write_b32 a47, 0\n"
    "v_mul_f32 v228, v215, v228\n"
    "v_mfma_f32_16x16x32_bf16 v[64:67], a[216:219], a[232:235], v[64:67]\n"
    "ds_read_b64_tr_b16 a[254:255], v198 offset:768\n"
    "s_add_u32 m0, s65, 55296\n"
    "v_mfma_f32_16x16x32_bf16 v[96:99], a[216:219], a[236:239], v[96:99]\n"
    "buffer_load_dwordx4 v205, s[44:47], s58 offen lds\n"
    "v_mul_f32 v229, v215, v229\n"
    "v_mfma_f32_16x16x32_bf16 v[128:131], a[216:219], a[240:243], v[128:131]\n"
    "s_add_u32 s58, s58, 0x10000\n"
    "v_mul_f32 v230, v215, v230\n"
    "v_mul_f32 v231, v215, v231\n"
    "v_mfma_f32_16x16x32_bf16 v[160:163], a[216:219], a[244:247], v[160:163]\n"
    "v_mul_f32 v232, v215, v232\n"
    "v_mul_f32 v233, v215, v233\n"
    "s_waitcnt lgkmcnt(2)\n"
    "v_mfma_f32_16x16x32_bf16 v[4:7], a[220:223], a[224:227], v[4:7]\n"
    "ds_read_b64_tr_b16 a[216:217], v197 offset:1024\n"
    "v_mul_f32 v234, v215, v234\n"
    "v_mfma_f32_16x16x32_bf16 v[36:39], a[220:223], a[228:231], v[36:39]\n"
    "v_mul_f32 v235, v215, v235\n"
    "v_cvt_pk_bf16_f32 v224, v228, v229\n"
    "v_mfma_f32_16x16x32_bf16 v[68:71], a[220:223], a[232:235], v[68:71]\n"
    "ds_read_b64_tr_b16 a[218:219], v197 offset:1280\n"
    "v_cvt_pk_bf16_f32 v225, v230, v231\n"
    "v_mfma_f32_16x16x32_bf16 v[100:103], a[220:223], a[236:239], v[100:103]\n"
    "v_cvt_pk_bf16_f32 v226, v232, v233\n"
    "v_cvt_pk_bf16_f32 v227, v234, v235\n"
    "v_mfma_f32_16x16x32_bf16 v[132:135], a[220:223], a[240:243], v[132:135]\n"
    "buffer_store_dwordx4 v[224:227], v242, s[52:55], s73 offen offset:1024 sc1\n"
    "v_accvgpr_read_b32 v228, a72\n"
    "v_mfma_f32_16x16x32_bf16 v[164:167], a[220:223], a[244:247], v[164:167]\n"
    "v_accvgpr_read_b32 v229, a73\n"
    "v_accvgpr_read_b32 v230, a74\n"
    "v_mfma_f32_16x16x32_bf16 v[8:11], a[248:251], a[224:227], v[8:11]\n"
    "ds_read_b64_tr_b16 a[220:221], v198 offset:1024\n"
    "v_accvgpr_read_b32 v231, a75\n"
    "v_mfma_f32_16x16x32_bf16 v[40:43], a[248:251], a[228:231], v[40:43]\n"
    "v_accvgpr_read_b32 v232, a76\n"
    "v_accvgpr_read_b32 v233, a77\n"
    "v_mfma_f32_16x16x32_bf16 v[72:75], a[248:251], a[232:235], v[72:75]\n"
    "ds_read_b64_tr_b16 a[222:223], v198 offset:1280\n"
    "v_accvgpr_read_b32 v234, a78\n"
    "v_mfma_f32_16x16x32_bf16 v[104:107], a[248:251], a[236:239], v[104:107]\n"
    "v_accvgpr_read_b32 v235, a79\n"
    "v_accvgpr_write_b32 a72, 0\n"
    "v_mfma_f32_16x16x32_bf16 v[136:139], a[248:251], a[240:243], v[136:139]\n"
    "v_accvgpr_write_b32 a73, 0\n"
    "v_accvgpr_write_b32 a74, 0\n"
    "v_mfma_f32_16x16x32_bf16 v[168:171], a[248:251], a[244:247], v[168:171]\n"
    "v_accvgpr_write_b32 a75, 0\n"
    "v_accvgpr_write_b32 a76, 0\n"
    "s_waitcnt lgkmcnt(2)\n"
    "v_mfma_f32_16x16x32_bf16 v[12:15], a[252:255], a[224:227], v[12:15]\n"
    "ds_read_b64_tr_b16 a[248:249], v197 offset:1536\n"
    "v_accvgpr_write_b32 a77, 0\n"
    "v_mfma_f32_16x16x32_bf16 v[44:47], a[252:255], a[228:231], v[44:47]\n"
    "v_accvgpr_write_b32 a78, 0\n"
    "v_accvgpr_write_b32 a79, 0\n"
    "v_mfma_f32_16x16x32_bf16 v[76:79], a[252:255], a[232:235], v[76:79]\n"
    "ds_read_b64_tr_b16 a[250:251], v197 offset:1792\n"
    "v_mul_f32 v228, v216, v228\n"
    "v_mfma_f32_16x16x32_bf16 v[108:111], a[252:255], a[236:239], v[108:111]\n"
    "v_mul_f32 v229, v216, v229\n"
    "v_mul_f32 v230, v216, v230\n"
    "v_mfma_f32_16x16x32_bf16 v[140:143], a[252:255], a[240:243], v[140:143]\n"
    "v_mul_f32 v231, v216, v231\n"
    "v_mul_f32 v232, v216, v232\n"
    "v_mfma_f32_16x16x32_bf16 v[172:175], a[252:255], a[244:247], v[172:175]\n"
    "v_mul_f32 v233, v216, v233\n"
    "v_mul_f32 v234, v216, v234\n"
    "v_mfma_f32_16x16x32_bf16 v[16:19], a[216:219], a[224:227], v[16:19]\n"
    "ds_read_b64_tr_b16 a[252:253], v198 offset:1536\n"
    "v_mul_f32 v235, v216, v235\n"
    "v_mfma_f32_16x16x32_bf16 v[48:51], a[216:219], a[228:231], v[48:51]\n"
    "v_cvt_pk_bf16_f32 v220, v228, v229\n"
    "v_cvt_pk_bf16_f32 v221, v230, v231\n"
    "v_mfma_f32_16x16x32_bf16 v[80:83], a[216:219], a[232:235], v[80:83]\n"
    "ds_read_b64_tr_b16 a[254:255], v198 offset:1792\n"
    "v_cvt_pk_bf16_f32 v222, v232, v233\n"
    "v_mfma_f32_16x16x32_bf16 v[112:115], a[216:219], a[236:239], v[112:115]\n"
    "v_cvt_pk_bf16_f32 v223, v234, v235\n"
    "v_mfma_f32_16x16x32_bf16 v[144:147], a[216:219], a[240:243], v[144:147]\n"
    "buffer_store_dwordx4 v[220:223], v242, s[52:55], s73 offen offset:2048 sc1\n"
    "v_accvgpr_read_b32 v228, a104\n"
    "v_mfma_f32_16x16x32_bf16 v[176:179], a[216:219], a[244:247], v[176:179]\n"
    "v_accvgpr_read_b32 v229, a105\n"
    "v_accvgpr_read_b32 v230, a106\n"
    "s_waitcnt lgkmcnt(2)\n"
    "v_mfma_f32_16x16x32_bf16 v[20:23], a[220:223], a[224:227], v[20:23]\n"
    "v_accvgpr_read_b32 v231, a107\n"
    "v_accvgpr_read_b32 v232, a108\n"
    "v_mfma_f32_16x16x32_bf16 v[52:55], a[220:223], a[228:231], v[52:55]\n"
    "v_accvgpr_read_b32 v233, a109\n"
    "v_accvgpr_read_b32 v234, a110\n"
    "v_mfma_f32_16x16x32_bf16 v[84:87], a[220:223], a[232:235], v[84:87]\n"
    "s_waitcnt vmcnt(7) lgkmcnt(0)\n"
    "s_barrier\n"
    "v_mfma_f32_16x16x32_bf16 v[116:119], a[220:223], a[236:239], v[116:119]\n"
    "ds_read_b64_tr_b16 a[216:217], v195 offset:0\n"
    "v_accvgpr_read_b32 v235, a111\n"
    "v_mfma_f32_16x16x32_bf16 v[148:151], a[220:223], a[240:243], v[148:151]\n"
    "ds_read_b64_tr_b16 a[218:219], v195 offset:256\n"
    "v_accvgpr_write_b32 a104, 0\n"
    "v_mfma_f32_16x16x32_bf16 v[180:183], a[220:223], a[244:247], v[180:183]\n"
    "ds_read_b128 a[192:195], v199 offset:0\n"
    "v_accvgpr_write_b32 a105, 0\n"
    "v_mfma_f32_16x16x32_bf16 v[24:27], a[248:251], a[224:227], v[24:27]\n"
    "ds_read_b128 a[196:199], v199 offset:2048\n"
    "v_accvgpr_write_b32 a106, 0\n"
    "v_mfma_f32_16x16x32_bf16 v[56:59], a[248:251], a[228:231], v[56:59]\n"
    "ds_read_b128 a[200:203], v199 offset:4096\n"
    "v_accvgpr_write_b32 a107, 0\n"
    "v_mfma_f32_16x16x32_bf16 v[88:91], a[248:251], a[232:235], v[88:91]\n"
    "ds_read_b128 a[204:207], v199 offset:6144\n"
    "v_accvgpr_write_b32 a108, 0\n"
    "v_mfma_f32_16x16x32_bf16 v[120:123], a[248:251], a[236:239], v[120:123]\n"
    "ds_read_b128 a[208:211], v199 offset:8192\n"
    "v_accvgpr_write_b32 a109, 0\n"
    "v_mfma_f32_16x16x32_bf16 v[152:155], a[248:251], a[240:243], v[152:155]\n"
    "ds_read_b128 a[212:215], v199 offset:10240\n"
    "s_add_u32 m0, s65, 98304\n"
    "v_mfma_f32_16x16x32_bf16 v[184:187], a[248:251], a[244:247], v[184:187]\n"
    "buffer_load_dwordx4 v204, s[44:47], s58 offen lds\n"
    "v_accvgpr_write_b32 a110, 0\n"
    "v_mfma_f32_16x16x32_bf16 v[28:31], a[252:255], a[224:227], v[28:31]\n"
    "ds_read_b64_tr_b16 a[220:221], v196 offset:0\n"
    "s_add_u32 s58, s58, 0x10000\n"
    "v_mfma_f32_16x16x32_bf16 v[60:63], a[252:255], a[228:231], v[60:63]\n"
    "ds_read_b64_tr_b16 a[222:223], v196 offset:256\n"
    "s_add_u32 m0, s65, 100352\n"
    "v_mfma_f32_16x16x32_bf16 v[92:95], a[252:255], a[232:235], v[92:95]\n"
    "buffer_load_dwordx4 v204, s[44:47], s58 offen lds\n"
    "v_accvgpr_write_b32 a111, 0\n"
    "v_mfma_f32_16x16x32_bf16 v[124:127], a[252:255], a[236:239], v[124:127]\n"
    "ds_read_b64_tr_b16 a[248:249], v195 offset:512\n"
    "s_add_u32 s58, s58, 0x10000\n"
    "v_mfma_f32_16x16x32_bf16 v[156:159], a[252:255], a[240:243], v[156:159]\n"
    "ds_read_b64_tr_b16 a[250:251], v195 offset:768\n"
    "s_add_u32 m0, s65, 102400\n"
    "v_mfma_f32_16x16x32_bf16 v[188:191], a[252:255], a[244:247], v[188:191]\n"
    "buffer_load_dwordx4 v205, s[44:47], s58 offen lds\n"
    "v_mul_f32 v228, v217, v228\n"
    "s_waitcnt lgkmcnt(4)\n"
    "v_mfma_f32_16x16x32_bf16 v[0:3], a[216:219], a[192:195], v[0:3]\n"
    "ds_read_b64_tr_b16 a[252:253], v196 offset:512\n"
    "s_add_u32 s58, s58, 0x10000\n"
    "v_mfma_f32_16x16x32_bf16 v[32:35], a[216:219], a[196:199], v[32:35]\n"
    "v_mul_f32 v229, v217, v229\n"
    "v_mul_f32 v230, v217, v230\n"
    "v_mfma_f32_16x16x32_bf16 v[64:67], a[216:219], a[200:203], v[64:67]\n"
    "ds_read_b64_tr_b16 a[254:255], v196 offset:768\n"
    "s_add_u32 m0, s65, 104448\n"
    "v_mfma_f32_16x16x32_bf16 v[96:99], a[216:219], a[204:207], v[96:99]\n"
    "buffer_load_dwordx4 v205, s[44:47], s58 offen lds\n"
    "v_mul_f32 v231, v217, v231\n"
    "v_mfma_f32_16x16x32_bf16 v[128:131], a[216:219], a[208:211], v[128:131]\n"
    "s_add_u32 s58, s58, 0x10000\n"
    "v_mul_f32 v232, v217, v232\n"
    "v_mul_f32 v233, v217, v233\n"
    "v_mfma_f32_16x16x32_bf16 v[160:163], a[216:219], a[212:215], v[160:163]\n"
    "s_add_u32 m0, s69, 139264\n"
    "v_mul_f32 v234, v217, v234\n"
    "v_mul_f32 v235, v217, v235\n"
    "s_waitcnt lgkmcnt(2)\n"
    "v_mfma_f32_16x16x32_bf16 v[4:7], a[220:223], a[192:195], v[4:7]\n"
    "ds_read_b64_tr_b16 a[216:217], v195 offset:1024\n"
    "s_add_u32 s59, s61, 512\n"
    "v_mfma_f32_16x16x32_bf16 v[36:39], a[220:223], a[196:199], v[36:39]\n"
    "buffer_load_dwordx4 v206, s[48:51], s59 offen lds\n"
    "v_cvt_pk_bf16_f32 v224, v228, v229\n"
    "v_mfma_f32_16x16x32_bf16 v[68:71], a[220:223], a[200:203], v[68:71]\n"
    "ds_read_b64_tr_b16 a[218:219], v195 offset:1280\n"
    "v_cvt_pk_bf16_f32 v225, v230, v231\n"
    "v_mfma_f32_16x16x32_bf16 v[100:103], a[220:223], a[204:207], v[100:103]\n"
    "s_add_u32 m0, s69, 140288\n"
    "v_cvt_pk_bf16_f32 v226, v232, v233\n"
    "v_cvt_pk_bf16_f32 v227, v234, v235\n"
    "v_mfma_f32_16x16x32_bf16 v[132:135], a[220:223], a[208:211], v[132:135]\n"
    "buffer_store_dwordx4 v[224:227], v242, s[52:55], s73 offen offset:3072 sc1\n"
    "s_add_u32 s59, s61, 9728\n"
    "v_mfma_f32_16x16x32_bf16 v[164:167], a[220:223], a[212:215], v[164:167]\n"
    "buffer_load_dwordx4 v207, s[48:51], s59 offen lds\n"
    "s_add_u32 s73, s72, 10240\n"
    "v_mfma_f32_16x16x32_bf16 v[8:11], a[248:251], a[192:195], v[8:11]\n"
    "ds_read_b64_tr_b16 a[220:221], v196 offset:1024\n"
    "v_accvgpr_read_b32 v228, a136\n"
    "v_mfma_f32_16x16x32_bf16 v[40:43], a[248:251], a[196:199], v[40:43]\n"
    "s_add_u32 m0, s69, 141312\n"
    "v_accvgpr_read_b32 v229, a137\n"
    "v_accvgpr_read_b32 v230, a138\n"
    "v_mfma_f32_16x16x32_bf16 v[72:75], a[248:251], a[200:203], v[72:75]\n"
    "ds_read_b64_tr_b16 a[222:223], v196 offset:1280\n"
    "s_add_u32 s59, s61, 18944\n"
    "v_mfma_f32_16x16x32_bf16 v[104:107], a[248:251], a[204:207], v[104:107]\n"
    "buffer_load_dwordx4 v206, s[48:51], s59 offen lds\n"
    "v_accvgpr_read_b32 v231, a139\n"
    "v_mfma_f32_16x16x32_bf16 v[136:139], a[248:251], a[208:211], v[136:139]\n"
    "v_accvgpr_read_b32 v232, a140\n"
    "v_accvgpr_read_b32 v233, a141\n"
    "v_mfma_f32_16x16x32_bf16 v[168:171], a[248:251], a[212:215], v[168:171]\n"
    "s_add_u32 m0, s69, 142336\n"
    "v_accvgpr_read_b32 v234, a142\n"
    "v_accvgpr_read_b32 v235, a143\n"
    "s_waitcnt lgkmcnt(2)\n"
    "v_mfma_f32_16x16x32_bf16 v[12:15], a[252:255], a[192:195], v[12:15]\n"
    "ds_read_b64_tr_b16 a[248:249], v195 offset:1536\n"
    "s_add_u32 s59, s61, 28160\n"
    "v_mfma_f32_16x16x32_bf16 v[44:47], a[252:255], a[196:199], v[44:47]\n"
    "buffer_load_dwordx4 v207, s[48:51], s59 offen lds\n"
    "v_accvgpr_write_b32 a136, 0\n"
    "v_mfma_f32_16x16x32_bf16 v[76:79], a[252:255], a[200:203], v[76:79]\n"
    "ds_read_b64_tr_b16 a[250:251], v195 offset:1792\n"
    "v_accvgpr_write_b32 a137, 0\n"
    "v_mfma_f32_16x16x32_bf16 v[108:111], a[252:255], a[204:207], v[108:111]\n"
    "s_add_u32 m0, s69, 143360\n"
    "v_accvgpr_write_b32 a138, 0\n"
    "v_accvgpr_write_b32 a139, 0\n"
    "v_mfma_f32_16x16x32_bf16 v[140:143], a[252:255], a[208:211], v[140:143]\n"
    "s_add_u32 s59, s61, 37376\n"
    "v_accvgpr_write_b32 a140, 0\n"
    "v_accvgpr_write_b32 a141, 0\n"
    "v_mfma_f32_16x16x32_bf16 v[172:175], a[252:255], a[212:215], v[172:175]\n"
    "buffer_load_dwordx4 v206, s[48:51], s59 offen lds\n"
    "v_accvgpr_write_b32 a142, 0\n"
    "v_mfma_f32_16x16x32_bf16 v[16:19], a[216:219], a[192:195], v[16:19]\n"
    "ds_read_b64_tr_b16 a[252:253], v196 offset:1536\n"
    "v_accvgpr_write_b32 a143, 0\n"
    "v_mfma_f32_16x16x32_bf16 v[48:51], a[216:219], a[196:199], v[48:51]\n"
    "s_add_u32 m0, s69, 144384\n"
    "v_mul_f32 v228, v218, v228\n"
    "v_mul_f32 v229, v218, v229\n"
    "v_mfma_f32_16x16x32_bf16 v[80:83], a[216:219], a[200:203], v[80:83]\n"
    "ds_read_b64_tr_b16 a[254:255], v196 offset:1792\n"
    "s_add_u32 s59, s61, 46592\n"
    "v_mfma_f32_16x16x32_bf16 v[112:115], a[216:219], a[204:207], v[112:115]\n"
    "buffer_load_dwordx4 v207, s[48:51], s59 offen lds\n"
    "v_mul_f32 v230, v218, v230\n"
    "v_mfma_f32_16x16x32_bf16 v[144:147], a[216:219], a[208:211], v[144:147]\n"
    "v_mul_f32 v231, v218, v231\n"
    "v_mul_f32 v232, v218, v232\n"
    "v_mfma_f32_16x16x32_bf16 v[176:179], a[216:219], a[212:215], v[176:179]\n"
    "v_mul_f32 v233, v218, v233\n"
    "v_mul_f32 v234, v218, v234\n"
    "s_waitcnt lgkmcnt(2)\n"
    "v_mfma_f32_16x16x32_bf16 v[20:23], a[220:223], a[192:195], v[20:23]\n"
    "v_mul_f32 v235, v218, v235\n"
    "v_cvt_pk_bf16_f32 v220, v228, v229\n"
    "v_mfma_f32_16x16x32_bf16 v[52:55], a[220:223], a[196:199], v[52:55]\n"
    "v_cvt_pk_bf16_f32 v221, v230, v231\n"
    "v_cvt_pk_bf16_f32 v222, v232, v233\n"
    "v_mfma_f32_16x16x32_bf16 v[84:87], a[220:223], a[200:203], v[84:87]\n"
    "s_waitcnt vmcnt(13) lgkmcnt(0)\n"
    "s_barrier\n"
    "v_mfma_f32_16x16x32_bf16 v[116:119], a[220:223], a[204:207], v[116:119]\n"
    "ds_read_b64_tr_b16 a[216:217], v195 offset:16384\n"
    "v_cvt_pk_bf16_f32 v223, v234, v235\n"
    "v_mfma_f32_16x16x32_bf16 v[148:151], a[220:223], a[208:211], v[148:151]\n"
    "ds_read_b64_tr_b16 a[218:219], v195 offset:16640\n"
    "buffer_store_dwordx4 v[220:223], v242, s[52:55], s73 offen sc1\n"
    "v_mfma_f32_16x16x32_bf16 v[180:183], a[220:223], a[212:215], v[180:183]\n"
    "ds_read_b128 a[224:227], v199 offset:512\n"
    "v_accvgpr_read_b32 v228, a168\n"
    "v_mfma_f32_16x16x32_bf16 v[24:27], a[248:251], a[192:195], v[24:27]\n"
    "ds_read_b128 a[228:231], v199 offset:2560\n"
    "v_accvgpr_read_b32 v229, a169\n"
    "v_mfma_f32_16x16x32_bf16 v[56:59], a[248:251], a[196:199], v[56:59]\n"
    "ds_read_b128 a[232:235], v199 offset:4608\n"
    "v_accvgpr_read_b32 v230, a170\n"
    "v_mfma_f32_16x16x32_bf16 v[88:91], a[248:251], a[200:203], v[88:91]\n"
    "ds_read_b128 a[236:239], v199 offset:6656\n"
    "v_accvgpr_read_b32 v231, a171\n"
    "v_mfma_f32_16x16x32_bf16 v[120:123], a[248:251], a[204:207], v[120:123]\n"
    "ds_read_b128 a[240:243], v199 offset:8704\n"
    "v_accvgpr_read_b32 v232, a172\n"
    "v_mfma_f32_16x16x32_bf16 v[152:155], a[248:251], a[208:211], v[152:155]\n"
    "ds_read_b128 a[244:247], v199 offset:10752\n"
    "s_add_u32 m0, s65, 32768\n"
    "v_mfma_f32_16x16x32_bf16 v[184:187], a[248:251], a[212:215], v[184:187]\n"
    "buffer_load_dwordx4 v204, s[44:47], s58 offen lds\n"
    "v_accvgpr_read_b32 v233, a173\n"
    "v_mfma_f32_16x16x32_bf16 v[28:31], a[252:255], a[192:195], v[28:31]\n"
    "ds_read_b64_tr_b16 a[220:221], v196 offset:16384\n"
    "s_add_u32 s58, s58, 0x10000\n"
    "v_mfma_f32_16x16x32_bf16 v[60:63], a[252:255], a[196:199], v[60:63]\n"
    "ds_read_b64_tr_b16 a[222:223], v196 offset:16640\n"
    "s_add_u32 m0, s65, 34816\n"
    "v_mfma_f32_16x16x32_bf16 v[92:95], a[252:255], a[200:203], v[92:95]\n"
    "buffer_load_dwordx4 v204, s[44:47], s58 offen lds\n"
    "v_accvgpr_read_b32 v234, a174\n"
    "v_mfma_f32_16x16x32_bf16 v[124:127], a[252:255], a[204:207], v[124:127]\n"
    "ds_read_b64_tr_b16 a[248:249], v195 offset:16896\n"
    "s_add_u32 s58, s58, 0x10000\n"
    "v_mfma_f32_16x16x32_bf16 v[156:159], a[252:255], a[208:211], v[156:159]\n"
    "ds_read_b64_tr_b16 a[250:251], v195 offset:17152\n"
    "s_add_u32 m0, s65, 36864\n"
    "v_mfma_f32_16x16x32_bf16 v[188:191], a[252:255], a[212:215], v[188:191]\n"
    "buffer_load_dwordx4 v205, s[44:47], s58 offen lds\n"
    "v_accvgpr_read_b32 v235, a175\n"
    "s_waitcnt lgkmcnt(4)\n"
    "v_mfma_f32_16x16x32_bf16 v[0:3], a[216:219], a[224:227], v[0:3]\n"
    "ds_read_b64_tr_b16 a[252:253], v196 offset:16896\n"
    "s_add_u32 s58, s58, 0x10000\n"
    "v_mfma_f32_16x16x32_bf16 v[32:35], a[216:219], a[228:231], v[32:35]\n"
    "v_accvgpr_write_b32 a168, 0\n"
    "v_accvgpr_write_b32 a169, 0\n"
    "v_mfma_f32_16x16x32_bf16 v[64:67], a[216:219], a[232:235], v[64:67]\n"
    "ds_read_b64_tr_b16 a[254:255], v196 offset:17152\n"
    "s_add_u32 m0, s65, 38912\n"
    "v_mfma_f32_16x16x32_bf16 v[96:99], a[216:219], a[236:239], v[96:99]\n"
    "buffer_load_dwordx4 v205, s[44:47], s58 offen lds\n"
    "v_accvgpr_write_b32 a170, 0\n"
    "v_mfma_f32_16x16x32_bf16 v[128:131], a[216:219], a[240:243], v[128:131]\n"
    "s_add_u32 s58, s58, 0x10000\n"
    "v_accvgpr_write_b32 a171, 0\n"
    "v_accvgpr_write_b32 a172, 0\n"
    "v_mfma_f32_16x16x32_bf16 v[160:163], a[216:219], a[244:247], v[160:163]\n"
    "v_accvgpr_write_b32 a173, 0\n"
    "v_accvgpr_write_b32 a174, 0\n"
    "s_waitcnt lgkmcnt(2)\n"
    "v_mfma_f32_16x16x32_bf16 v[4:7], a[220:223], a[224:227], v[4:7]\n"
    "ds_read_b64_tr_b16 a[216:217], v195 offset:17408\n"
    "v_accvgpr_write_b32 a175, 0\n"
    "v_mfma_f32_16x16x32_bf16 v[36:39], a[220:223], a[228:231], v[36:39]\n"
    "v_mul_f32 v228, v219, v228\n"
    "v_mul_f32 v229, v219, v229\n"
    "v_mfma_f32_16x16x32_bf16 v[68:71], a[220:223], a[232:235], v[68:71]\n"
    "ds_read_b64_tr_b16 a[218:219], v195 offset:17664\n"
    "v_mul_f32 v230, v219, v230\n"
    "v_mfma_f32_16x16x32_bf16 v[100:103], a[220:223], a[236:239], v[100:103]\n"
    "v_mul_f32 v231, v219, v231\n"
    "v_mul_f32 v232, v219, v232\n"
    "v_mfma_f32_16x16x32_bf16 v[132:135], a[220:223], a[240:243], v[132:135]\n"
    "v_mul_f32 v233, v219, v233\n"
    "v_mul_f32 v234, v219, v234\n"
    "v_mfma_f32_16x16x32_bf16 v[164:167], a[220:223], a[244:247], v[164:167]\n"
    "v_mul_f32 v235, v219, v235\n"
    "v_cvt_pk_bf16_f32 v224, v228, v229\n"
    "v_mfma_f32_16x16x32_bf16 v[8:11], a[248:251], a[224:227], v[8:11]\n"
    "ds_read_b64_tr_b16 a[220:221], v196 offset:17408\n"
    "v_cvt_pk_bf16_f32 v225, v230, v231\n"
    "v_mfma_f32_16x16x32_bf16 v[40:43], a[248:251], a[228:231], v[40:43]\n"
    "v_cvt_pk_bf16_f32 v226, v232, v233\n"
    "v_cvt_pk_bf16_f32 v227, v234, v235\n"
    "v_mfma_f32_16x16x32_bf16 v[72:75], a[248:251], a[232:235], v[72:75]\n"
    "ds_read_b64_tr_b16 a[222:223], v196 offset:17664\n"
    "buffer_store_dwordx4 v[224:227], v242, s[52:55], s73 offen offset:1024 sc1\n"
    "v_mfma_f32_16x16x32_bf16 v[104:107], a[248:251], a[236:239], v[104:107]\n"
    "s_add_u32 s73, s72, 12288\n"
    "v_accvgpr_read_b32 v228, a16\n"
    "v_accvgpr_read_b32 v229, a17\n"
    "v_mfma_f32_16x16x32_bf16 v[136:139], a[248:251], a[240:243], v[136:139]\n"
    "v_accvgpr_read_b32 v230, a18\n"
    "v_accvgpr_read_b32 v231, a19\n"
    "v_mfma_f32_16x16x32_bf16 v[168:171], a[248:251], a[244:247], v[168:171]\n"
    "v_accvgpr_read_b32 v232, a20\n"
    "v_accvgpr_read_b32 v233, a21\n"
    "s_waitcnt lgkmcnt(2)\n"
    "v_mfma_f32_16x16x32_bf16 v[12:15], a[252:255], a[224:227], v[12:15]\n"
    "ds_read_b64_tr_b16 a[248:249], v195 offset:17920\n"
    "v_accvgpr_read_b32 v234, a22\n"
    "v_mfma_f32_16x16x32_bf16 v[44:47], a[252:255], a[228:231], v[44:47]\n"
    "v_accvgpr_read_b32 v235, a23\n"
    "v_accvgpr_write_b32 a16, 0\n"
    "v_mfma_f32_16x16x32_bf16 v[76:79], a[252:255], a[232:235], v[76:79]\n"
    "ds_read_b64_tr_b16 a[250:251], v195 offset:18176\n"
    "v_accvgpr_write_b32 a17, 0\n"
    "v_mfma_f32_16x16x32_bf16 v[108:111], a[252:255], a[236:239], v[108:111]\n"
    "v_accvgpr_write_b32 a18, 0\n"
    "v_accvgpr_write_b32 a19, 0\n"
    "v_mfma_f32_16x16x32_bf16 v[140:143], a[252:255], a[240:243], v[140:143]\n"
    "v_accvgpr_write_b32 a20, 0\n"
    "v_accvgpr_write_b32 a21, 0\n"
    "v_mfma_f32_16x16x32_bf16 v[172:175], a[252:255], a[244:247], v[172:175]\n"
    "v_accvgpr_write_b32 a22, 0\n"
    "v_accvgpr_write_b32 a23, 0\n"
    "v_mfma_f32_16x16x32_bf16 v[16:19], a[216:219], a[224:227], v[16:19]\n"
    "ds_read_b64_tr_b16 a[252:253], v196 offset:17920\n"
    "v_mul_f32 v228, v214, v228\n"
    "v_mfma_f32_16x16x32_bf16 v[48:51], a[216:219], a[228:231], v[48:51]\n"
    "v_mul_f32 v229, v214, v229\n"
    "v_mul_f32 v230, v214, v230\n"
    "v_mfma_f32_16x16x32_bf16 v[80:83], a[216:219], a[232:235], v[80:83]\n"
    "ds_read_b64_tr_b16 a[254:255], v196 offset:18176\n"
    "v_mul_f32 v231, v214, v231\n"
    "v_mfma_f32_16x16x32_bf16 v[112:115], a[216:219], a[236:239], v[112:115]\n"
    "v_mul_f32 v232, v214, v232\n"
    "v_mul_f32 v233, v214, v233\n"
    "v_mfma_f32_16x16x32_bf16 v[144:147], a[216:219], a[240:243], v[144:147]\n"
    "v_mul_f32 v234, v214, v234\n"
    "v_mul_f32 v235, v214, v235\n"
    "v_mfma_f32_16x16x32_bf16 v[176:179], a[216:219], a[244:247], v[176:179]\n"
    "v_cvt_pk_bf16_f32 v220, v228, v229\n"
    "v_cvt_pk_bf16_f32 v221, v230, v231\n"
    "s_waitcnt lgkmcnt(2)\n"
    "v_mfma_f32_16x16x32_bf16 v[20:23], a[220:223], a[224:227], v[20:23]\n"
    "v_cvt_pk_bf16_f32 v222, v232, v233\n"
    "v_cvt_pk_bf16_f32 v223, v234, v235\n"
    "v_mfma_f32_16x16x32_bf16 v[52:55], a[220:223], a[228:231], v[52:55]\n"
    "buffer_store_dwordx4 v[220:223], v242, s[52:55], s73 offen sc1\n"
    "v_accvgpr_read_b32 v228, a48\n"
    "v_mfma_f32_16x16x32_bf16 v[84:87], a[220:223], a[232:235], v[84:87]\n"
    "s_waitcnt vmcnt(7) lgkmcnt(0)\n"
    "s_barrier\n"
    "v_mfma_f32_16x16x32_bf16 v[116:119], a[220:223], a[236:239], v[116:119]\n"
    "ds_read_b64_tr_b16 a[216:217], v197 offset:0\n"
    "v_accvgpr_read_b32 v229, a49\n"
    "v_mfma_f32_16x16x32_bf16 v[148:151], a[220:223], a[240:243], v[148:151]\n"
    "ds_read_b64_tr_b16 a[218:219], v197 offset:256\n"
    "v_accvgpr_read_b32 v230, a50\n"
    "v_mfma_f32_16x16x32_bf16 v[180:183], a[220:223], a[244:247], v[180:183]\n"
    "ds_read_b128 a[192:195], v199 offset:24576\n"
    "v_accvgpr_read_b32 v231, a51\n"
    "v_mfma_f32_16x16x32_bf16 v[24:27], a[248:251], a[224:227], v[24:27]\n"
    "ds_read_b128 a[196:199], v199 offset:26624\n"
    "v_accvgpr_read_b32 v232, a52\n"
    "v_mfma_f32_16x16x32_bf16 v[56:59], a[248:251], a[228:231], v[56:59]\n"
    "ds_read_b128 a[200:203], v199 offset:28672\n"
    "v_accvgpr_read_b32 v233, a53\n"
    "v_mfma_f32_16x16x32_bf16 v[88:91], a[248:251], a[232:235], v[88:91]\n"
    "ds_read_b128 a[204:207], v199 offset:30720\n"
    "v_accvgpr_read_b32 v234, a54\n"
    "v_mfma_f32_16x16x32_bf16 v[120:123], a[248:251], a[236:239], v[120:123]\n"
    "ds_read_b128 a[208:211], v199 offset:32768\n"
    "v_accvgpr_read_b32 v235, a55\n"
    "v_mfma_f32_16x16x32_bf16 v[152:155], a[248:251], a[240:243], v[152:155]\n"
    "ds_read_b128 a[212:215], v199 offset:34816\n"
    "s_add_u32 m0, s65, 49152\n"
    "v_mfma_f32_16x16x32_bf16 v[184:187], a[248:251], a[244:247], v[184:187]\n"
    "buffer_load_dwordx4 v204, s[44:47], s58 offen lds\n"
    "v_accvgpr_write_b32 a48, 0\n"
    "v_mfma_f32_16x16x32_bf16 v[28:31], a[252:255], a[224:227], v[28:31]\n"
    "ds_read_b64_tr_b16 a[220:221], v198 offset:0\n"
    "s_add_u32 s58, s58, 0x10000\n"
    "v_mfma_f32_16x16x32_bf16 v[60:63], a[252:255], a[228:231], v[60:63]\n"
    "ds_read_b64_tr_b16 a[222:223], v198 offset:256\n"
    "s_add_u32 m0, s65, 51200\n"
    "v_mfma_f32_16x16x32_bf16 v[92:95], a[252:255], a[232:235], v[92:95]\n"
    "buffer_load_dwordx4 v204, s[44:47], s58 offen lds\n"
    "v_accvgpr_write_b32 a49, 0\n"
    "v_mfma_f32_16x16x32_bf16 v[124:127], a[252:255], a[236:239], v[124:127]\n"
    "ds_read_b64_tr_b16 a[248:249], v197 offset:512\n"
    "s_add_u32 s58, s58, 0x10000\n"
    "v_mfma_f32_16x16x32_bf16 v[156:159], a[252:255], a[240:243], v[156:159]\n"
    "ds_read_b64_tr_b16 a[250:251], v197 offset:768\n"
    "s_add_u32 m0, s65, 53248\n"
    "v_mfma_f32_16x16x32_bf16 v[188:191], a[252:255], a[244:247], v[188:191]\n"
    "buffer_load_dwordx4 v205, s[44:47], s58 offen lds\n"
    "v_accvgpr_write_b32 a50, 0\n"
    "s_waitcnt lgkmcnt(4)\n"
    "v_mfma_f32_16x16x32_bf16 v[0:3], a[216:219], a[192:195], v[0:3]\n"
    "ds_read_b64_tr_b16 a[252:253], v198 offset:512\n"
    "s_add_u32 s58, s58, 0x10000\n"
    "v_mfma_f32_16x16x32_bf16 v[32:35], a[216:219], a[196:199], v[32:35]\n"
    "v_accvgpr_write_b32 a51, 0\n"
    "v_accvgpr_write_b32 a52, 0\n"
    "v_mfma_f32_16x16x32_bf16 v[64:67], a[216:219], a[200:203], v[64:67]\n"
    "ds_read_b64_tr_b16 a[254:255], v198 offset:768\n"
    "s_add_u32 m0, s65, 55296\n"
    "v_mfma_f32_16x16x32_bf16 v[96:99], a[216:219], a[204:207], v[96:99]\n"
    "buffer_load_dwordx4 v205, s[44:47], s58 offen lds\n"
    "v_accvgpr_write_b32 a53, 0\n"
    "v_mfma_f32_16x16x32_bf16 v[128:131], a[216:219], a[208:211], v[128:131]\n"
    "s_add_u32 s58, s58, 0x10000\n"
    "v_accvgpr_write_b32 a54, 0\n"
    "v_accvgpr_write_b32 a55, 0\n"
    "v_mfma_f32_16x16x32_bf16 v[160:163], a[216:219], a[212:215], v[160:163]\n"
    "s_add_u32 m0, s69, 114688\n"
    "v_mul_f32 v228, v215, v228\n"
    "v_mul_f32 v229, v215, v229\n"
    "s_waitcnt lgkmcnt(2)\n"
    "v_mfma_f32_16x16x32_bf16 v[4:7], a[220:223], a[192:195], v[4:7]\n"
    "ds_read_b64_tr_b16 a[216:217], v197 offset:1024\n"
    "s_add_u32 s59, s61, 640\n"
    "v_mfma_f32_16x16x32_bf16 v[36:39], a[220:223], a[196:199], v[36:39]\n"
    "buffer_load_dwordx4 v206, s[48:51], s59 offen lds\n"
    "v_mul_f32 v230, v215, v230\n"
    "v_mfma_f32_16x16x32_bf16 v[68:71], a[220:223], a[200:203], v[68:71]\n"
    "ds_read_b64_tr_b16 a[218:219], v197 offset:1280\n"
    "v_mul_f32 v231, v215, v231\n"
    "v_mfma_f32_16x16x32_bf16 v[100:103], a[220:223], a[204:207], v[100:103]\n"
    "s_add_u32 m0, s69, 115712\n"
    "v_mul_f32 v232, v215, v232\n"
    "v_mul_f32 v233, v215, v233\n"
    "v_mfma_f32_16x16x32_bf16 v[132:135], a[220:223], a[208:211], v[132:135]\n"
    "s_add_u32 s59, s61, 9856\n"
    "v_mul_f32 v234, v215, v234\n"
    "v_mul_f32 v235, v215, v235\n"
    "v_mfma_f32_16x16x32_bf16 v[164:167], a[220:223], a[212:215], v[164:167]\n"
    "buffer_load_dwordx4 v207, s[48:51], s59 offen lds\n"
    "v_cvt_pk_bf16_f32 v224, v228, v229\n"
    "v_mfma_f32_16x16x32_bf16 v[8:11], a[248:251], a[192:195], v[8:11]\n"
    "ds_read_b64_tr_b16 a[220:221], v198 offset:1024\n"
    "v_cvt_pk_bf16_f32 v225, v230, v231\n"
    "v_mfma_f32_16x16x32_bf16 v[40:43], a[248:251], a[196:199], v[40:43]\n"
    "s_add_u32 m0, s69, 116736\n"
    "v_cvt_pk_bf16_f32 v226, v232, v233\n"
    "v_cvt_pk_bf16_f32 v227, v234, v235\n"
    "v_mfma_f32_16x16x32_bf16 v[72:75], a[248:251], a[200:203], v[72:75]\n"
    "ds_read_b64_tr_b16 a[222:223], v198 offset:1280\n"
    "buffer_store_dwordx4 v[224:227], v242, s[52:55], s73 offen offset:1024 sc1\n"
    "s_add_u32 s59, s61, 19072\n"
    "v_mfma_f32_16x16x32_bf16 v[104:107], a[248:251], a[204:207], v[104:107]\n"
    "buffer_load_dwordx4 v206, s[48:51], s59 offen lds\n"
    "v_accvgpr_read_b32 v228, a80\n"
    "v_mfma_f32_16x16x32_bf16 v[136:139], a[248:251], a[208:211], v[136:139]\n"
    "v_accvgpr_read_b32 v229, a81\n"
    "v_accvgpr_read_b32 v230, a82\n"
    "v_mfma_f32_16x16x32_bf16 v[168:171], a[248:251], a[212:215], v[168:171]\n"
    "s_add_u32 m0, s69, 117760\n"
    "v_accvgpr_read_b32 v231, a83\n"
    "v_accvgpr_read_b32 v232, a84\n"
    "s_waitcnt lgkmcnt(2)\n"
    "v_mfma_f32_16x16x32_bf16 v[12:15], a[252:255], a[192:195], v[12:15]\n"
    "ds_read_b64_tr_b16 a[248:249], v197 offset:1536\n"
    "s_add_u32 s59, s61, 28288\n"
    "v_mfma_f32_16x16x32_bf16 v[44:47], a[252:255], a[196:199], v[44:47]\n"
    "buffer_load_dwordx4 v207, s[48:51], s59 offen lds\n"
    "v_accvgpr_read_b32 v233, a85\n"
    "v_mfma_f32_16x16x32_bf16 v[76:79], a[252:255], a[200:203], v[76:79]\n"
    "ds_read_b64_tr_b16 a[250:251], v197 offset:1792\n"
    "v_accvgpr_read_b32 v234, a86\n"
    "v_mfma_f32_16x16x32_bf16 v[108:111], a[252:255], a[204:207], v[108:111]\n"
    "s_add_u32 m0, s69, 118784\n"
    "v_accvgpr_read_b32 v235, a87\n"
    "v_accvgpr_write_b32 a80, 0\n"
    "v_mfma_f32_16x16x32_bf16 v[140:143], a[252:255], a[208:211], v[140:143]\n"
    "s_add_u32 s59, s61, 37504\n"
    "v_accvgpr_write_b32 a81, 0\n"
    "v_accvgpr_write_b32 a82, 0\n"
    "v_mfma_f32_16x16x32_bf16 v[172:175], a[252:255], a[212:215], v[172:175]\n"
    "buffer_load_dwordx4 v206, s[48:51], s59 offen lds\n"
    "v_accvgpr_write_b32 a83, 0\n"
    "v_mfma_f32_16x16x32_bf16 v[16:19], a[216:219], a[192:195], v[16:19]\n"
    "ds_read_b64_tr_b16 a[252:253], v198 offset:1536\n"
    "v_accvgpr_write_b32 a84, 0\n"
    "v_mfma_f32_16x16x32_bf16 v[48:51], a[216:219], a[196:199], v[48:51]\n"
    "s_add_u32 m0, s69, 119808\n"
    "v_accvgpr_write_b32 a85, 0\n"
    "v_accvgpr_write_b32 a86, 0\n"
    "v_mfma_f32_16x16x32_bf16 v[80:83], a[216:219], a[200:203], v[80:83]\n"
    "ds_read_b64_tr_b16 a[254:255], v198 offset:1792\n"
    "s_add_u32 s59, s61, 46720\n"
    "v_mfma_f32_16x16x32_bf16 v[112:115], a[216:219], a[204:207], v[112:115]\n"
    "buffer_load_dwordx4 v207, s[48:51], s59 offen lds\n"
    "v_accvgpr_write_b32 a87, 0\n"
    "v_mfma_f32_16x16x32_bf16 v[144:147], a[216:219], a[208:211], v[144:147]\n"
    "v_mul_f32 v228, v216, v228\n"
    "v_mul_f32 v229, v216, v229\n"
    "v_mfma_f32_16x16x32_bf16 v[176:179], a[216:219], a[212:215], v[176:179]\n"
    "v_mul_f32 v230, v216, v230\n"
    "v_mul_f32 v231, v216, v231\n"
    "s_waitcnt lgkmcnt(2)\n"
    "v_mfma_f32_16x16x32_bf16 v[20:23], a[220:223], a[192:195], v[20:23]\n"
    "v_mul_f32 v232, v216, v232\n"
    "v_mul_f32 v233, v216, v233\n"
    "v_mfma_f32_16x16x32_bf16 v[52:55], a[220:223], a[196:199], v[52:55]\n"
    "v_mul_f32 v234, v216, v234\n"
    "v_mul_f32 v235, v216, v235\n"
    "v_mfma_f32_16x16x32_bf16 v[84:87], a[220:223], a[200:203], v[84:87]\n"
    "s_waitcnt vmcnt(13) lgkmcnt(0)\n"
    "s_barrier\n"
    "v_mfma_f32_16x16x32_bf16 v[116:119], a[220:223], a[204:207], v[116:119]\n"
    "ds_read_b64_tr_b16 a[216:217], v195 offset:0\n"
    "v_cvt_pk_bf16_f32 v220, v228, v229\n"
    "v_mfma_f32_16x16x32_bf16 v[148:151], a[220:223], a[208:211], v[148:151]\n"
    "ds_read_b64_tr_b16 a[218:219], v195 offset:256\n"
    "v_cvt_pk_bf16_f32 v221, v230, v231\n"
    "v_mfma_f32_16x16x32_bf16 v[180:183], a[220:223], a[212:215], v[180:183]\n"
    "ds_read_b128 a[224:227], v199 offset:25088\n"
    "v_cvt_pk_bf16_f32 v222, v232, v233\n"
    "v_mfma_f32_16x16x32_bf16 v[24:27], a[248:251], a[192:195], v[24:27]\n"
    "ds_read_b128 a[228:231], v199 offset:27136\n"
    "v_cvt_pk_bf16_f32 v223, v234, v235\n"
    "v_mfma_f32_16x16x32_bf16 v[56:59], a[248:251], a[196:199], v[56:59]\n"
    "ds_read_b128 a[232:235], v199 offset:29184\n"
    "buffer_store_dwordx4 v[220:223], v242, s[52:55], s73 offen offset:2048 sc1\n"
    "v_mfma_f32_16x16x32_bf16 v[88:91], a[248:251], a[200:203], v[88:91]\n"
    "ds_read_b128 a[236:239], v199 offset:31232\n"
    "v_accvgpr_read_b32 v228, a112\n"
    "v_mfma_f32_16x16x32_bf16 v[120:123], a[248:251], a[204:207], v[120:123]\n"
    "ds_read_b128 a[240:243], v199 offset:33280\n"
    "v_accvgpr_read_b32 v229, a113\n"
    "v_mfma_f32_16x16x32_bf16 v[152:155], a[248:251], a[208:211], v[152:155]\n"
    "ds_read_b128 a[244:247], v199 offset:35328\n"
    "s_add_u32 m0, s65, 98304\n"
    "v_mfma_f32_16x16x32_bf16 v[184:187], a[248:251], a[212:215], v[184:187]\n"
    "buffer_load_dwordx4 v204, s[44:47], s58 offen lds\n"
    "v_accvgpr_read_b32 v230, a114\n"
    "v_mfma_f32_16x16x32_bf16 v[28:31], a[252:255], a[192:195], v[28:31]\n"
    "ds_read_b64_tr_b16 a[220:221], v196 offset:0\n"
    "s_add_u32 s58, s58, 0x10000\n"
    "v_mfma_f32_16x16x32_bf16 v[60:63], a[252:255], a[196:199], v[60:63]\n"
    "ds_read_b64_tr_b16 a[222:223], v196 offset:256\n"
    "s_add_u32 m0, s65, 100352\n"
    "v_mfma_f32_16x16x32_bf16 v[92:95], a[252:255], a[200:203], v[92:95]\n"
    "buffer_load_dwordx4 v204, s[44:47], s58 offen lds\n"
    "v_accvgpr_read_b32 v231, a115\n"
    "v_mfma_f32_16x16x32_bf16 v[124:127], a[252:255], a[204:207], v[124:127]\n"
    "ds_read_b64_tr_b16 a[248:249], v195 offset:512\n"
    "s_add_u32 s58, s58, 0x10000\n"
    "v_mfma_f32_16x16x32_bf16 v[156:159], a[252:255], a[208:211], v[156:159]\n"
    "ds_read_b64_tr_b16 a[250:251], v195 offset:768\n"
    "s_add_u32 m0, s65, 102400\n"
    "v_mfma_f32_16x16x32_bf16 v[188:191], a[252:255], a[212:215], v[188:191]\n"
    "buffer_load_dwordx4 v205, s[44:47], s58 offen lds\n"
    "v_accvgpr_read_b32 v232, a116\n"
    "s_waitcnt lgkmcnt(4)\n"
    "v_mfma_f32_16x16x32_bf16 v[0:3], a[216:219], a[224:227], v[0:3]\n"
    "ds_read_b64_tr_b16 a[252:253], v196 offset:512\n"
    "s_add_u32 s58, s58, 0x10000\n"
    "v_mfma_f32_16x16x32_bf16 v[32:35], a[216:219], a[228:231], v[32:35]\n"
    "v_accvgpr_read_b32 v233, a117\n"
    "v_accvgpr_read_b32 v234, a118\n"
    "v_mfma_f32_16x16x32_bf16 v[64:67], a[216:219], a[232:235], v[64:67]\n"
    "ds_read_b64_tr_b16 a[254:255], v196 offset:768\n"
    "s_add_u32 m0, s65, 104448\n"
    "v_mfma_f32_16x16x32_bf16 v[96:99], a[216:219], a[236:239], v[96:99]\n"
    "buffer_load_dwordx4 v205, s[44:47], s58 offen lds\n"
    "v_accvgpr_read_b32 v235, a119\n"
    "v_mfma_f32_16x16x32_bf16 v[128:131], a[216:219], a[240:243], v[128:131]\n"
    "s_add_u32 s58, s58, 0x10000\n"
    "v_accvgpr_write_b32 a112, 0\n"
    "v_accvgpr_write_b32 a113, 0\n"
    "v_mfma_f32_16x16x32_bf16 v[160:163], a[216:219], a[244:247], v[160:163]\n"
    "v_accvgpr_write_b32 a114, 0\n"
    "v_accvgpr_write_b32 a115, 0\n"
    "s_waitcnt lgkmcnt(2)\n"
    "v_mfma_f32_16x16x32_bf16 v[4:7], a[220:223], a[224:227], v[4:7]\n"
    "ds_read_b64_tr_b16 a[216:217], v195 offset:1024\n"
    "v_accvgpr_write_b32 a116, 0\n"
    "v_mfma_f32_16x16x32_bf16 v[36:39], a[220:223], a[228:231], v[36:39]\n"
    "v_accvgpr_write_b32 a117, 0\n"
    "v_accvgpr_write_b32 a118, 0\n"
    "v_mfma_f32_16x16x32_bf16 v[68:71], a[220:223], a[232:235], v[68:71]\n"
    "ds_read_b64_tr_b16 a[218:219], v195 offset:1280\n"
    "v_accvgpr_write_b32 a119, 0\n"
    "v_mfma_f32_16x16x32_bf16 v[100:103], a[220:223], a[236:239], v[100:103]\n"
    "v_mul_f32 v228, v217, v228\n"
    "v_mul_f32 v229, v217, v229\n"
    "v_mfma_f32_16x16x32_bf16 v[132:135], a[220:223], a[240:243], v[132:135]\n"
    "v_mul_f32 v230, v217, v230\n"
    "v_mul_f32 v231, v217, v231\n"
    "v_mfma_f32_16x16x32_bf16 v[164:167], a[220:223], a[244:247], v[164:167]\n"
    "v_mul_f32 v232, v217, v232\n"
    "v_mul_f32 v233, v217, v233\n"
    "v_mfma_f32_16x16x32_bf16 v[8:11], a[248:251], a[224:227], v[8:11]\n"
    "ds_read_b64_tr_b16 a[220:221], v196 offset:1024\n"
    "v_mul_f32 v234, v217, v234\n"
    "v_mfma_f32_16x16x32_bf16 v[40:43], a[248:251], a[228:231], v[40:43]\n"
    "v_mul_f32 v235, v217, v235\n"
    "v_cvt_pk_bf16_f32 v224, v228, v229\n"
    "v_mfma_f32_16x16x32_bf16 v[72:75], a[248:251], a[232:235], v[72:75]\n"
    "ds_read_b64_tr_b16 a[222:223], v196 offset:1280\n"
    "v_cvt_pk_bf16_f32 v225, v230, v231\n"
    "v_mfma_f32_16x16x32_bf16 v[104:107], a[248:251], a[236:239], v[104:107]\n"
    "v_cvt_pk_bf16_f32 v226, v232, v233\n"
    "v_cvt_pk_bf16_f32 v227, v234, v235\n"
    "v_mfma_f32_16x16x32_bf16 v[136:139], a[248:251], a[240:243], v[136:139]\n"
    "buffer_store_dwordx4 v[224:227], v242, s[52:55], s73 offen offset:3072 sc1\n"
    "v_mfma_f32_16x16x32_bf16 v[168:171], a[248:251], a[244:247], v[168:171]\n"
    "s_add_u32 s73, s72, 16384\n"
    "v_accvgpr_read_b32 v228, a144\n"
    "v_accvgpr_read_b32 v229, a145\n"
    "s_waitcnt lgkmcnt(2)\n"
    "v_mfma_f32_16x16x32_bf16 v[12:15], a[252:255], a[224:227], v[12:15]\n"
    "ds_read_b64_tr_b16 a[248:249], v195 offset:1536\n"
    "v_accvgpr_read_b32 v230, a146\n"
    "v_mfma_f32_16x16x32_bf16 v[44:47], a[252:255], a[228:231], v[44:47]\n"
    "v_accvgpr_read_b32 v231, a147\n"
    "v_accvgpr_read_b32 v232, a148\n"
    "v_mfma_f32_16x16x32_bf16 v[76:79], a[252:255], a[232:235], v[76:79]\n"
    "ds_read_b64_tr_b16 a[250:251], v195 offset:1792\n"
    "v_accvgpr_read_b32 v233, a149\n"
    "v_mfma_f32_16x16x32_bf16 v[108:111], a[252:255], a[236:239], v[108:111]\n"
    "v_accvgpr_read_b32 v234, a150\n"
    "v_accvgpr_read_b32 v235, a151\n"
    "v_mfma_f32_16x16x32_bf16 v[140:143], a[252:255], a[240:243], v[140:143]\n"
    "v_accvgpr_write_b32 a144, 0\n"
    "v_accvgpr_write_b32 a145, 0\n"
    "v_mfma_f32_16x16x32_bf16 v[172:175], a[252:255], a[244:247], v[172:175]\n"
    "v_accvgpr_write_b32 a146, 0\n"
    "v_accvgpr_write_b32 a147, 0\n"
    "v_mfma_f32_16x16x32_bf16 v[16:19], a[216:219], a[224:227], v[16:19]\n"
    "ds_read_b64_tr_b16 a[252:253], v196 offset:1536\n"
    "v_accvgpr_write_b32 a148, 0\n"
    "v_mfma_f32_16x16x32_bf16 v[48:51], a[216:219], a[228:231], v[48:51]\n"
    "v_accvgpr_write_b32 a149, 0\n"
    "v_accvgpr_write_b32 a150, 0\n"
    "v_mfma_f32_16x16x32_bf16 v[80:83], a[216:219], a[232:235], v[80:83]\n"
    "ds_read_b64_tr_b16 a[254:255], v196 offset:1792\n"
    "v_accvgpr_write_b32 a151, 0\n"
    "v_mfma_f32_16x16x32_bf16 v[112:115], a[216:219], a[236:239], v[112:115]\n"
    "v_mul_f32 v228, v218, v228\n"
    "v_mul_f32 v229, v218, v229\n"
    "v_mfma_f32_16x16x32_bf16 v[144:147], a[216:219], a[240:243], v[144:147]\n"
    "v_mul_f32 v230, v218, v230\n"
    "v_mul_f32 v231, v218, v231\n"
    "v_mfma_f32_16x16x32_bf16 v[176:179], a[216:219], a[244:247], v[176:179]\n"
    "v_mul_f32 v232, v218, v232\n"
    "v_mul_f32 v233, v218, v233\n"
    "s_waitcnt lgkmcnt(2)\n"
    "v_mfma_f32_16x16x32_bf16 v[20:23], a[220:223], a[224:227], v[20:23]\n"
    "v_mul_f32 v234, v218, v234\n"
    "v_mul_f32 v235, v218, v235\n"
    "v_mfma_f32_16x16x32_bf16 v[52:55], a[220:223], a[228:231], v[52:55]\n"
    "v_cvt_pk_bf16_f32 v220, v228, v229\n"
    "v_cvt_pk_bf16_f32 v221, v230, v231\n"
    "v_mfma_f32_16x16x32_bf16 v[84:87], a[220:223], a[232:235], v[84:87]\n"
    "s_waitcnt vmcnt(6) lgkmcnt(0)\n"
    "s_barrier\n"
    "v_mfma_f32_16x16x32_bf16 v[116:119], a[220:223], a[236:239], v[116:119]\n"
    "ds_read_b64_tr_b16 a[216:217], v195 offset:16384\n"
    "v_cvt_pk_bf16_f32 v222, v232, v233\n"
    "v_mfma_f32_16x16x32_bf16 v[148:151], a[220:223], a[240:243], v[148:151]\n"
    "ds_read_b64_tr_b16 a[218:219], v195 offset:16640\n"
    "v_cvt_pk_bf16_f32 v223, v234, v235\n"
    "v_mfma_f32_16x16x32_bf16 v[180:183], a[220:223], a[244:247], v[180:183]\n"
    "ds_read_b128 a[192:195], v199 offset:0\n"
    "buffer_store_dwordx4 v[220:223], v242, s[52:55], s73 offen sc1\n"
    "v_mfma_f32_16x16x32_bf16 v[24:27], a[248:251], a[224:227], v[24:27]\n"
    "ds_read_b128 a[196:199], v199 offset:2048\n"
    "v_accvgpr_read_b32 v228, a176\n"
    "v_mfma_f32_16x16x32_bf16 v[56:59], a[248:251], a[228:231], v[56:59]\n"
    "ds_read_b128 a[200:203], v199 offset:4096\n"
    "v_accvgpr_read_b32 v229, a177\n"
    "v_mfma_f32_16x16x32_bf16 v[88:91], a[248:251], a[232:235], v[88:91]\n"
    "ds_read_b128 a[204:207], v199 offset:6144\n"
    "v_accvgpr_read_b32 v230, a178\n"
    "v_mfma_f32_16x16x32_bf16 v[120:123], a[248:251], a[236:239], v[120:123]\n"
    "ds_read_b128 a[208:211], v199 offset:8192\n"
    "v_accvgpr_read_b32 v231, a179\n"
    "v_mfma_f32_16x16x32_bf16 v[152:155], a[248:251], a[240:243], v[152:155]\n"
    "ds_read_b128 a[212:215], v199 offset:10240\n"
    "s_add_u32 m0, s65, 32768\n"
    "v_mfma_f32_16x16x32_bf16 v[184:187], a[248:251], a[244:247], v[184:187]\n"
    "buffer_load_dwordx4 v204, s[44:47], s58 offen lds\n"
    "v_accvgpr_read_b32 v232, a180\n"
    "v_mfma_f32_16x16x32_bf16 v[28:31], a[252:255], a[224:227], v[28:31]\n"
    "ds_read_b64_tr_b16 a[220:221], v196 offset:16384\n"
    "s_add_u32 s58, s58, 0x10000\n"
    "v_mfma_f32_16x16x32_bf16 v[60:63], a[252:255], a[228:231], v[60:63]\n"
    "ds_read_b64_tr_b16 a[222:223], v196 offset:16640\n"
    "s_add_u32 m0, s65, 34816\n"
    "v_mfma_f32_16x16x32_bf16 v[92:95], a[252:255], a[232:235], v[92:95]\n"
    "buffer_load_dwordx4 v204, s[44:47], s58 offen lds\n"
    "v_accvgpr_read_b32 v233, a181\n"
    "v_mfma_f32_16x16x32_bf16 v[124:127], a[252:255], a[236:239], v[124:127]\n"
    "ds_read_b64_tr_b16 a[248:249], v195 offset:16896\n"
    "s_add_u32 s58, s58, 0x10000\n"
    "v_mfma_f32_16x16x32_bf16 v[156:159], a[252:255], a[240:243], v[156:159]\n"
    "ds_read_b64_tr_b16 a[250:251], v195 offset:17152\n"
    "s_add_u32 m0, s65, 36864\n"
    "v_mfma_f32_16x16x32_bf16 v[188:191], a[252:255], a[244:247], v[188:191]\n"
    "buffer_load_dwordx4 v205, s[44:47], s58 offen lds\n"
    "v_accvgpr_read_b32 v234, a182\n"
    "s_waitcnt lgkmcnt(4)\n"
    "v_mfma_f32_16x16x32_bf16 v[0:3], a[216:219], a[192:195], v[0:3]\n"
    "ds_read_b64_tr_b16 a[252:253], v196 offset:16896\n"
    "s_add_u32 s58, s58, 0x10000\n"
    "v_mfma_f32_16x16x32_bf16 v[32:35], a[216:219], a[196:199], v[32:35]\n"
    "v_accvgpr_read_b32 v235, a183\n"
    "v_accvgpr_write_b32 a176, 0\n"
    "v_mfma_f32_16x16x32_bf16 v[64:67], a[216:219], a[200:203], v[64:67]\n"
    "ds_read_b64_tr_b16 a[254:255], v196 offset:17152\n"
    "s_add_u32 m0, s65, 38912\n"
    "v_mfma_f32_16x16x32_bf16 v[96:99], a[216:219], a[204:207], v[96:99]\n"
    "buffer_load_dwordx4 v205, s[44:47], s58 offen lds\n"
    "v_accvgpr_write_b32 a177, 0\n"
    "v_mfma_f32_16x16x32_bf16 v[128:131], a[216:219], a[208:211], v[128:131]\n"
    "s_add_u32 s58, s58, 0x10000\n"
    "v_accvgpr_write_b32 a178, 0\n"
    "v_accvgpr_write_b32 a179, 0\n"
    "v_mfma_f32_16x16x32_bf16 v[160:163], a[216:219], a[212:215], v[160:163]\n"
    "s_add_u32 m0, s69, 139264\n"
    "v_accvgpr_write_b32 a180, 0\n"
    "v_accvgpr_write_b32 a181, 0\n"
    "s_waitcnt lgkmcnt(2)\n"
    "v_mfma_f32_16x16x32_bf16 v[4:7], a[220:223], a[192:195], v[4:7]\n"
    "ds_read_b64_tr_b16 a[216:217], v195 offset:17408\n"
    "s_add_u32 s59, s61, 768\n"
    "v_mfma_f32_16x16x32_bf16 v[36:39], a[220:223], a[196:199], v[36:39]\n"
    "buffer_load_dwordx4 v206, s[48:51], s59 offen lds\n"
    "v_accvgpr_write_b32 a182, 0\n"
    "v_mfma_f32_16x16x32_bf16 v[68:71], a[220:223], a[200:203], v[68:71]\n"
    "ds_read_b64_tr_b16 a[218:219], v195 offset:17664\n"
    "v_accvgpr_write_b32 a183, 0\n"
    "v_mfma_f32_16x16x32_bf16 v[100:103], a[220:223], a[204:207], v[100:103]\n"
    "s_add_u32 m0, s69, 140288\n"
    "v_mul_f32 v228, v219, v228\n"
    "v_mul_f32 v229, v219, v229\n"
    "v_mfma_f32_16x16x32_bf16 v[132:135], a[220:223], a[208:211], v[132:135]\n"
    "s_add_u32 s59, s61, 9984\n"
    "v_mul_f32 v230, v219, v230\n"
    "v_mul_f32 v231, v219, v231\n"
    "v_mfma_f32_16x16x32_bf16 v[164:167], a[220:223], a[212:215], v[164:167]\n"
    "buffer_load_dwordx4 v207, s[48:51], s59 offen lds\n"
    "v_mul_f32 v232, v219, v232\n"
    "v_mfma_f32_16x16x32_bf16 v[8:11], a[248:251], a[192:195], v[8:11]\n"
    "ds_read_b64_tr_b16 a[220:221], v196 offset:17408\n"
    "v_mul_f32 v233, v219, v233\n"
    "v_mfma_f32_16x16x32_bf16 v[40:43], a[248:251], a[196:199], v[40:43]\n"
    "s_add_u32 m0, s69, 141312\n"
    "v_mul_f32 v234, v219, v234\n"
    "v_mul_f32 v235, v219, v235\n"
    "v_mfma_f32_16x16x32_bf16 v[72:75], a[248:251], a[200:203], v[72:75]\n"
    "ds_read_b64_tr_b16 a[222:223], v196 offset:17664\n"
    "s_add_u32 s59, s61, 19200\n"
    "v_mfma_f32_16x16x32_bf16 v[104:107], a[248:251], a[204:207], v[104:107]\n"
    "buffer_load_dwordx4 v206, s[48:51], s59 offen lds\n"
    "v_cvt_pk_bf16_f32 v224, v228, v229\n"
    "v_mfma_f32_16x16x32_bf16 v[136:139], a[248:251], a[208:211], v[136:139]\n"
    "v_cvt_pk_bf16_f32 v225, v230, v231\n"
    "v_cvt_pk_bf16_f32 v226, v232, v233\n"
    "v_mfma_f32_16x16x32_bf16 v[168:171], a[248:251], a[212:215], v[168:171]\n"
    "s_add_u32 m0, s69, 142336\n"
    "v_cvt_pk_bf16_f32 v227, v234, v235\n"
    "s_waitcnt lgkmcnt(2)\n"
    "v_mfma_f32_16x16x32_bf16 v[12:15], a[252:255], a[192:195], v[12:15]\n"
    "ds_read_b64_tr_b16 a[248:249], v195 offset:17920\n"
    "buffer_store_dwordx4 v[224:227], v242, s[52:55], s73 offen offset:1024 sc1\n"
    "s_add_u32 s59, s61, 28416\n"
    "v_mfma_f32_16x16x32_bf16 v[44:47], a[252:255], a[196:199], v[44:47]\n"
    "buffer_load_dwordx4 v207, s[48:51], s59 offen lds\n"
    "s_add_u32 s73, s72, 18432\n"
    "v_mfma_f32_16x16x32_bf16 v[76:79], a[252:255], a[200:203], v[76:79]\n"
    "ds_read_b64_tr_b16 a[250:251], v195 offset:18176\n"
    "v_accvgpr_read_b32 v228, a24\n"
    "v_mfma_f32_16x16x32_bf16 v[108:111], a[252:255], a[204:207], v[108:111]\n"
    "s_add_u32 m0, s69, 143360\n"
    "v_accvgpr_read_b32 v229, a25\n"
    "v_accvgpr_read_b32 v230, a26\n"
    "v_mfma_f32_16x16x32_bf16 v[140:143], a[252:255], a[208:211], v[140:143]\n"
    "s_add_u32 s59, s61, 37632\n"
    "v_accvgpr_read_b32 v231, a27\n"
    "v_accvgpr_read_b32 v232, a28\n"
    "v_mfma_f32_16x16x32_bf16 v[172:175], a[252:255], a[212:215], v[172:175]\n"
    "buffer_load_dwordx4 v206, s[48:51], s59 offen lds\n"
    "v_accvgpr_read_b32 v233, a29\n"
    "v_mfma_f32_16x16x32_bf16 v[16:19], a[216:219], a[192:195], v[16:19]\n"
    "ds_read_b64_tr_b16 a[252:253], v196 offset:17920\n"
    "v_accvgpr_read_b32 v234, a30\n"
    "v_mfma_f32_16x16x32_bf16 v[48:51], a[216:219], a[196:199], v[48:51]\n"
    "s_add_u32 m0, s69, 144384\n"
    "v_accvgpr_read_b32 v235, a31\n"
    "v_accvgpr_write_b32 a24, 0\n"
    "v_mfma_f32_16x16x32_bf16 v[80:83], a[216:219], a[200:203], v[80:83]\n"
    "ds_read_b64_tr_b16 a[254:255], v196 offset:18176\n"
    "s_add_u32 s59, s61, 46848\n"
    "v_mfma_f32_16x16x32_bf16 v[112:115], a[216:219], a[204:207], v[112:115]\n"
    "buffer_load_dwordx4 v207, s[48:51], s59 offen lds\n"
    "v_accvgpr_write_b32 a25, 0\n"
    "v_mfma_f32_16x16x32_bf16 v[144:147], a[216:219], a[208:211], v[144:147]\n"
    "v_accvgpr_write_b32 a26, 0\n"
    "v_accvgpr_write_b32 a27, 0\n"
    "v_mfma_f32_16x16x32_bf16 v[176:179], a[216:219], a[212:215], v[176:179]\n"
    "v_accvgpr_write_b32 a28, 0\n"
    "v_accvgpr_write_b32 a29, 0\n"
    "s_waitcnt lgkmcnt(2)\n"
    "v_mfma_f32_16x16x32_bf16 v[20:23], a[220:223], a[192:195], v[20:23]\n"
    "v_accvgpr_write_b32 a30, 0\n"
    "v_accvgpr_write_b32 a31, 0\n"
    "v_mfma_f32_16x16x32_bf16 v[52:55], a[220:223], a[196:199], v[52:55]\n"
    "v_mul_f32 v228, v214, v228\n"
    "v_mul_f32 v229, v214, v229\n"
    "v_mfma_f32_16x16x32_bf16 v[84:87], a[220:223], a[200:203], v[84:87]\n"
    "s_waitcnt vmcnt(13) lgkmcnt(0)\n"
    "s_barrier\n"
    "v_mfma_f32_16x16x32_bf16 v[116:119], a[220:223], a[204:207], v[116:119]\n"
    "ds_read_b64_tr_b16 a[216:217], v197 offset:0\n"
    "v_mul_f32 v230, v214, v230\n"
    "v_mfma_f32_16x16x32_bf16 v[148:151], a[220:223], a[208:211], v[148:151]\n"
    "ds_read_b64_tr_b16 a[218:219], v197 offset:256\n"
    "v_mul_f32 v231, v214, v231\n"
    "v_mfma_f32_16x16x32_bf16 v[180:183], a[220:223], a[212:215], v[180:183]\n"
    "ds_read_b128 a[224:227], v199 offset:512\n"
    "v_mul_f32 v232, v214, v232\n"
    "v_mfma_f32_16x16x32_bf16 v[24:27], a[248:251], a[192:195], v[24:27]\n"
    "ds_read_b128 a[228:231], v199 offset:2560\n"
    "v_mul_f32 v233, v214, v233\n"
    "v_mfma_f32_16x16x32_bf16 v[56:59], a[248:251], a[196:199], v[56:59]\n"
    "ds_read_b128 a[232:235], v199 offset:4608\n"
    "v_mul_f32 v234, v214, v234\n"
    "v_mfma_f32_16x16x32_bf16 v[88:91], a[248:251], a[200:203], v[88:91]\n"
    "ds_read_b128 a[236:239], v199 offset:6656\n"
    "v_mul_f32 v235, v214, v235\n"
    "v_mfma_f32_16x16x32_bf16 v[120:123], a[248:251], a[204:207], v[120:123]\n"
    "ds_read_b128 a[240:243], v199 offset:8704\n"
    "v_cvt_pk_bf16_f32 v220, v228, v229\n"
    "v_mfma_f32_16x16x32_bf16 v[152:155], a[248:251], a[208:211], v[152:155]\n"
    "ds_read_b128 a[244:247], v199 offset:10752\n"
    "s_add_u32 m0, s65, 49152\n"
    "v_mfma_f32_16x16x32_bf16 v[184:187], a[248:251], a[212:215], v[184:187]\n"
    "buffer_load_dwordx4 v204, s[44:47], s58 offen lds\n"
    "v_cvt_pk_bf16_f32 v221, v230, v231\n"
    "v_mfma_f32_16x16x32_bf16 v[28:31], a[252:255], a[192:195], v[28:31]\n"
    "ds_read_b64_tr_b16 a[220:221], v198 offset:0\n"
    "s_add_u32 s58, s58, 0x10000\n"
    "v_mfma_f32_16x16x32_bf16 v[60:63], a[252:255], a[196:199], v[60:63]\n"
    "ds_read_b64_tr_b16 a[222:223], v198 offset:256\n"
    "s_add_u32 m0, s65, 51200\n"
    "v_mfma_f32_16x16x32_bf16 v[92:95], a[252:255], a[200:203], v[92:95]\n"
    "buffer_load_dwordx4 v204, s[44:47], s58 offen lds\n"
    "v_cvt_pk_bf16_f32 v222, v232, v233\n"
    "v_mfma_f32_16x16x32_bf16 v[124:127], a[252:255], a[204:207], v[124:127]\n"
    "ds_read_b64_tr_b16 a[248:249], v197 offset:512\n"
    "s_add_u32 s58, s58, 0x10000\n"
    "v_mfma_f32_16x16x32_bf16 v[156:159], a[252:255], a[208:211], v[156:159]\n"
    "ds_read_b64_tr_b16 a[250:251], v197 offset:768\n"
    "s_add_u32 m0, s65, 53248\n"
    "v_mfma_f32_16x16x32_bf16 v[188:191], a[252:255], a[212:215], v[188:191]\n"
    "buffer_load_dwordx4 v205, s[44:47], s58 offen lds\n"
    "v_cvt_pk_bf16_f32 v223, v234, v235\n"
    "s_waitcnt lgkmcnt(4)\n"
    "v_mfma_f32_16x16x32_bf16 v[0:3], a[216:219], a[224:227], v[0:3]\n"
    "ds_read_b64_tr_b16 a[252:253], v198 offset:512\n"
    "buffer_store_dwordx4 v[220:223], v242, s[52:55], s73 offen sc1\n"
    "s_add_u32 s58, s58, 0x10000\n"
    "v_mfma_f32_16x16x32_bf16 v[32:35], a[216:219], a[228:231], v[32:35]\n"
    "v_accvgpr_read_b32 v228, a56\n"
    "v_accvgpr_read_b32 v229, a57\n"
    "v_mfma_f32_16x16x32_bf16 v[64:67], a[216:219], a[232:235], v[64:67]\n"
    "ds_read_b64_tr_b16 a[254:255], v198 offset:768\n"
    "s_add_u32 m0, s65, 55296\n"
    "v_mfma_f32_16x16x32_bf16 v[96:99], a[216:219], a[236:239], v[96:99]\n"
    "buffer_load_dwordx4 v205, s[44:47], s58 offen lds\n"
    "v_accvgpr_read_b32 v230, a58\n"
    "v_mfma_f32_16x16x32_bf16 v[128:131], a[216:219], a[240:243], v[128:131]\n"
    "s_add_u32 s58, s58, 0x10000\n"
    "v_accvgpr_read_b32 v231, a59\n"
    "v_accvgpr_read_b32 v232, a60\n"
    "v_mfma_f32_16x16x32_bf16 v[160:163], a[216:219], a[244:247], v[160:163]\n"
    "v_accvgpr_read_b32 v233, a61\n"
    "v_accvgpr_read_b32 v234, a62\n"
    "s_waitcnt lgkmcnt(2)\n"
    "v_mfma_f32_16x16x32_bf16 v[4:7], a[220:223], a[224:227], v[4:7]\n"
    "ds_read_b64_tr_b16 a[216:217], v197 offset:1024\n"
    "v_accvgpr_read_b32 v235, a63\n"
    "v_mfma_f32_16x16x32_bf16 v[36:39], a[220:223], a[228:231], v[36:39]\n"
    "v_accvgpr_write_b32 a56, 0\n"
    "v_accvgpr_write_b32 a57, 0\n"
    "v_mfma_f32_16x16x32_bf16 v[68:71], a[220:223], a[232:235], v[68:71]\n"
    "ds_read_b64_tr_b16 a[218:219], v197 offset:1280\n"
    "v_accvgpr_write_b32 a58, 0\n"
    "v_mfma_f32_16x16x32_bf16 v[100:103], a[220:223], a[236:239], v[100:103]\n"
    "v_accvgpr_write_b32 a59, 0\n"
    "v_accvgpr_write_b32 a60, 0\n"
    "v_mfma_f32_16x16x32_bf16 v[132:135], a[220:223], a[240:243], v[132:135]\n"
    "v_accvgpr_write_b32 a61, 0\n"
    "v_accvgpr_write_b32 a62, 0\n"
    "v_mfma_f32_16x16x32_bf16 v[164:167], a[220:223], a[244:247], v[164:167]\n"
    "v_accvgpr_write_b32 a63, 0\n"
    "v_mul_f32 v228, v215, v228\n"
    "v_mfma_f32_16x16x32_bf16 v[8:11], a[248:251], a[224:227], v[8:11]\n"
    "ds_read_b64_tr_b16 a[220:221], v198 offset:1024\n"
    "v_mul_f32 v229, v215, v229\n"
    "v_mfma_f32_16x16x32_bf16 v[40:43], a[248:251], a[228:231], v[40:43]\n"
    "v_mul_f32 v230, v215, v230\n"
    "v_mul_f32 v231, v215, v231\n"
    "v_mfma_f32_16x16x32_bf16 v[72:75], a[248:251], a[232:235], v[72:75]\n"
    "ds_read_b64_tr_b16 a[222:223], v198 offset:1280\n"
    "v_mul_f32 v232, v215, v232\n"
    "v_mfma_f32_16x16x32_bf16 v[104:107], a[248:251], a[236:239], v[104:107]\n"
    "v_mul_f32 v233, v215, v233\n"
    "v_mul_f32 v234, v215, v234\n"
    "v_mfma_f32_16x16x32_bf16 v[136:139], a[248:251], a[240:243], v[136:139]\n"
    "v_mul_f32 v235, v215, v235\n"
    "v_cvt_pk_bf16_f32 v224, v228, v229\n"
    "v_mfma_f32_16x16x32_bf16 v[168:171], a[248:251], a[244:247], v[168:171]\n"
    "v_cvt_pk_bf16_f32 v225, v230, v231\n"
    "v_cvt_pk_bf16_f32 v226, v232, v233\n"
    "s_waitcnt lgkmcnt(2)\n"
    "v_mfma_f32_16x16x32_bf16 v[12:15], a[252:255], a[224:227], v[12:15]\n"
    "ds_read_b64_tr_b16 a[248:249], v197 offset:1536\n"
    "v_cvt_pk_bf16_f32 v227, v234, v235\n"
    "v_mfma_f32_16x16x32_bf16 v[44:47], a[252:255], a[228:231], v[44:47]\n"
    "buffer_store_dwordx4 v[224:227], v242, s[52:55], s73 offen offset:1024 sc1\n"
    "v_accvgpr_read_b32 v228, a88\n"
    "v_mfma_f32_16x16x32_bf16 v[76:79], a[252:255], a[232:235], v[76:79]\n"
    "ds_read_b64_tr_b16 a[250:251], v197 offset:1792\n"
    "v_accvgpr_read_b32 v229, a89\n"
    "v_mfma_f32_16x16x32_bf16 v[108:111], a[252:255], a[236:239], v[108:111]\n"
    "v_accvgpr_read_b32 v230, a90\n"
    "v_accvgpr_read_b32 v231, a91\n"
    "v_mfma_f32_16x16x32_bf16 v[140:143], a[252:255], a[240:243], v[140:143]\n"
    "v_accvgpr_read_b32 v232, a92\n"
    "v_accvgpr_read_b32 v233, a93\n"
    "v_mfma_f32_16x16x32_bf16 v[172:175], a[252:255], a[244:247], v[172:175]\n"
    "v_accvgpr_read_b32 v234, a94\n"
    "v_accvgpr_read_b32 v235, a95\n"
    "v_mfma_f32_16x16x32_bf16 v[16:19], a[216:219], a[224:227], v[16:19]\n"
    "ds_read_b64_tr_b16 a[252:253], v198 offset:1536\n"
    "v_accvgpr_write_b32 a88, 0\n"
    "v_mfma_f32_16x16x32_bf16 v[48:51], a[216:219], a[228:231], v[48:51]\n"
    "v_accvgpr_write_b32 a89, 0\n"
    "v_accvgpr_write_b32 a90, 0\n"
    "v_mfma_f32_16x16x32_bf16 v[80:83], a[216:219], a[232:235], v[80:83]\n"
    "ds_read_b64_tr_b16 a[254:255], v198 offset:1792\n"
    "v_accvgpr_write_b32 a91, 0\n"
    "v_mfma_f32_16x16x32_bf16 v[112:115], a[216:219], a[236:239], v[112:115]\n"
    "v_accvgpr_write_b32 a92, 0\n"
    "v_accvgpr_write_b32 a93, 0\n"
    "v_mfma_f32_16x16x32_bf16 v[144:147], a[216:219], a[240:243], v[144:147]\n"
    "v_accvgpr_write_b32 a94, 0\n"
    "v_accvgpr_write_b32 a95, 0\n"
    "v_mfma_f32_16x16x32_bf16 v[176:179], a[216:219], a[244:247], v[176:179]\n"
    "v_mul_f32 v228, v216, v228\n"
    "v_mul_f32 v229, v216, v229\n"
    "s_waitcnt lgkmcnt(2)\n"
    "v_mfma_f32_16x16x32_bf16 v[20:23], a[220:223], a[224:227], v[20:23]\n"
    "v_mul_f32 v230, v216, v230\n"
    "v_mul_f32 v231, v216, v231\n"
    "v_mfma_f32_16x16x32_bf16 v[52:55], a[220:223], a[228:231], v[52:55]\n"
    "v_mul_f32 v232, v216, v232\n"
    "v_mul_f32 v233, v216, v233\n"
    "v_mfma_f32_16x16x32_bf16 v[84:87], a[220:223], a[232:235], v[84:87]\n"
    "s_waitcnt vmcnt(6) lgkmcnt(0)\n"
    "s_barrier\n"
    "v_mfma_f32_16x16x32_bf16 v[116:119], a[220:223], a[236:239], v[116:119]\n"
    "ds_read_b64_tr_b16 a[216:217], v195 offset:0\n"
    "v_mul_f32 v234, v216, v234\n"
    "v_mfma_f32_16x16x32_bf16 v[148:151], a[220:223], a[240:243], v[148:151]\n"
    "ds_read_b64_tr_b16 a[218:219], v195 offset:256\n"
    "v_mul_f32 v235, v216, v235\n"
    "v_mfma_f32_16x16x32_bf16 v[180:183], a[220:223], a[244:247], v[180:183]\n"
    "ds_read_b128 a[192:195], v199 offset:24576\n"
    "v_cvt_pk_bf16_f32 v220, v228, v229\n"
    "v_mfma_f32_16x16x32_bf16 v[24:27], a[248:251], a[224:227], v[24:27]\n"
    "ds_read_b128 a[196:199], v199 offset:26624\n"
    "v_cvt_pk_bf16_f32 v221, v230, v231\n"
    "v_mfma_f32_16x16x32_bf16 v[56:59], a[248:251], a[228:231], v[56:59]\n"
    "ds_read_b128 a[200:203], v199 offset:28672\n"
    "v_cvt_pk_bf16_f32 v222, v232, v233\n"
    "v_mfma_f32_16x16x32_bf16 v[88:91], a[248:251], a[232:235], v[88:91]\n"
    "ds_read_b128 a[204:207], v199 offset:30720\n"
    "v_cvt_pk_bf16_f32 v223, v234, v235\n"
    "v_mfma_f32_16x16x32_bf16 v[120:123], a[248:251], a[236:239], v[120:123]\n"
    "ds_read_b128 a[208:211], v199 offset:32768\n"
    "buffer_store_dwordx4 v[220:223], v242, s[52:55], s73 offen offset:2048 sc1\n"
    "v_mfma_f32_16x16x32_bf16 v[152:155], a[248:251], a[240:243], v[152:155]\n"
    "ds_read_b128 a[212:215], v199 offset:34816\n"
    "s_add_u32 m0, s65, 98304\n"
    "v_mfma_f32_16x16x32_bf16 v[184:187], a[248:251], a[244:247], v[184:187]\n"
    "buffer_load_dwordx4 v204, s[44:47], s58 offen lds\n"
    "v_accvgpr_read_b32 v228, a120\n"
    "v_mfma_f32_16x16x32_bf16 v[28:31], a[252:255], a[224:227], v[28:31]\n"
    "ds_read_b64_tr_b16 a[220:221], v196 offset:0\n"
    "s_add_u32 s58, s58, 0x10000\n"
    "v_mfma_f32_16x16x32_bf16 v[60:63], a[252:255], a[228:231], v[60:63]\n"
    "ds_read_b64_tr_b16 a[222:223], v196 offset:256\n"
    "s_add_u32 m0, s65, 100352\n"
    "v_mfma_f32_16x16x32_bf16 v[92:95], a[252:255], a[232:235], v[92:95]\n"
    "buffer_load_dwordx4 v204, s[44:47], s58 offen lds\n"
    "v_accvgpr_read_b32 v229, a121\n"
    "v_mfma_f32_16x16x32_bf16 v[124:127], a[252:255], a[236:239], v[124:127]\n"
    "ds_read_b64_tr_b16 a[248:249], v195 offset:512\n"
    "s_add_u32 s58, s58, 0x10000\n"
    "v_mfma_f32_16x16x32_bf16 v[156:159], a[252:255], a[240:243], v[156:159]\n"
    "ds_read_b64_tr_b16 a[250:251], v195 offset:768\n"
    "s_add_u32 m0, s65, 102400\n"
    "v_mfma_f32_16x16x32_bf16 v[188:191], a[252:255], a[244:247], v[188:191]\n"
    "buffer_load_dwordx4 v205, s[44:47], s58 offen lds\n"
    "v_accvgpr_read_b32 v230, a122\n"
    "s_waitcnt lgkmcnt(4)\n"
    "v_mfma_f32_16x16x32_bf16 v[0:3], a[216:219], a[192:195], v[0:3]\n"
    "ds_read_b64_tr_b16 a[252:253], v196 offset:512\n"
    "s_add_u32 s58, s58, 0x10000\n"
    "v_mfma_f32_16x16x32_bf16 v[32:35], a[216:219], a[196:199], v[32:35]\n"
    "v_accvgpr_read_b32 v231, a123\n"
    "v_accvgpr_read_b32 v232, a124\n"
    "v_mfma_f32_16x16x32_bf16 v[64:67], a[216:219], a[200:203], v[64:67]\n"
    "ds_read_b64_tr_b16 a[254:255], v196 offset:768\n"
    "s_add_u32 m0, s65, 104448\n"
    "v_mfma_f32_16x16x32_bf16 v[96:99], a[216:219], a[204:207], v[96:99]\n"
    "buffer_load_dwordx4 v205, s[44:47], s58 offen lds\n"
    "v_accvgpr_read_b32 v233, a125\n"
    "v_mfma_f32_16x16x32_bf16 v[128:131], a[216:219], a[208:211], v[128:131]\n"
    "s_add_u32 s58, s58, 0x10000\n"
    "v_accvgpr_read_b32 v234, a126\n"
    "v_accvgpr_read_b32 v235, a127\n"
    "v_mfma_f32_16x16x32_bf16 v[160:163], a[216:219], a[212:215], v[160:163]\n"
    "s_add_u32 m0, s69, 114688\n"
    "v_accvgpr_write_b32 a120, 0\n"
    "v_accvgpr_write_b32 a121, 0\n"
    "s_waitcnt lgkmcnt(2)\n"
    "v_mfma_f32_16x16x32_bf16 v[4:7], a[220:223], a[192:195], v[4:7]\n"
    "ds_read_b64_tr_b16 a[216:217], v195 offset:1024\n"
    "s_add_u32 s59, s61, 896\n"
    "v_mfma_f32_16x16x32_bf16 v[36:39], a[220:223], a[196:199], v[36:39]\n"
    "buffer_load_dwordx4 v206, s[48:51], s59 offen lds\n"
    "v_accvgpr_write_b32 a122, 0\n"
    "v_mfma_f32_16x16x32_bf16 v[68:71], a[220:223], a[200:203], v[68:71]\n"
    "ds_read_b64_tr_b16 a[218:219], v195 offset:1280\n"
    "v_accvgpr_write_b32 a123, 0\n"
    "v_mfma_f32_16x16x32_bf16 v[100:103], a[220:223], a[204:207], v[100:103]\n"
    "s_add_u32 m0, s69, 115712\n"
    "v_accvgpr_write_b32 a124, 0\n"
    "v_accvgpr_write_b32 a125, 0\n"
    "v_mfma_f32_16x16x32_bf16 v[132:135], a[220:223], a[208:211], v[132:135]\n"
    "s_add_u32 s59, s61, 10112\n"
    "v_accvgpr_write_b32 a126, 0\n"
    "v_accvgpr_write_b32 a127, 0\n"
    "v_mfma_f32_16x16x32_bf16 v[164:167], a[220:223], a[212:215], v[164:167]\n"
    "buffer_load_dwordx4 v207, s[48:51], s59 offen lds\n"
    "v_mul_f32 v228, v217, v228\n"
    "v_mfma_f32_16x16x32_bf16 v[8:11], a[248:251], a[192:195], v[8:11]\n"
    "ds_read_b64_tr_b16 a[220:221], v196 offset:1024\n"
    "v_mul_f32 v229, v217, v229\n"
    "v_mfma_f32_16x16x32_bf16 v[40:43], a[248:251], a[196:199], v[40:43]\n"
    "s_add_u32 m0, s69, 116736\n"
    "v_mul_f32 v230, v217, v230\n"
    "v_mul_f32 v231, v217, v231\n"
    "v_mfma_f32_16x16x32_bf16 v[72:75], a[248:251], a[200:203], v[72:75]\n"
    "ds_read_b64_tr_b16 a[222:223], v196 offset:1280\n"
    "s_add_u32 s59, s61, 19328\n"
    "v_mfma_f32_16x16x32_bf16 v[104:107], a[248:251], a[204:207], v[104:107]\n"
    "buffer_load_dwordx4 v206, s[48:51], s59 offen lds\n"
    "v_mul_f32 v232, v217, v232\n"
    "v_mfma_f32_16x16x32_bf16 v[136:139], a[248:251], a[208:211], v[136:139]\n"
    "v_mul_f32 v233, v217, v233\n"
    "v_mfma_f32_16x16x32_bf16 v[168:171], a[248:251], a[212:215], v[168:171]\n"
    "s_add_u32 m0, s69, 117760\n"
    "v_mul_f32 v234, v217, v234\n"
    "s_waitcnt lgkmcnt(2)\n"
    "v_mfma_f32_16x16x32_bf16 v[12:15], a[252:255], a[192:195], v[12:15]\n"
    "ds_read_b64_tr_b16 a[248:249], v195 offset:1536\n"
    "s_add_u32 s59, s61, 28544\n"
    "v_mfma_f32_16x16x32_bf16 v[44:47], a[252:255], a[196:199], v[44:47]\n"
    "buffer_load_dwordx4 v207, s[48:51], s59 offen lds\n"
    "v_mul_f32 v235, v217, v235\n"
    "v_mfma_f32_16x16x32_bf16 v[76:79], a[252:255], a[200:203], v[76:79]\n"
    "ds_read_b64_tr_b16 a[250:251], v195 offset:1792\n"
    "v_cvt_pk_bf16_f32 v224, v228, v229\n"
    "v_mfma_f32_16x16x32_bf16 v[108:111], a[252:255], a[204:207], v[108:111]\n"
    "s_add_u32 m0, s69, 118784\n"
    "v_cvt_pk_bf16_f32 v225, v230, v231\n"
    "v_mfma_f32_16x16x32_bf16 v[140:143], a[252:255], a[208:211], v[140:143]\n"
    "s_add_u32 s59, s61, 37760\n"
    "v_cvt_pk_bf16_f32 v226, v232, v233\n"
    "v_mfma_f32_16x16x32_bf16 v[172:175], a[252:255], a[212:215], v[172:175]\n"
    "buffer_load_dwordx4 v206, s[48:51], s59 offen lds\n"
    "v_cvt_pk_bf16_f32 v227, v234, v235\n"
    "v_mfma_f32_16x16x32_bf16 v[16:19], a[216:219], a[192:195], v[16:19]\n"
    "ds_read_b64_tr_b16 a[252:253], v196 offset:1536\n"
    "buffer_store_dwordx4 v[224:227], v242, s[52:55], s73 offen offset:3072 sc1\n"
    "v_mfma_f32_16x16x32_bf16 v[48:51], a[216:219], a[196:199], v[48:51]\n"
    "s_add_u32 s73, s72, 22528\n"
    "s_add_u32 m0, s69, 119808\n"
    "v_accvgpr_read_b32 v228, a152\n"
    "v_mfma_f32_16x16x32_bf16 v[80:83], a[216:219], a[200:203], v[80:83]\n"
    "ds_read_b64_tr_b16 a[254:255], v196 offset:1792\n"
    "s_add_u32 s59, s61, 46976\n"
    "v_mfma_f32_16x16x32_bf16 v[112:115], a[216:219], a[204:207], v[112:115]\n"
    "buffer_load_dwordx4 v207, s[48:51], s59 offen lds\n"
    "v_accvgpr_read_b32 v229, a153\n"
    "v_mfma_f32_16x16x32_bf16 v[144:147], a[216:219], a[208:211], v[144:147]\n"
    "v_accvgpr_read_b32 v230, a154\n"
    "v_mfma_f32_16x16x32_bf16 v[176:179], a[216:219], a[212:215], v[176:179]\n"
    "v_accvgpr_read_b32 v231, a155\n"
    "s_waitcnt lgkmcnt(2)\n"
    "v_mfma_f32_16x16x32_bf16 v[20:23], a[220:223], a[192:195], v[20:23]\n"
    "v_accvgpr_read_b32 v232, a156\n"
    "v_mfma_f32_16x16x32_bf16 v[52:55], a[220:223], a[196:199], v[52:55]\n"
    "v_accvgpr_read_b32 v233, a157\n"
    "v_mfma_f32_16x16x32_bf16 v[84:87], a[220:223], a[200:203], v[84:87]\n"
    "s_waitcnt vmcnt(13) lgkmcnt(0)\n"
    "s_barrier\n"
    "v_mfma_f32_16x16x32_bf16 v[116:119], a[220:223], a[204:207], v[116:119]\n"
    "ds_read_b64_tr_b16 a[216:217], v195 offset:16384\n"
    "v_accvgpr_read_b32 v234, a158\n"
    "v_mfma_f32_16x16x32_bf16 v[148:151], a[220:223], a[208:211], v[148:151]\n"
    "ds_read_b64_tr_b16 a[218:219], v195 offset:16640\n"
    "v_accvgpr_read_b32 v235, a159\n"
    "v_mfma_f32_16x16x32_bf16 v[180:183], a[220:223], a[212:215], v[180:183]\n"
    "ds_read_b128 a[224:227], v199 offset:25088\n"
    "v_accvgpr_write_b32 a152, 0\n"
    "v_mfma_f32_16x16x32_bf16 v[24:27], a[248:251], a[192:195], v[24:27]\n"
    "ds_read_b128 a[228:231], v199 offset:27136\n"
    "v_accvgpr_write_b32 a153, 0\n"
    "v_mfma_f32_16x16x32_bf16 v[56:59], a[248:251], a[196:199], v[56:59]\n"
    "ds_read_b128 a[232:235], v199 offset:29184\n"
    "v_accvgpr_write_b32 a154, 0\n"
    "v_mfma_f32_16x16x32_bf16 v[88:91], a[248:251], a[200:203], v[88:91]\n"
    "ds_read_b128 a[236:239], v199 offset:31232\n"
    "v_accvgpr_write_b32 a155, 0\n"
    "v_mfma_f32_16x16x32_bf16 v[120:123], a[248:251], a[204:207], v[120:123]\n"
    "ds_read_b128 a[240:243], v199 offset:33280\n"
    "v_accvgpr_write_b32 a156, 0\n"
    "v_mfma_f32_16x16x32_bf16 v[152:155], a[248:251], a[208:211], v[152:155]\n"
    "ds_read_b128 a[244:247], v199 offset:35328\n"
    "s_add_u32 m0, s65, 32768\n"
    "v_mfma_f32_16x16x32_bf16 v[184:187], a[248:251], a[212:215], v[184:187]\n"
    "buffer_load_dwordx4 v204, s[44:47], s58 offen lds\n"
    "v_accvgpr_write_b32 a157, 0\n"
    "v_mfma_f32_16x16x32_bf16 v[28:31], a[252:255], a[192:195], v[28:31]\n"
    "ds_read_b64_tr_b16 a[220:221], v196 offset:16384\n"
    "s_add_u32 s58, s58, 0x10000\n"
    "v_mfma_f32_16x16x32_bf16 v[60:63], a[252:255], a[196:199], v[60:63]\n"
    "ds_read_b64_tr_b16 a[222:223], v196 offset:16640\n"
    "s_add_u32 m0, s65, 34816\n"
    "v_mfma_f32_16x16x32_bf16 v[92:95], a[252:255], a[200:203], v[92:95]\n"
    "buffer_load_dwordx4 v204, s[44:47], s58 offen lds\n"
    "v_accvgpr_write_b32 a158, 0\n"
    "v_mfma_f32_16x16x32_bf16 v[124:127], a[252:255], a[204:207], v[124:127]\n"
    "ds_read_b64_tr_b16 a[248:249], v195 offset:16896\n"
    "s_add_u32 s58, s58, 0x10000\n"
    "v_mfma_f32_16x16x32_bf16 v[156:159], a[252:255], a[208:211], v[156:159]\n"
    "ds_read_b64_tr_b16 a[250:251], v195 offset:17152\n"
    "s_add_u32 m0, s65, 36864\n"
    "v_mfma_f32_16x16x32_bf16 v[188:191], a[252:255], a[212:215], v[188:191]\n"
    "buffer_load_dwordx4 v205, s[44:47], s58 offen lds\n"
    "v_accvgpr_write_b32 a159, 0\n"
    "s_waitcnt lgkmcnt(4)\n"
    "v_mfma_f32_16x16x32_bf16 v[0:3], a[216:219], a[224:227], v[0:3]\n"
    "ds_read_b64_tr_b16 a[252:253], v196 offset:16896\n"
    "s_add_u32 s58, s58, 0x10000\n"
    "v_mfma_f32_16x16x32_bf16 v[32:35], a[216:219], a[228:231], v[32:35]\n"
    "v_mul_f32 v228, v218, v228\n"
    "v_mfma_f32_16x16x32_bf16 v[64:67], a[216:219], a[232:235], v[64:67]\n"
    "ds_read_b64_tr_b16 a[254:255], v196 offset:17152\n"
    "s_add_u32 m0, s65, 38912\n"
    "v_mfma_f32_16x16x32_bf16 v[96:99], a[216:219], a[236:239], v[96:99]\n"
    "buffer_load_dwordx4 v205, s[44:47], s58 offen lds\n"
    "v_mul_f32 v229, v218, v229\n"
    "v_mfma_f32_16x16x32_bf16 v[128:131], a[216:219], a[240:243], v[128:131]\n"
    "s_add_u32 s58, s58, 0x10000\n"
    "v_mul_f32 v230, v218, v230\n"
    "v_mfma_f32_16x16x32_bf16 v[160:163], a[216:219], a[244:247], v[160:163]\n"
    "v_mul_f32 v231, v218, v231\n"
    "s_waitcnt lgkmcnt(2)\n"
    "v_mfma_f32_16x16x32_bf16 v[4:7], a[220:223], a[224:227], v[4:7]\n"
    "ds_read_b64_tr_b16 a[216:217], v195 offset:17408\n"
    "v_mul_f32 v232, v218, v232\n"
    "v_mfma_f32_16x16x32_bf16 v[36:39], a[220:223], a[228:231], v[36:39]\n"
    "v_mul_f32 v233, v218, v233\n"
    "v_mfma_f32_16x16x32_bf16 v[68:71], a[220:223], a[232:235], v[68:71]\n"
    "ds_read_b64_tr_b16 a[218:219], v195 offset:17664\n"
    "v_mul_f32 v234, v218, v234\n"
    "v_mfma_f32_16x16x32_bf16 v[100:103], a[220:223], a[236:239], v[100:103]\n"
    "v_mul_f32 v235, v218, v235\n"
    "v_mfma_f32_16x16x32_bf16 v[132:135], a[220:223], a[240:243], v[132:135]\n"
    "v_cvt_pk_bf16_f32 v220, v228, v229\n"
    "v_mfma_f32_16x16x32_bf16 v[164:167], a[220:223], a[244:247], v[164:167]\n"
    "v_cvt_pk_bf16_f32 v221, v230, v231\n"
    "v_mfma_f32_16x16x32_bf16 v[8:11], a[248:251], a[224:227], v[8:11]\n"
    "ds_read_b64_tr_b16 a[220:221], v196 offset:17408\n"
    "v_cvt_pk_bf16_f32 v222, v232, v233\n"
    "v_mfma_f32_16x16x32_bf16 v[40:43], a[248:251], a[228:231], v[40:43]\n"
    "v_cvt_pk_bf16_f32 v223, v234, v235\n"
    "v_mfma_f32_16x16x32_bf16 v[72:75], a[248:251], a[232:235], v[72:75]\n"
    "ds_read_b64_tr_b16 a[222:223], v196 offset:17664\n"
    "buffer_store_dwordx4 v[220:223], v242, s[52:55], s73 offen sc1\n"
    "v_mfma_f32_16x16x32_bf16 v[104:107], a[248:251], a[236:239], v[104:107]\n"
    "v_accvgpr_read_b32 v228, a184\n"
    "v_mfma_f32_16x16x32_bf16 v[136:139], a[248:251], a[240:243], v[136:139]\n"
    "v_accvgpr_read_b32 v229, a185\n"
    "v_mfma_f32_16x16x32_bf16 v[168:171], a[248:251], a[244:247], v[168:171]\n"
    "v_accvgpr_read_b32 v230, a186\n"
    "s_waitcnt lgkmcnt(2)\n"
    "v_mfma_f32_16x16x32_bf16 v[12:15], a[252:255], a[224:227], v[12:15]\n"
    "ds_read_b64_tr_b16 a[248:249], v195 offset:17920\n"
    "v_accvgpr_read_b32 v231, a187\n"
    "v_mfma_f32_16x16x32_bf16 v[44:47], a[252:255], a[228:231], v[44:47]\n"
    "v_accvgpr_read_b32 v232, a188\n"
    "v_mfma_f32_16x16x32_bf16 v[76:79], a[252:255], a[232:235], v[76:79]\n"
    "ds_read_b64_tr_b16 a[250:251], v195 offset:18176\n"
    "v_accvgpr_read_b32 v233, a189\n"
    "v_mfma_f32_16x16x32_bf16 v[108:111], a[252:255], a[236:239], v[108:111]\n"
    "v_accvgpr_read_b32 v234, a190\n"
    "v_mfma_f32_16x16x32_bf16 v[140:143], a[252:255], a[240:243], v[140:143]\n"
    "v_accvgpr_read_b32 v235, a191\n"
    "v_mfma_f32_16x16x32_bf16 v[172:175], a[252:255], a[244:247], v[172:175]\n"
    "v_accvgpr_write_b32 a184, 0\n"
    "v_mfma_f32_16x16x32_bf16 v[16:19], a[216:219], a[224:227], v[16:19]\n"
    "ds_read_b64_tr_b16 a[252:253], v196 offset:17920\n"
    "v_accvgpr_write_b32 a185, 0\n"
    "v_mfma_f32_16x16x32_bf16 v[48:51], a[216:219], a[228:231], v[48:51]\n"
    "v_accvgpr_write_b32 a186, 0\n"
    "v_mfma_f32_16x16x32_bf16 v[80:83], a[216:219], a[232:235], v[80:83]\n"
    "ds_read_b64_tr_b16 a[254:255], v196 offset:18176\n"
    "v_accvgpr_write_b32 a187, 0\n"
    "v_mfma_f32_16x16x32_bf16 v[112:115], a[216:219], a[236:239], v[112:115]\n"
    "v_accvgpr_write_b32 a188, 0\n"
    "v_mfma_f32_16x16x32_bf16 v[144:147], a[216:219], a[240:243], v[144:147]\n"
    "v_accvgpr_write_b32 a189, 0\n"
    "v_mfma_f32_16x16x32_bf16 v[176:179], a[216:219], a[244:247], v[176:179]\n"
    "v_accvgpr_write_b32 a190, 0\n"
    "s_waitcnt lgkmcnt(2)\n"
    "v_mfma_f32_16x16x32_bf16 v[20:23], a[220:223], a[224:227], v[20:23]\n"
    "v_accvgpr_write_b32 a191, 0\n"
    "v_mfma_f32_16x16x32_bf16 v[52:55], a[220:223], a[228:231], v[52:55]\n"
    "v_mul_f32 v228, v219, v228\n"
    "v_mfma_f32_16x16x32_bf16 v[84:87], a[220:223], a[232:235], v[84:87]\n"
    "s_waitcnt vmcnt(5) lgkmcnt(0)\n"
    "s_barrier\n"
    "v_mfma_f32_16x16x32_bf16 v[116:119], a[220:223], a[236:239], v[116:119]\n"
    "ds_read_b64_tr_b16 a[216:217], v197 offset:0\n"
    "v_mul_f32 v229, v219, v229\n"
    "v_mfma_f32_16x16x32_bf16 v[148:151], a[220:223], a[240:243], v[148:151]\n"
    "ds_read_b64_tr_b16 a[218:219], v197 offset:256\n"
    "v_mul_f32 v230, v219, v230\n"
    "v_mfma_f32_16x16x32_bf16 v[180:183], a[220:223], a[244:247], v[180:183]\n"
    "ds_read_b128 a[192:195], v199 offset:0\n"
    "v_mul_f32 v231, v219, v231\n"
    "v_mfma_f32_16x16x32_bf16 v[24:27], a[248:251], a[224:227], v[24:27]\n"
    "ds_read_b128 a[196:199], v199 offset:2048\n"
    "v_mul_f32 v232, v219, v232\n"
    "v_mfma_f32_16x16x32_bf16 v[56:59], a[248:251], a[228:231], v[56:59]\n"
    "ds_read_b128 a[200:203], v199 offset:4096\n"
    "v_mul_f32 v233, v219, v233\n"
    "v_mfma_f32_16x16x32_bf16 v[88:91], a[248:251], a[232:235], v[88:91]\n"
    "ds_read_b128 a[204:207], v199 offset:6144\n"
    "v_mul_f32 v234, v219, v234\n"
    "v_mfma_f32_16x16x32_bf16 v[120:123], a[248:251], a[236:239], v[120:123]\n"
    "ds_read_b128 a[208:211], v199 offset:8192\n"
    "v_mul_f32 v235, v219, v235\n"
    "v_mfma_f32_16x16x32_bf16 v[152:155], a[248:251], a[240:243], v[152:155]\n"
    "ds_read_b128 a[212:215], v199 offset:10240\n"
    "s_add_u32 m0, s65, 49152\n"
    "v_mfma_f32_16x16x32_bf16 v[184:187], a[248:251], a[244:247], v[184:187]\n"
    "buffer_load_dwordx4 v204, s[44:47], s58 offen lds\n"
    "v_cvt_pk_bf16_f32 v224, v228, v229\n"
    "v_mfma_f32_16x16x32_bf16 v[28:31], a[252:255], a[224:227], v[28:31]\n"
    "ds_read_b64_tr_b16 a[220:221], v198 offset:0\n"
    "s_add_u32 s58, s58, 0x10000\n"
    "v_mfma_f32_16x16x32_bf16 v[60:63], a[252:255], a[228:231], v[60:63]\n"
    "ds_read_b64_tr_b16 a[222:223], v198 offset:256\n"
    "s_add_u32 m0, s65, 51200\n"
    "v_mfma_f32_16x16x32_bf16 v[92:95], a[252:255], a[232:235], v[92:95]\n"
    "buffer_load_dwordx4 v204, s[44:47], s58 offen lds\n"
    "v_cvt_pk_bf16_f32 v225, v230, v231\n"
    "v_mfma_f32_16x16x32_bf16 v[124:127], a[252:255], a[236:239], v[124:127]\n"
    "ds_read_b64_tr_b16 a[248:249], v197 offset:512\n"
    "s_add_u32 s58, s58, 0x10000\n"
    "v_mfma_f32_16x16x32_bf16 v[156:159], a[252:255], a[240:243], v[156:159]\n"
    "ds_read_b64_tr_b16 a[250:251], v197 offset:768\n"
    "s_add_u32 m0, s65, 53248\n"
    "v_mfma_f32_16x16x32_bf16 v[188:191], a[252:255], a[244:247], v[188:191]\n"
    "buffer_load_dwordx4 v205, s[44:47], s58 offen lds\n"
    "v_cvt_pk_bf16_f32 v226, v232, v233\n"
    "s_waitcnt lgkmcnt(4)\n"
    "v_mfma_f32_16x16x32_bf16 v[0:3], a[216:219], a[192:195], v[0:3]\n"
    "ds_read_b64_tr_b16 a[252:253], v198 offset:512\n"
    "s_add_u32 s58, s58, 0x10000\n"
    "v_mfma_f32_16x16x32_bf16 v[32:35], a[216:219], a[196:199], v[32:35]\n"
    "v_cvt_pk_bf16_f32 v227, v234, v235\n"
    "v_mfma_f32_16x16x32_bf16 v[64:67], a[216:219], a[200:203], v[64:67]\n"
    "ds_read_b64_tr_b16 a[254:255], v198 offset:768\n"
    "buffer_store_dwordx4 v[224:227], v242, s[52:55], s73 offen offset:1024 sc1\n"
    "s_add_u32 m0, s65, 55296\n"
    "v_mfma_f32_16x16x32_bf16 v[96:99], a[216:219], a[204:207], v[96:99]\n"
    "buffer_load_dwordx4 v205, s[44:47], s58 offen lds\n"
    "v_mfma_f32_16x16x32_bf16 v[128:131], a[216:219], a[208:211], v[128:131]\n"
    "s_add_u32 s58, s58, 0x10000\n"
    "v_mfma_f32_16x16x32_bf16 v[160:163], a[216:219], a[212:215], v[160:163]\n"
    "s_add_u32 m0, s69, 139264\n"
    "s_waitcnt lgkmcnt(2)\n"
    "v_mfma_f32_16x16x32_bf16 v[4:7], a[220:223], a[192:195], v[4:7]\n"
    "ds_read_b64_tr_b16 a[216:217], v197 offset:1024\n"
    "s_add_u32 s59, s61, 1024\n"
    "v_mfma_f32_16x16x32_bf16 v[36:39], a[220:223], a[196:199], v[36:39]\n"
    "buffer_load_dwordx4 v206, s[48:51], s59 offen lds\n"
    "v_mfma_f32_16x16x32_bf16 v[68:71], a[220:223], a[200:203], v[68:71]\n"
    "ds_read_b64_tr_b16 a[218:219], v197 offset:1280\n"
    "v_mfma_f32_16x16x32_bf16 v[100:103], a[220:223], a[204:207], v[100:103]\n"
    "s_add_u32 m0, s69, 140288\n"
    "v_mfma_f32_16x16x32_bf16 v[132:135], a[220:223], a[208:211], v[132:135]\n"
    "s_add_u32 s59, s61, 10240\n"
    "v_mfma_f32_16x16x32_bf16 v[164:167], a[220:223], a[212:215], v[164:167]\n"
    "buffer_load_dwordx4 v207, s[48:51], s59 offen lds\n"
    "v_mfma_f32_16x16x32_bf16 v[8:11], a[248:251], a[192:195], v[8:11]\n"
    "ds_read_b64_tr_b16 a[220:221], v198 offset:1024\n"
    "v_mfma_f32_16x16x32_bf16 v[40:43], a[248:251], a[196:199], v[40:43]\n"
    "s_add_u32 m0, s69, 141312\n"
    "v_mfma_f32_16x16x32_bf16 v[72:75], a[248:251], a[200:203], v[72:75]\n"
    "ds_read_b64_tr_b16 a[222:223], v198 offset:1280\n"
    "s_add_u32 s59, s61, 19456\n"
    "v_mfma_f32_16x16x32_bf16 v[104:107], a[248:251], a[204:207], v[104:107]\n"
    "buffer_load_dwordx4 v206, s[48:51], s59 offen lds\n"
    "v_mfma_f32_16x16x32_bf16 v[136:139], a[248:251], a[208:211], v[136:139]\n"
    "v_mfma_f32_16x16x32_bf16 v[168:171], a[248:251], a[212:215], v[168:171]\n"
    "s_add_u32 m0, s69, 142336\n"
    "s_waitcnt lgkmcnt(2)\n"
    "v_mfma_f32_16x16x32_bf16 v[12:15], a[252:255], a[192:195], v[12:15]\n"
    "ds_read_b64_tr_b16 a[248:249], v197 offset:1536\n"
    "s_add_u32 s59, s61, 28672\n"
    "v_mfma_f32_16x16x32_bf16 v[44:47], a[252:255], a[196:199], v[44:47]\n"
    "buffer_load_dwordx4 v207, s[48:51], s59 offen lds\n"
    "v_mfma_f32_16x16x32_bf16 v[76:79], a[252:255], a[200:203], v[76:79]\n"
    "ds_read_b64_tr_b16 a[250:251], v197 offset:1792\n"
    "v_mfma_f32_16x16x32_bf16 v[108:111], a[252:255], a[204:207], v[108:111]\n"
    "s_add_u32 m0, s69, 143360\n"
    "v_mfma_f32_16x16x32_bf16 v[140:143], a[252:255], a[208:211], v[140:143]\n"
    "s_add_u32 s59, s61, 37888\n"
    "v_mfma_f32_16x16x32_bf16 v[172:175], a[252:255], a[212:215], v[172:175]\n"
    "buffer_load_dwordx4 v206, s[48:51], s59 offen lds\n"
    "v_mfma_f32_16x16x32_bf16 v[16:19], a[216:219], a[192:195], v[16:19]\n"
    "ds_read_b64_tr_b16 a[252:253], v198 offset:1536\n"
    "v_mfma_f32_16x16x32_bf16 v[48:51], a[216:219], a[196:199], v[48:51]\n"
    "s_add_u32 m0, s69, 144384\n"
    "v_mfma_f32_16x16x32_bf16 v[80:83], a[216:219], a[200:203], v[80:83]\n"
    "ds_read_b64_tr_b16 a[254:255], v198 offset:1792\n"
    "s_add_u32 s59, s61, 47104\n"
    "v_mfma_f32_16x16x32_bf16 v[112:115], a[216:219], a[204:207], v[112:115]\n"
    "buffer_load_dwordx4 v207, s[48:51], s59 offen lds\n"
    "v_mfma_f32_16x16x32_bf16 v[144:147], a[216:219], a[208:211], v[144:147]\n"
    "v_mfma_f32_16x16x32_bf16 v[176:179], a[216:219], a[212:215], v[176:179]\n"
    "s_waitcnt lgkmcnt(2)\n"
    "v_mfma_f32_16x16x32_bf16 v[20:23], a[220:223], a[192:195], v[20:23]\n"
    "v_mfma_f32_16x16x32_bf16 v[52:55], a[220:223], a[196:199], v[52:55]\n"
    "v_mfma_f32_16x16x32_bf16 v[84:87], a[220:223], a[200:203], v[84:87]\n"
    "s_waitcnt vmcnt(12) lgkmcnt(0)\n"
    "s_barrier\n"
    "v_mfma_f32_16x16x32_bf16 v[116:119], a[220:223], a[204:207], v[116:119]\n"
    "ds_read_b64_tr_b16 a[216:217], v195 offset:0\n"
    "v_mfma_f32_16x16x32_bf16 v[148:151], a[220:223], a[208:211], v[148:151]\n"
    "ds_read_b64_tr_b16 a[218:219], v195 offset:256\n"
    "v_mfma_f32_16x16x32_bf16 v[180:183], a[220:223], a[212:215], v[180:183]\n"
    "ds_read_b128 a[224:227], v199 offset:512\n"
    "v_mfma_f32_16x16x32_bf16 v[24:27], a[248:251], a[192:195], v[24:27]\n"
    "ds_read_b128 a[228:231], v199 offset:2560\n"
    "v_mfma_f32_16x16x32_bf16 v[56:59], a[248:251], a[196:199], v[56:59]\n"
    "ds_read_b128 a[232:235], v199 offset:4608\n"
    "v_mfma_f32_16x16x32_bf16 v[88:91], a[248:251], a[200:203], v[88:91]\n"
    "ds_read_b128 a[236:239], v199 offset:6656\n"
    "v_mfma_f32_16x16x32_bf16 v[120:123], a[248:251], a[204:207], v[120:123]\n"
    "ds_read_b128 a[240:243], v199 offset:8704\n"
    "v_mfma_f32_16x16x32_bf16 v[152:155], a[248:251], a[208:211], v[152:155]\n"
    "ds_read_b128 a[244:247], v199 offset:10752\n"
    "s_add_u32 m0, s65, 98304\n"
    "v_mfma_f32_16x16x32_bf16 v[184:187], a[248:251], a[212:215], v[184:187]\n"
    "buffer_load_dwordx4 v204, s[44:47], s58 offen lds\n"
    "v_mfma_f32_16x16x32_bf16 v[28:31], a[252:255], a[192:195], v[28:31]\n"
    "ds_read_b64_tr_b16 a[220:221], v196 offset:0\n"
    "s_add_u32 s58, s58, 0x10000\n"
    "v_mfma_f32_16x16x32_bf16 v[60:63], a[252:255], a[196:199], v[60:63]\n"
    "ds_read_b64_tr_b16 a[222:223], v196 offset:256\n"
    "s_add_u32 m0, s65, 100352\n"
    "v_mfma_f32_16x16x32_bf16 v[92:95], a[252:255], a[200:203], v[92:95]\n"
    "buffer_load_dwordx4 v204, s[44:47], s58 offen lds\n"
    "v_mfma_f32_16x16x32_bf16 v[124:127], a[252:255], a[204:207], v[124:127]\n"
    "ds_read_b64_tr_b16 a[248:249], v195 offset:512\n"
    "s_add_u32 s58, s58, 0x10000\n"
    "v_mfma_f32_16x16x32_bf16 v[156:159], a[252:255], a[208:211], v[156:159]\n"
    "ds_read_b64_tr_b16 a[250:251], v195 offset:768\n"
    "s_add_u32 m0, s65, 102400\n"
    "v_mfma_f32_16x16x32_bf16 v[188:191], a[252:255], a[212:215], v[188:191]\n"
    "buffer_load_dwordx4 v205, s[44:47], s58 offen lds\n"
    "s_waitcnt lgkmcnt(4)\n"
    "v_mfma_f32_16x16x32_bf16 v[0:3], a[216:219], a[224:227], v[0:3]\n"
    "ds_read_b64_tr_b16 a[252:253], v196 offset:512\n"
    "s_add_u32 s58, s58, 0x10000\n"
    "v_mfma_f32_16x16x32_bf16 v[32:35], a[216:219], a[228:231], v[32:35]\n"
    "v_mfma_f32_16x16x32_bf16 v[64:67], a[216:219], a[232:235], v[64:67]\n"
    "ds_read_b64_tr_b16 a[254:255], v196 offset:768\n"
    "s_add_u32 m0, s65, 104448\n"
    "v_mfma_f32_16x16x32_bf16 v[96:99], a[216:219], a[236:239], v[96:99]\n"
    "buffer_load_dwordx4 v205, s[44:47], s58 offen lds\n"
    "v_mfma_f32_16x16x32_bf16 v[128:131], a[216:219], a[240:243], v[128:131]\n"
    "s_add_u32 s58, s58, 0x10000\n"
    "v_mfma_f32_16x16x32_bf16 v[160:163], a[216:219], a[244:247], v[160:163]\n"
    "s_waitcnt lgkmcnt(2)\n"
    "v_mfma_f32_16x16x32_bf16 v[4:7], a[220:223], a[224:227], v[4:7]\n"
    "ds_read_b64_tr_b16 a[216:217], v195 offset:1024\n"
    "v_mfma_f32_16x16x32_bf16 v[36:39], a[220:223], a[228:231], v[36:39]\n"
    "v_mfma_f32_16x16x32_bf16 v[68:71], a[220:223], a[232:235], v[68:71]\n"
    "ds_read_b64_tr_b16 a[218:219], v195 offset:1280\n"
    "v_mfma_f32_16x16x32_bf16 v[100:103], a[220:223], a[236:239], v[100:103]\n"
    "v_mfma_f32_16x16x32_bf16 v[132:135], a[220:223], a[240:243], v[132:135]\n"
    "v_mfma_f32_16x16x32_bf16 v[164:167], a[220:223], a[244:247], v[164:167]\n"
    "v_mfma_f32_16x16x32_bf16 v[8:11], a[248:251], a[224:227], v[8:11]\n"
    "ds_read_b64_tr_b16 a[220:221], v196 offset:1024\n"
    "v_mfma_f32_16x16x32_bf16 v[40:43], a[248:251], a[228:231], v[40:43]\n"
    "v_mfma_f32_16x16x32_bf16 v[72:75], a[248:251], a[232:235], v[72:75]\n"
    "ds_read_b64_tr_b16 a[222:223], v196 offset:1280\n"
    "v_mfma_f32_16x16x32_bf16 v[104:107], a[248:251], a[236:239], v[104:107]\n"
    "v_mfma_f32_16x16x32_bf16 v[136:139], a[248:251], a[240:243], v[136:139]\n"
    "v_mfma_f32_16x16x32_bf16 v[168:171], a[248:251], a[244:247], v[168:171]\n"
    "s_waitcnt lgkmcnt(2)\n"
    "v_mfma_f32_16x16x32_bf16 v[12:15], a[252:255], a[224:227], v[12:15]\n"
    "ds_read_b64_tr_b16 a[248:249], v195 offset:1536\n"
    "v_mfma_f32_16x16x32_bf16 v[44:47], a[252:255], a[228:231], v[44:47]\n"
    "v_mfma_f32_16x16x32_bf16 v[76:79], a[252:255], a[232:235], v[76:79]\n"
    "ds_read_b64_tr_b16 a[250:251], v195 offset:1792\n"
    "v_mfma_f32_16x16x32_bf16 v[108:111], a[252:255], a[236:239], v[108:111]\n"
    "v_mfma_f32_16x16x32_bf16 v[140:143], a[252:255], a[240:243], v[140:143]\n"
    "v_mfma_f32_16x16x32_bf16 v[172:175], a[252:255], a[244:247], v[172:175]\n"
    "v_mfma_f32_16x16x32_bf16 v[16:19], a[216:219], a[224:227], v[16:19]\n"
    "ds_read_b64_tr_b16 a[252:253], v196 offset:1536\n"
    "v_mfma_f32_16x16x32_bf16 v[48:51], a[216:219], a[228:231], v[48:51]\n"
    "v_mfma_f32_16x16x32_bf16 v[80:83], a[216:219], a[232:235], v[80:83]\n"
    "ds_read_b64_tr_b16 a[254:255], v196 offset:1792\n"
    "v_mfma_f32_16x16x32_bf16 v[112:115], a[216:219], a[236:239], v[112:115]\n"
    "v_mfma_f32_16x16x32_bf16 v[144:147], a[216:219], a[240:243], v[144:147]\n"
    "v_mfma_f32_16x16x32_bf16 v[176:179], a[216:219], a[244:247], v[176:179]\n"
    "s_waitcnt lgkmcnt(2)\n"
    "v_mfma_f32_16x16x32_bf16 v[20:23], a[220:223], a[224:227], v[20:23]\n"
    "v_mfma_f32_16x16x32_bf16 v[52:55], a[220:223], a[228:231], v[52:55]\n"
    "v_mfma_f32_16x16x32_bf16 v[84:87], a[220:223], a[232:235], v[84:87]\n"
    "s_waitcnt vmcnt(4) lgkmcnt(0)\n"
    "s_barrier\n"
    "v_mfma_f32_16x16x32_bf16 v[116:119], a[220:223], a[236:239], v[116:119]\n"
    "ds_read_b64_tr_b16 a[216:217], v195 offset:16384\n"
    "v_mfma_f32_16x16x32_bf16 v[148:151], a[220:223], a[240:243], v[148:151]\n"
    "ds_read_b64_tr_b16 a[218:219], v195 offset:16640\n"
    "v_mfma_f32_16x16x32_bf16 v[180:183], a[220:223], a[244:247], v[180:183]\n"
    "ds_read_b128 a[192:195], v199 offset:24576\n"
    "v_mfma_f32_16x16x32_bf16 v[24:27], a[248:251], a[224:227], v[24:27]\n"
    "ds_read_b128 a[196:199], v199 offset:26624\n"
    "v_mfma_f32_16x16x32_bf16 v[56:59], a[248:251], a[228:231], v[56:59]\n"
    "ds_read_b128 a[200:203], v199 offset:28672\n"
    "v_mfma_f32_16x16x32_bf16 v[88:91], a[248:251], a[232:235], v[88:91]\n"
    "ds_read_b128 a[204:207], v199 offset:30720\n"
    "v_mfma_f32_16x16x32_bf16 v[120:123], a[248:251], a[236:239], v[120:123]\n"
    "ds_read_b128 a[208:211], v199 offset:32768\n"
    "v_mfma_f32_16x16x32_bf16 v[152:155], a[248:251], a[240:243], v[152:155]\n"
    "ds_read_b128 a[212:215], v199 offset:34816\n"
    "s_add_u32 m0, s65, 32768\n"
    "v_mfma_f32_16x16x32_bf16 v[184:187], a[248:251], a[244:247], v[184:187]\n"
    "buffer_load_dwordx4 v201, s[36:39], s56 offen nt lds\n"
    "v_mfma_f32_16x16x32_bf16 v[28:31], a[252:255], a[224:227], v[28:31]\n"
    "ds_read_b64_tr_b16 a[220:221], v196 offset:16384\n"
    "v_mfma_f32_16x16x32_bf16 v[60:63], a[252:255], a[228:231], v[60:63]\n"
    "ds_read_b64_tr_b16 a[222:223], v196 offset:16640\n"
    "v_mfma_f32_16x16x32_bf16 v[92:95], a[252:255], a[232:235], v[92:95]\n"
    "buffer_load_dwordx4 v202, s[36:39], s56 offen offset:2048 nt lds\n"
    "v_mfma_f32_16x16x32_bf16 v[124:127], a[252:255], a[236:239], v[124:127]\n"
    "ds_read_b64_tr_b16 a[248:249], v195 offset:16896\n"
    "s_add_u32 s92, s92, 0x1000\n"
    "v_mfma_f32_16x16x32_bf16 v[156:159], a[252:255], a[240:243], v[156:159]\n"
    "ds_read_b64_tr_b16 a[250:251], v195 offset:17152\n"
    "s_xor_b32 s56, s92, %[rot]\n"
    "s_add_u32 m0, s65, 36864\n"
    "v_mfma_f32_16x16x32_bf16 v[188:191], a[252:255], a[244:247], v[188:191]\n"
    "buffer_load_dwordx4 v201, s[36:39], s56 offen nt lds\n"
    "s_waitcnt lgkmcnt(4)\n"
    "v_mfma_f32_16x16x32_bf16 v[0:3], a[216:219], a[192:195], v[0:3]\n"
    "ds_read_b64_tr_b16 a[252:253], v196 offset:16896\n"
    "v_mfma_f32_16x16x32_bf16 v[32:35], a[216:219], a[196:199], v[32:35]\n"
    "v_mfma_f32_16x16x32_bf16 v[64:67], a[216:219], a[200:203], v[64:67]\n"
    "ds_read_b64_tr_b16 a[254:255], v196 offset:17152\n"
    "v_mfma_f32_16x16x32_bf16 v[96:99], a[216:219], a[204:207], v[96:99]\n"
    "buffer_load_dwordx4 v202, s[36:39], s56 offen offset:2048 nt lds\n"
    "v_mfma_f32_16x16x32_bf16 v[128:131], a[216:219], a[208:211], v[128:131]\n"
    "s_add_u32 s92, s92, 0x1000\n"
    "v_mfma_f32_16x16x32_bf16 v[160:163], a[216:219], a[212:215], v[160:163]\n"
    "s_xor_b32 s56, s92, %[rot]\n"
    "s_waitcnt lgkmcnt(2)\n"
    "v_mfma_f32_16x16x32_bf16 v[4:7], a[220:223], a[192:195], v[4:7]\n"
    "ds_read_b64_tr_b16 a[216:217], v195 offset:17408\n"
    "v_mfma_f32_16x16x32_bf16 v[36:39], a[220:223], a[196:199], v[36:39]\n"
    "v_mfma_f32_16x16x32_bf16 v[68:71], a[220:223], a[200:203], v[68:71]\n"
    "ds_read_b64_tr_b16 a[218:219], v195 offset:17664\n"
    "v_mfma_f32_16x16x32_bf16 v[100:103], a[220:223], a[204:207], v[100:103]\n"
    "v_mfma_f32_16x16x32_bf16 v[132:135], a[220:223], a[208:211], v[132:135]\n"
    "v_mfma_f32_16x16x32_bf16 v[164:167], a[220:223], a[212:215], v[164:167]\n"
    "v_mfma_f32_16x16x32_bf16 v[8:11], a[248:251], a[192:195], v[8:11]\n"
    "ds_read_b64_tr_b16 a[220:221], v196 offset:17408\n"
    "v_mfma_f32_16x16x32_bf16 v[40:43], a[248:251], a[196:199], v[40:43]\n"
    "v_mfma_f32_16x16x32_bf16 v[72:75], a[248:251], a[200:203], v[72:75]\n"
    "ds_read_b64_tr_b16 a[222:223], v196 offset:17664\n"
    "v_mfma_f32_16x16x32_bf16 v[104:107], a[248:251], a[204:207], v[104:107]\n"
    "v_mfma_f32_16x16x32_bf16 v[136:139], a[248:251], a[208:211], v[136:139]\n"
    "v_mfma_f32_16x16x32_bf16 v[168:171], a[248:251], a[212:215], v[168:171]\n"
    "s_waitcnt lgkmcnt(2)\n"
    "v_mfma_f32_16x16x32_bf16 v[12:15], a[252:255], a[192:195], v[12:15]\n"
    "ds_read_b64_tr_b16 a[248:249], v195 offset:17920\n"
    "v_mfma_f32_16x16x32_bf16 v[44:47], a[252:255], a[196:199], v[44:47]\n"
    "v_mfma_f32_16x16x32_bf16 v[76:79], a[252:255], a[200:203], v[76:79]\n"
    "ds_read_b64_tr_b16 a[250:251], v195 offset:18176\n"
    "v_mfma_f32_16x16x32_bf16 v[108:111], a[252:255], a[204:207], v[108:111]\n"
    "v_mfma_f32_16x16x32_bf16 v[140:143], a[252:255], a[208:211], v[140:143]\n"
    "v_mfma_f32_16x16x32_bf16 v[172:175], a[252:255], a[212:215], v[172:175]\n"
    "v_mfma_f32_16x16x32_bf16 v[16:19], a[216:219], a[192:195], v[16:19]\n"
    "ds_read_b64_tr_b16 a[252:253], v196 offset:17920\n"
    "v_mfma_f32_16x16x32_bf16 v[48:51], a[216:219], a[196:199], v[48:51]\n"
    "v_mfma_f32_16x16x32_bf16 v[80:83], a[216:219], a[200:203], v[80:83]\n"
    "ds_read_b64_tr_b16 a[254:255], v196 offset:18176\n"
    "v_mfma_f32_16x16x32_bf16 v[112:115], a[216:219], a[204:207], v[112:115]\n"
    "v_mfma_f32_16x16x32_bf16 v[144:147], a[216:219], a[208:211], v[144:147]\n"
    "v_mfma_f32_16x16x32_bf16 v[176:179], a[216:219], a[212:215], v[176:179]\n"
    "s_waitcnt lgkmcnt(2)\n"
    "v_mfma_f32_16x16x32_bf16 v[20:23], a[220:223], a[192:195], v[20:23]\n"
    "v_mfma_f32_16x16x32_bf16 v[52:55], a[220:223], a[196:199], v[52:55]\n"
    "v_mfma_f32_16x16x32_bf16 v[84:87], a[220:223], a[200:203], v[84:87]\n"
    "s_waitcnt vmcnt(4) lgkmcnt(0)\n"
    "s_barrier\n"
    "v_mfma_f32_16x16x32_bf16 v[116:119], a[220:223], a[204:207], v[116:119]\n"
    "ds_read_b64_tr_b16 a[216:217], v197 offset:0\n"
    "v_mfma_f32_16x16x32_bf16 v[148:151], a[220:223], a[208:211], v[148:151]\n"
    "ds_read_b64_tr_b16 a[218:219], v197 offset:256\n"
    "v_mfma_f32_16x16x32_bf16 v[180:183], a[220:223], a[212:215], v[180:183]\n"
    "ds_read_b128 a[224:227], v199 offset:25088\n"
    "v_mfma_f32_16x16x32_bf16 v[24:27], a[248:251], a[192:195], v[24:27]\n"
    "ds_read_b128 a[228:231], v199 offset:27136\n"
    "v_mfma_f32_16x16x32_bf16 v[56:59], a[248:251], a[196:199], v[56:59]\n"
    "ds_read_b128 a[232:235], v199 offset:29184\n"
    "v_mfma_f32_16x16x32_bf16 v[88:91], a[248:251], a[200:203], v[88:91]\n"
    "ds_read_b128 a[236:239], v199 offset:31232\n"
    "v_mfma_f32_16x16x32_bf16 v[120:123], a[248:251], a[204:207], v[120:123]\n"
    "ds_read_b128 a[240:243], v199 offset:33280\n"
    "v_mfma_f32_16x16x32_bf16 v[152:155], a[248:251], a[208:211], v[152:155]\n"
    "ds_read_b128 a[244:247], v199 offset:35328\n"
    "s_add_u32 m0, s65, 49152\n"
    "v_mfma_f32_16x16x32_bf16 v[184:187], a[248:251], a[212:215], v[184:187]\n"
    "buffer_load_dwordx4 v201, s[36:39], s56 offen nt lds\n"
    "v_mfma_f32_16x16x32_bf16 v[28:31], a[252:255], a[192:195], v[28:31]\n"
    "ds_read_b64_tr_b16 a[220:221], v198 offset:0\n"
    "v_mfma_f32_16x16x32_bf16 v[60:63], a[252:255], a[196:199], v[60:63]\n"
    "ds_read_b64_tr_b16 a[222:223], v198 offset:256\n"
    "v_mfma_f32_16x16x32_bf16 v[92:95], a[252:255], a[200:203], v[92:95]\n"
    "buffer_load_dwordx4 v202, s[36:39], s56 offen offset:2048 nt lds\n"
    "v_mfma_f32_16x16x32_bf16 v[124:127], a[252:255], a[204:207], v[124:127]\n"
    "ds_read_b64_tr_b16 a[248:249], v197 offset:512\n"
    "s_add_u32 s92, s92, 0x1000\n"
    "v_mfma_f32_16x16x32_bf16 v[156:159], a[252:255], a[208:211], v[156:159]\n"
    "ds_read_b64_tr_b16 a[250:251], v197 offset:768\n"
    "s_xor_b32 s56, s92, %[rot]\n"
    "s_add_u32 m0, s65, 53248\n"
    "v_mfma_f32_16x16x32_bf16 v[188:191], a[252:255], a[212:215], v[188:191]\n"
    "buffer_load_dwordx4 v201, s[36:39], s56 offen nt lds\n"
    "s_waitcnt lgkmcnt(4)\n"
    "v_mfma_f32_16x16x32_bf16 v[0:3], a[216:219], a[224:227], v[0:3]\n"
    "ds_read_b64_tr_b16 a[252:253], v198 offset:512\n"
    "v_mfma_f32_16x16x32_bf16 v[32:35], a[216:219], a[228:231], v[32:35]\n"
    "v_mfma_f32_16x16x32_bf16 v[64:67], a[216:219], a[232:235], v[64:67]\n"
    "ds_read_b64_tr_b16 a[254:255], v198 offset:768\n"
    "v_mfma_f32_16x16x32_bf16 v[96:99], a[216:219], a[236:239], v[96:99]\n"
    "buffer_load_dwordx4 v202, s[36:39], s56 offen offset:2048 nt lds\n"
    "v_mfma_f32_16x16x32_bf16 v[128:131], a[216:219], a[240:243], v[128:131]\n"
    "ds_read_b128 a[192:195], v192 offset:0\n"
    "s_add_u32 s92, s92, 0x1000\n"
    "v_mfma_f32_16x16x32_bf16 v[160:163], a[216:219], a[244:247], v[160:163]\n"
    "s_xor_b32 s56, s92, %[rot]\n"
    "s_waitcnt lgkmcnt(3)\n"
    "v_mfma_f32_16x16x32_bf16 v[4:7], a[220:223], a[224:227], v[4:7]\n"
    "ds_read_b64_tr_b16 a[216:217], v197 offset:1024\n"
    "v_mfma_f32_16x16x32_bf16 v[36:39], a[220:223], a[228:231], v[36:39]\n"
    "v_mfma_f32_16x16x32_bf16 v[68:71], a[220:223], a[232:235], v[68:71]\n"
    "ds_read_b64_tr_b16 a[218:219], v197 offset:1280\n"
    "v_mfma_f32_16x16x32_bf16 v[100:103], a[220:223], a[236:239], v[100:103]\n"
    "v_mfma_f32_16x16x32_bf16 v[132:135], a[220:223], a[240:243], v[132:135]\n"
    "ds_read_b128 a[196:199], v192 offset:512\n"
    "v_mfma_f32_16x16x32_bf16 v[164:167], a[220:223], a[244:247], v[164:167]\n"
    "v_mfma_f32_16x16x32_bf16 v[8:11], a[248:251], a[224:227], v[8:11]\n"
    "ds_read_b64_tr_b16 a[220:221], v198 offset:1024\n"
    "v_mfma_f32_16x16x32_bf16 v[40:43], a[248:251], a[228:231], v[40:43]\n"
    "v_mfma_f32_16x16x32_bf16 v[72:75], a[248:251], a[232:235], v[72:75]\n"
    "ds_read_b64_tr_b16 a[222:223], v198 offset:1280\n"
    "v_mfma_f32_16x16x32_bf16 v[104:107], a[248:251], a[236:239], v[104:107]\n"
    "v_mfma_f32_16x16x32_bf16 v[136:139], a[248:251], a[240:243], v[136:139]\n"
    "ds_read_b128 a[200:203], v192 offset:1024\n"
    "v_mul_f32 v0, s76, v0\n"
    "v_mul_f32 v1, s76, v1\n"
    "v_mfma_f32_16x16x32_bf16 v[168:171], a[248:251], a[244:247], v[168:171]\n"
    "v_mul_f32 v2, s76, v2\n"
    "v_mul_f32 v3, s76, v3\n"
    "s_waitcnt lgkmcnt(3)\n"
    "v_mfma_f32_16x16x32_bf16 v[12:15], a[252:255], a[224:227], v[12:15]\n"
    "ds_read_b64_tr_b16 a[248:249], v197 offset:1536\n"
    "v_mul_f32 v4, s76, v4\n"
    "v_mul_f32 v5, s76, v5\n"
    "v_mfma_f32_16x16x32_bf16 v[44:47], a[252:255], a[228:231], v[44:47]\n"
    "v_mul_f32 v6, s76, v6\n"
    "v_mul_f32 v7, s76, v7\n"
    "v_cvt_pk_bf16_f32 v0, v0, v1\n"
    "v_mfma_f32_16x16x32_bf16 v[76:79], a[252:255], a[232:235], v[76:79]\n"
    "ds_read_b64_tr_b16 a[250:251], v197 offset:1792\n"
    "v_cvt_pk_bf16_f32 v1, v2, v3\n"
    "v_cvt_pk_bf16_f32 v2, v4, v5\n"
    "v_mfma_f32_16x16x32_bf16 v[108:111], a[252:255], a[236:239], v[108:111]\n"
    "v_cvt_pk_bf16_f32 v3, v6, v7\n"
    "v_mul_f32 v32, s76, v32\n"
    "v_mul_f32 v33, s76, v33\n"
    "v_mfma_f32_16x16x32_bf16 v[140:143], a[252:255], a[240:243], v[140:143]\n"
    "ds_read_b128 a[204:207], v192 offset:1536\n"
    "v_permlane16_swap_b32 v0, v2\n"
    "v_mfma_f32_16x16x32_bf16 v[172:175], a[252:255], a[244:247], v[172:175]\n"
    "v_permlane16_swap_b32 v1, v3\n"
    "v_mfma_f32_16x16x32_bf16 v[16:19], a[216:219], a[224:227], v[16:19]\n"
    "ds_read_b64_tr_b16 a[252:253], v198 offset:1536\n"
    "v_mul_f32 v34, s76, v34\n"
    "v_mul_f32 v35, s76, v35\n"
    "v_mfma_f32_16x16x32_bf16 v[48:51], a[216:219], a[228:231], v[48:51]\n"
    "v_mul_f32 v36, s76, v36\n"
    "v_mul_f32 v37, s76, v37\n"
    "v_mfma_f32_16x16x32_bf16 v[80:83], a[216:219], a[232:235], v[80:83]\n"
    "ds_read_b64_tr_b16 a[254:255], v198 offset:1792\n"
    "v_mul_f32 v38, s76, v38\n"
    "v_mul_f32 v39, s76, v39\n"
    "v_mfma_f32_16x16x32_bf16 v[112:115], a[216:219], a[236:239], v[112:115]\n"
    "v_cvt_pk_bf16_f32 v32, v32, v33\n"
    "v_cvt_pk_bf16_f32 v33, v34, v35\n"
    "v_mfma_f32_16x16x32_bf16 v[144:147], a[216:219], a[240:243], v[144:147]\n"
    "ds_read_b128 a[208:211], v192 offset:4096\n"
    "v_cvt_pk_bf16_f32 v34, v36, v37\n"
    "v_cvt_pk_bf16_f32 v35, v38, v39\n"
    "v_mfma_f32_16x16x32_bf16 v[176:179], a[216:219], a[244:247], v[176:179]\n"
    "s_nop 1\n"
    "s_waitcnt lgkmcnt(3)\n"
    "v_mfma_f32_16x16x32_bf16 v[20:23], a[220:223], a[224:227], v[20:23]\n"
    "v_permlane16_swap_b32 v32, v34\n"
    "v_mfma_f32_16x16x32_bf16 v[52:55], a[220:223], a[228:231], v[52:55]\n"
    "v_permlane16_swap_b32 v33, v35\n"
    "v_mfma_f32_16x16x32_bf16 v[84:87], a[220:223], a[232:235], v[84:87]\n"
    "v_mul_f32 v8, s76, v8\n"
    "v_mul_f32 v9, s76, v9\n"
    "v_mul_f32 v10, s76, v10\n"
    "v_mfma_f32_16x16x32_bf16 v[116:119], a[220:223], a[236:239], v[116:119]\n"
    "v_mul_f32 v11, s76, v11\n"
    "v_mul_f32 v12, s76, v12\n"
    "v_mul_f32 v13, s76, v13\n"
    "v_mfma_f32_16x16x32_bf16 v[148:151], a[220:223], a[240:243], v[148:151]\n"
    "ds_read_b128 a[212:215], v192 offset:4608\n"
    "v_mul_f32 v14, s76, v14\n"
    "v_mul_f32 v15, s76, v15\n"
    "v_mfma_f32_16x16x32_bf16 v[180:183], a[220:223], a[244:247], v[180:183]\n"
    "v_cvt_pk_bf16_f32 v4, v8, v9\n"
    "v_cvt_pk_bf16_f32 v5, v10, v11\n"
    "v_mfma_f32_16x16x32_bf16 v[24:27], a[248:251], a[224:227], v[24:27]\n"
    "ds_read_b128 a[216:219], v192 offset:5120\n"
    "v_cvt_pk_bf16_f32 v6, v12, v13\n"
    "v_cvt_pk_bf16_f32 v7, v14, v15\n"
    "v_mfma_f32_16x16x32_bf16 v[56:59], a[248:251], a[228:231], v[56:59]\n"
    "v_mul_f32 v40, s76, v40\n"
    "v_mul_f32 v41, s76, v41\n"
    "v_mfma_f32_16x16x32_bf16 v[88:91], a[248:251], a[232:235], v[88:91]\n"
    "ds_read_b128 a[220:223], v192 offset:5632\n"
    "v_permlane16_swap_b32 v4, v6\n"
    "v_mfma_f32_16x16x32_bf16 v[120:123], a[248:251], a[236:239], v[120:123]\n"
    "v_permlane16_swap_b32 v5, v7\n"
    "v_mfma_f32_16x16x32_bf16 v[152:155], a[248:251], a[240:243], v[152:155]\n"
    "v_mul_f32 v42, s76, v42\n"
    "v_mul_f32 v43, s76, v43\n"
    "v_mfma_f32_16x16x32_bf16 v[184:187], a[248:251], a[244:247], v[184:187]\n"
    "v_mul_f32 v44, s76, v44\n"
    "v_mul_f32 v45, s76, v45\n"
    "s_waitcnt lgkmcnt(2)\n"
    "v_mfma_f32_16x16x32_bf16 v[28:31], a[252:255], a[224:227], v[28:31]\n"
    "v_mul_f32 v46, s76, v46\n"
    "v_mul_f32 v47, s76, v47\n"
    "v_mfma_f32_16x16x32_bf16 v[60:63], a[252:255], a[228:231], v[60:63]\n"
    "v_cvt_pk_bf16_f32 v36, v40, v41\n"
    "v_cvt_pk_bf16_f32 v37, v42, v43\n"
    "v_mfma_f32_16x16x32_bf16 v[92:95], a[252:255], a[232:235], v[92:95]\n"
    "v_cvt_pk_bf16_f32 v38, v44, v45\n"
    "v_cvt_pk_bf16_f32 v39, v46, v47\n"
    "v_mfma_f32_16x16x32_bf16 v[124:127], a[252:255], a[236:239], v[124:127]\n"
    "s_nop 1\n"
    "v_mfma_f32_16x16x32_bf16 v[156:159], a[252:255], a[240:243], v[156:159]\n"
    "v_permlane16_swap_b32 v36, v38\n"
    "v_mfma_f32_16x16x32_bf16 v[188:191], a[252:255], a[244:247], v[188:191]\n"
    "v_permlane16_swap_b32 v37, v39\n"
    "v_pk_mul_f32 v[16:17], v[16:17], v[240:241] op_sel_hi:[1,0]\n"
    "v_pk_mul_f32 v[18:19], v[18:19], v[240:241] op_sel_hi:[1,0]\n"
    "v_pk_mul_f32 v[20:21], v[20:21], v[240:241] op_sel_hi:[1,0]\n"
    "v_pk_mul_f32 v[22:23], v[22:23], v[240:241] op_sel_hi:[1,0]\n"
    "v_cvt_pk_bf16_f32 v8, v16, v17\n"
    "v_cvt_pk_bf16_f32 v9, v18, v19\n"
    "v_cvt_pk_bf16_f32 v10, v20, v21\n"
    "v_cvt_pk_bf16_f32 v11, v22, v23\n"
    "v_pk_mul_f32 v[48:49], v[48:49], v[240:241] op_sel_hi:[1,0]\n"
    "v_pk_mul_f32 v[50:51], v[50:51], v[240:241] op_sel_hi:[1,0]\n"
    "v_permlane16_swap_b32 v8, v10\n"
    "v_permlane16_swap_b32 v9, v11\n"
    "v_pk_mul_f32 v[52:53], v[52:53], v[240:241] op_sel_hi:[1,0]\n"
    "v_pk_mul_f32 v[54:55], v[54:55], v[240:241] op_sel_hi:[1,0]\n"
    "v_cvt_pk_bf16_f32 v40, v48, v49\n"
    "v_cvt_pk_bf16_f32 v41, v50, v51\n"
    "v_cvt_pk_bf16_f32 v42, v52, v53\n"
    "v_cvt_pk_bf16_f32 v43, v54, v55\n"
    "v_pk_mul_f32 v[64:65], v[64:65], v[240:241] op_sel_hi:[1,0]\n"
    "v_pk_mul_f32 v[66:67], v[66:67], v[240:241] op_sel_hi:[1,0]\n"
    "v_permlane16_swap_b32 v40, v42\n"
    "v_permlane16_swap_b32 v41, v43\n"
    "v_pk_mul_f32 v[68:69], v[68:69], v[240:241] op_sel_hi:[1,0]\n"
    "v_pk_mul_f32 v[70:71], v[70:71], v[240:241] op_sel_hi:[1,0]\n"
    "v_cvt_pk_bf16_f32 v64, v64, v65\n"
    "v_cvt_pk_bf16_f32 v65, v66, v67\n"
    "v_cvt_pk_bf16_f32 v66, v68, v69\n"
    "v_cvt_pk_bf16_f32 v67, v70, v71\n"
    "v_pk_mul_f32 v[96:97], v[96:97], v[240:241] op_sel_hi:[1,0]\n"
    "v_pk_mul_f32 v[98:99], v[98:99], v[240:241] op_sel_hi:[1,0]\n"
    "v_permlane16_swap_b32 v64, v66\n"
    "v_permlane16_swap_b32 v65, v67\n"
    "v_pk_mul_f32 v[100:101], v[100:101], v[240:241] op_sel_hi:[1,0]\n"
    "v_pk_mul_f32 v[102:103], v[102:103], v[240:241] op_sel_hi:[1,0]\n"
    "v_cvt_pk_bf16_f32 v96, v96, v97\n"
    "v_cvt_pk_bf16_f32 v97, v98, v99\n"
    "v_cvt_pk_bf16_f32 v98, v100, v101\n"
    "v_cvt_pk_bf16_f32 v99, v102, v103\n"
    "v_pk_mul_f32 v[72:73], v[72:73], v[240:241] op_sel_hi:[1,0]\n"
    "v_pk_mul_f32 v[74:75], v[74:75], v[240:241] op_sel_hi:[1,0]\n"
    "v_permlane16_swap_b32 v96, v98\n"
    "v_permlane16_swap_b32 v97, v99\n"
    "v_pk_mul_f32 v[76:77], v[76:77], v[240:241] op_sel_hi:[1,0]\n"
    "v_pk_mul_f32 v[78:79], v[78:79], v[240:241] op_sel_hi:[1,0]\n"
    "v_cvt_pk_bf16_f32 v68, v72, v73\n"
    "v_cvt_pk_bf16_f32 v69, v74, v75\n"
    "v_cvt_pk_bf16_f32 v70, v76, v77\n"
    "v_cvt_pk_bf16_f32 v71, v78, v79\n"
    "v_pk_mul_f32 v[104:105], v[104:105], v[240:241] op_sel_hi:[1,0]\n"
    "v_pk_mul_f32 v[106:107], v[106:107], v[240:241] op_sel_hi:[1,0]\n"
    "v_permlane16_swap_b32 v68, v70\n"
    "v_permlane16_swap_b32 v69, v71\n"
    "v_pk_mul_f32 v[108:109], v[108:109], v[240:241] op_sel_hi:[1,0]\n"
    "v_pk_mul_f32 v[110:111], v[110:111], v[240:241] op_sel_hi:[1,0]\n"
    "v_cvt_pk_bf16_f32 v100, v104, v105\n"
    "v_cvt_pk_bf16_f32 v101, v106, v107\n"
    "v_cvt_pk_bf16_f32 v102, v108, v109\n"
    "v_cvt_pk_bf16_f32 v103, v110, v111\n"
    "v_pk_mul_f32 v[80:81], v[80:81], v[240:241] op_sel_hi:[1,0]\n"
    "v_pk_mul_f32 v[82:83], v[82:83], v[240:241] op_sel_hi:[1,0]\n"
    "v_permlane16_swap_b32 v100, v102\n"
    "v_permlane16_swap_b32 v101, v103\n"
    "v_pk_mul_f32 v[84:85], v[84:85], v[240:241] op_sel_hi:[1,0]\n"
    "v_pk_mul_f32 v[86:87], v[86:87], v[240:241] op_sel_hi:[1,0]\n"
    "v_cvt_pk_bf16_f32 v72, v80, v81\n"
    "v_cvt_pk_bf16_f32 v73, v82, v83\n"
    "v_cvt_pk_bf16_f32 v74, v84, v85\n"
    "v_cvt_pk_bf16_f32 v75, v86, v87\n"
    "v_pk_mul_f32 v[112:113], v[112:113], v[240:241] op_sel_hi:[1,0]\n"
    "v_pk_mul_f32 v[114:115], v[114:115], v[240:241] op_sel_hi:[1,0]\n"
    "v_permlane16_swap_b32 v72, v74\n"
    "v_permlane16_swap_b32 v73, v75\n"
    "v_pk_mul_f32 v[116:117], v[116:117], v[240:241] op_sel_hi:[1,0]\n"
    "v_pk_mul_f32 v[118:119], v[118:119], v[240:241] op_sel_hi:[1,0]\n"
    "v_cvt_pk_bf16_f32 v104, v112, v113\n"
    "v_cvt_pk_bf16_f32 v105, v114, v115\n"
    "v_cvt_pk_bf16_f32 v106, v116, v117\n"
    "v_cvt_pk_bf16_f32 v107, v118, v119\n"
    "v_pk_mul_f32 v[128:129], v[128:129], v[240:241] op_sel_hi:[1,0]\n"
    "v_pk_mul_f32 v[130:131], v[130:131], v[240:241] op_sel_hi:[1,0]\n"
    "v_permlane16_swap_b32 v104, v106\n"
    "v_permlane16_swap_b32 v105, v107\n"
    "v_pk_mul_f32 v[132:133], v[132:133], v[240:241] op_sel_hi:[1,0]\n"
    "v_pk_mul_f32 v[134:135], v[134:135], v[240:241] op_sel_hi:[1,0]\n"
    "v_cvt_pk_bf16_f32 v128, v128, v129\n"
    "v_cvt_pk_bf16_f32 v129, v130, v131\n"
    "v_cvt_pk_bf16_f32 v130, v132, v133\n"
    "v_cvt_pk_bf16_f32 v131, v134, v135\n"
    "v_pk_mul_f32 v[160:161], v[160:161], v[240:241] op_sel_hi:[1,0]\n"
    "v_pk_mul_f32 v[162:163], v[162:163], v[240:241] op_sel_hi:[1,0]\n"
    "v_permlane16_swap_b32 v128, v130\n"
    "v_permlane16_swap_b32 v129, v131\n"
    "v_pk_mul_f32 v[164:165], v[164:165], v[240:241] op_sel_hi:[1,0]\n"
    "v_pk_mul_f32 v[166:167], v[166:167], v[240:241] op_sel_hi:[1,0]\n"
    "v_cvt_pk_bf16_f32 v160, v160, v161\n"
    "v_cvt_pk_bf16_f32 v161, v162, v163\n"
    "v_cvt_pk_bf16_f32 v162, v164, v165\n"
    "v_cvt_pk_bf16_f32 v163, v166, v167\n"
    "v_pk_mul_f32 v[136:137], v[136:137], v[240:241] op_sel_hi:[1,0]\n"
    "v_pk_mul_f32 v[138:139], v[138:139], v[240:241] op_sel_hi:[1,0]\n"
    "v_permlane16_swap_b32 v160, v162\n"
    "v_permlane16_swap_b32 v161, v163\n"
    "v_pk_mul_f32 v[140:141], v[140:141], v[240:241] op_sel_hi:[1,0]\n"
    "v_pk_mul_f32 v[142:143], v[142:143], v[240:241] op_sel_hi:[1,0]\n"
    "v_cvt_pk_bf16_f32 v132, v136, v137\n"
    "v_cvt_pk_bf16_f32 v133, v138, v139\n"
    "v_cvt_pk_bf16_f32 v134, v140, v141\n"
    "v_cvt_pk_bf16_f32 v135, v142, v143\n"
    "v_pk_mul_f32 v[168:169], v[168:169], v[240:241] op_sel_hi:[1,0]\n"
    "v_pk_mul_f32 v[170:171], v[170:171], v[240:241] op_sel_hi:[1,0]\n"
    "v_permlane16_swap_b32 v132, v134\n"
    "v_permlane16_swap_b32 v133, v135\n"
    "v_pk_mul_f32 v[172:173], v[172:173], v[240:241] op_sel_hi:[1,0]\n"
    "v_pk_mul_f32 v[174:175], v[174:175], v[240:241] op_sel_hi:[1,0]\n"
    "v_cvt_pk_bf16_f32 v164, v168, v169\n"
    "v_cvt_pk_bf16_f32 v165, v170, v171\n"
    "v_cvt_pk_bf16_f32 v166, v172, v173\n"
    "v_cvt_pk_bf16_f32 v167, v174, v175\n"
    "v_pk_mul_f32 v[144:145], v[144:145], v[240:241] op_sel_hi:[1,0]\n"
    "v_pk_mul_f32 v[146:147], v[146:147], v[240:241] op_sel_hi:[1,0]\n"
    "v_permlane16_swap_b32 v164, v166\n"
    "v_permlane16_swap_b32 v165, v167\n"
    "v_pk_mul_f32 v[148:149], v[148:149], v[240:241] op_sel_hi:[1,0]\n"
    "v_pk_mul_f32 v[150:151], v[150:151], v[240:241] op_sel_hi:[1,0]\n"
    "v_cvt_pk_bf16_f32 v136, v144, v145\n"
    "v_cvt_pk_bf16_f32 v137, v146, v147\n"
    "v_cvt_pk_bf16_f32 v138, v148, v149\n"
    "v_cvt_pk_bf16_f32 v139, v150, v151\n"
    "v_pk_mul_f32 v[176:177], v[176:177], v[240:241] op_sel_hi:[1,0]\n"
    "v_pk_mul_f32 v[178:179], v[178:179], v[240:241] op_sel_hi:[1,0]\n"
    "v_permlane16_swap_b32 v136, v138\n"
    "v_permlane16_swap_b32 v137, v139\n"
    "v_pk_mul_f32 v[180:181], v[180:181], v[240:241] op_sel_hi:[1,0]\n"
    "v_pk_mul_f32 v[182:183], v[182:183], v[240:241] op_sel_hi:[1,0]\n"
    "v_cvt_pk_bf16_f32 v168, v176, v177\n"
    "v_cvt_pk_bf16_f32 v169, v178, v179\n"
    "v_cvt_pk_bf16_f32 v170, v180, v181\n"
    "v_cvt_pk_bf16_f32 v171, v182, v183\n"
    "v_pk_mul_f32 v[24:25], v[24:25], v[240:241] op_sel_hi:[1,0]\n"
    "v_pk_mul_f32 v[26:27], v[26:27], v[240:241] op_sel_hi:[1,0]\n"
    "v_permlane16_swap_b32 v168, v170\n"
    "v_permlane16_swap_b32 v169, v171\n"
    "v_pk_mul_f32 v[28:29], v[28:29], v[240:241] op_sel_hi:[1,0]\n"
    "v_pk_mul_f32 v[30:31], v[30:31], v[240:241] op_sel_hi:[1,0]\n"
    "v_cvt_pk_bf16_f32 v12, v24, v25\n"
    "v_cvt_pk_bf16_f32 v13, v26, v27\n"
    "v_cvt_pk_bf16_f32 v14, v28, v29\n"
    "v_cvt_pk_bf16_f32 v15, v30, v31\n"
    "v_pk_mul_f32 v[56:57], v[56:57], v[240:241] op_sel_hi:[1,0]\n"
    "v_pk_mul_f32 v[58:59], v[58:59], v[240:241] op_sel_hi:[1,0]\n"
    "v_permlane16_swap_b32 v12, v14\n"
    "v_permlane16_swap_b32 v13, v15\n"
    "v_pk_mul_f32 v[60:61], v[60:61], v[240:241] op_sel_hi:[1,0]\n"
    "v_pk_mul_f32 v[62:63], v[62:63], v[240:241] op_sel_hi:[1,0]\n"
    "v_cvt_pk_bf16_f32 v44, v56, v57\n"
    "v_cvt_pk_bf16_f32 v45, v58, v59\n"
    "v_cvt_pk_bf16_f32 v46, v60, v61\n"
    "v_cvt_pk_bf16_f32 v47, v62, v63\n"
    "v_pk_mul_f32 v[88:89], v[88:89], v[240:241] op_sel_hi:[1,0]\n"
    "v_pk_mul_f32 v[90:91], v[90:91], v[240:241] op_sel_hi:[1,0]\n"
    "v_permlane16_swap_b32 v44, v46\n"
    "v_permlane16_swap_b32 v45, v47\n"
    "v_pk_mul_f32 v[92:93], v[92:93], v[240:241] op_sel_hi:[1,0]\n"
    "v_pk_mul_f32 v[94:95], v[94:95], v[240:241] op_sel_hi:[1,0]\n"
    "v_cvt_pk_bf16_f32 v76, v88, v89\n"
    "v_cvt_pk_bf16_f32 v77, v90, v91\n"
    "v_cvt_pk_bf16_f32 v78, v92, v93\n"
    "v_cvt_pk_bf16_f32 v79, v94, v95\n"
    "v_pk_mul_f32 v[120:121], v[120:121], v[240:241] op_sel_hi:[1,0]\n"
    "v_pk_mul_f32 v[122:123], v[122:123], v[240:241] op_sel_hi:[1,0]\n"
    "v_permlane16_swap_b32 v76, v78\n"
    "v_permlane16_swap_b32 v77, v79\n"
    "v_pk_mul_f32 v[124:125], v[124:125], v[240:241] op_sel_hi:[1,0]\n"
    "v_pk_mul_f32 v[126:127], v[126:127], v[240:241] op_sel_hi:[1,0]\n"
    "v_cvt_pk_bf16_f32 v108, v120, v121\n"
    "v_cvt_pk_bf16_f32 v109, v122, v123\n"
    "v_cvt_pk_bf16_f32 v110, v124, v125\n"
    "v_cvt_pk_bf16_f32 v111, v126, v127\n"
    "v_pk_mul_f32 v[152:153], v[152:153], v[240:241] op_sel_hi:[1,0]\n"
    "v_pk_mul_f32 v[154:155], v[154:155], v[240:241] op_sel_hi:[1,0]\n"
    "v_permlane16_swap_b32 v108, v110\n"
    "v_permlane16_swap_b32 v109, v111\n"
    "v_pk_mul_f32 v[156:157], v[156:157], v[240:241] op_sel_hi:[1,0]\n"
    "v_pk_mul_f32 v[158:159], v[158:159], v[240:241] op_sel_hi:[1,0]\n"
    "v_cvt_pk_bf16_f32 v140, v152, v153\n"
    "v_cvt_pk_bf16_f32 v141, v154, v155\n"
    "v_cvt_pk_bf16_f32 v142, v156, v157\n"
    "v_cvt_pk_bf16_f32 v143, v158, v159\n"
    "v_pk_mul_f32 v[184:185], v[184:185], v[240:241] op_sel_hi:[1,0]\n"
    "v_pk_mul_f32 v[186:187], v[186:187], v[240:241] op_sel_hi:[1,0]\n"
    "v_permlane16_swap_b32 v140, v142\n"
    "v_permlane16_swap_b32 v141, v143\n"
    "v_pk_mul_f32 v[188:189], v[188:189], v[240:241] op_sel_hi:[1,0]\n"
    "v_pk_mul_f32 v[190:191], v[190:191], v[240:241] op_sel_hi:[1,0]\n"
    "v_cvt_pk_bf16_f32 v172, v184, v185\n"
    "v_cvt_pk_bf16_f32 v173, v186, v187\n"
    "v_cvt_pk_bf16_f32 v174, v188, v189\n"
    "v_cvt_pk_bf16_f32 v175, v190, v191\n"
    "s_nop 1\n"
    "v_permlane16_swap_b32 v172, v174\n"
    "v_permlane16_swap_b32 v173, v175\n"
    "s_waitcnt vmcnt(19) lgkmcnt(0)\n"
    "s_cmp_lg_u64 s[78:79], 0\n"
    "s_cbranch_scc1 L_pub0%=\n"
    "s_mov_b64 exec, 3\n"
    "global_store_dwordx2 v252, v[248:249], %[pcnt] offset:0 sc1\n"
    "s_mov_b64 exec, -1\n"
    "s_mov_b32 s83, 2\n"
    "L_pub0%=:\n"
    "s_branch L_p0%=\n"
    "L_final%=:\n"
    "s_add_u32 s72, s72, 98304\n"
    "s_cmp_lg_u64 s[78:79], 0\n"
    "s_cbranch_scc1 L_exit%=\n"
    "s_mov_b32 s60, 0x40000\n"
    "L_poll0%=:\n"
    "v_cmp_ne_u64 vcc, v[246:247], v[248:249]\n"
    "s_sub_u32 s60, s60, 1\n"
    "s_cbranch_scc1 L_poll0%=_ok\n"
    "s_cbranch_vccz L_poll0%=_ok\n"
    "s_sleep 8\n"
    "global_load_dwordx2 v[246:247], v241, %[pcnt] offset:0 sc1\n"
    "s_waitcnt vmcnt(0)\n"
    "s_branch L_poll0%=\n"
    "L_poll0%=_ok:\n"
    "s_waitcnt lgkmcnt(0)\n"
    "s_barrier\n"
    "s_mov_b64 s[36:37], %[pa]\n"
    "s_mov_b32 s38, 196608\n"
    "s_mov_b32 s39, 0x20000\n"
    "s_mov_b64 s[40:41], %[pb]\n"
    "s_mov_b32 s42, 294912\n"
    "s_mov_b32 s43, 0x20000\n"
    "s_mov_b64 s[44:45], %[pc]\n"
    "s_mov_b32 s46, 221184\n"
    "s_mov_b32 s47, 0x20000\n"
    "s_bitcmp1_b32 %[wv], 0\n"
    "s_cselect_b32 s56, 8, 0\n"
    "s_cselect_b32 s57, 7, 1\n"
    "s_cselect_b32 s58, 6, 2\n"
    "s_cselect_b32 s59, 5, 3\n"
    "s_cselect_b32 s60, 4, 4\n"
    "s_cselect_b32 s61, 3, 5\n"
    "s_cselect_b32 s62, 2, 6\n"
    "s_cselect_b32 s63, 1, 7\n"
    "s_cselect_b32 s64, 0, 8\n"
    "s_lshl_b32 s56, s56, 10\n"
    "s_lshl_b32 s57, s57, 10\n"
    "s_lshl_b32 s58, s58, 10\n"
    "s_lshl_b32 s59, s59, 10\n"
    "s_lshl_b32 s60, s60, 10\n"
    "s_lshl_b32 s61, s61, 10\n"
    "s_lshl_b32 s62, s62, 10\n"
    "s_lshl_b32 s63, s63, 10\n"
    "s_lshl_b32 s64, s64, 10\n"
    "s_bitcmp1_b32 %[wv], 1\n"
    "s_cselect_b32 s65, 0xc00, 0\n"
    "s_cselect_b32 s66, 0, 0xc00\n"
    "s_cselect_b32 s67, 110592, 0\n"
    "s_mov_b32 s69, 3\n"
    "s_mov_b32 s68, 0\n"
    "v_mov_b32 v244, v242\n"
    "v_mov_b32 v245, v242\n"
    "buffer_load_dwordx4 v[0:3], v244, s[36:39], s65 offen sc1\n"
    "buffer_load_dwordx4 v[4:7], v244, s[36:39], s65 offen offset:1024 sc1\n"
    "buffer_load_dwordx4 v[8:11], v244, s[36:39], s65 offen offset:2048 sc1\n"
    "buffer_load_dwordx4 v[12:15], v244, s[36:39], s66 offen sc1\n"
    "buffer_load_dwordx4 v[16:19], v244, s[36:39], s66 offen offset:1024 sc1\n"
    "buffer_load_dwordx4 v[20:23], v244, s[36:39], s66 offen offset:2048 sc1\n"
    "v_add_u32 v244, 0x1800, v244\n"
    "buffer_load_dwordx4 v[24:27], v245, s[40:43], s56 offen sc1\n"
    "buffer_load_dwordx4 v[28:31], v245, s[40:43], s57 offen sc1\n"
    "buffer_load_dwordx4 v[32:35], v245, s[40:43], s58 offen sc1\n"
    "buffer_load_dwordx4 v[36:39], v245, s[40:43], s59 offen sc1\n"
    "buffer_load_dwordx4 v[40:43], v245, s[40:43], s60 offen sc1\n"
    "buffer_load_dwordx4 v[44:47], v245, s[40:43], s61 offen sc1\n"
    "buffer_load_dwordx4 v[48:51], v245, s[40:43], s62 offen sc1\n"
    "buffer_load_dwordx4 v[52:55], v245, s[40:43], s63 offen sc1\n"
    "buffer_load_dwordx4 v[56:59], v245, s[40:43], s64 offen sc1\n"
    "v_add_u32 v245, 0x2400, v245\n"
    "buffer_load_dwordx4 v[60:63], v244, s[36:39], s65 offen sc1\n"
    "buffer_load_dwordx4 v[64:67], v244, s[36:39], s65 offen offset:1024 sc1\n"
    "buffer_load_dwordx4 v[68:71], v244, s[36:39], s65 offen offset:2048 sc1\n"
    "buffer_load_dwordx4 v[72:75], v244, s[36:39], s66 offen sc1\n"
    "buffer_load_dwordx4 v[76:79], v244, s[36:39], s66 offen offset:1024 sc1\n"
    "buffer_load_dwordx4 v[80:83], v244, s[36:39], s66 offen offset:2048 sc1\n"
    "v_add_u32 v244, 0x1800, v244\n"
    "buffer_load_dwordx4 v[84:87], v245, s[40:43], s56 offen sc1\n"
    "buffer_load_dwordx4 v[88:91], v245, s[40:43], s57 offen sc1\n"
    "buffer_load_dwordx4 v[92:95], v245, s[40:43], s58 offen sc1\n"
    "buffer_load_dwordx4 v[96:99], v245, s[40:43], s59 offen sc1\n"
    "buffer_load_dwordx4 v[100:103], v245, s[40:43], s60 offen sc1\n"
    "buffer_load_dwordx4 v[104:107], v245, s[40:43], s61 offen sc1\n"
    "buffer_load_dwordx4 v[108:111], v245, s[40:43], s62 offen sc1\n"
    "buffer_load_dwordx4 v[112:115], v245, s[40:43], s63 offen sc1\n"
    "buffer_load_dwordx4 v[116:119], v245, s[40:43], s64 offen sc1\n"
    "v_add_u32 v245, 0x2400, v245\n"
    "buffer_load_dwordx4 v[120:123], v244, s[36:39], s65 offen sc1\n"
    "buffer_load_dwordx4 v[124:127], v244, s[36:39], s65 offen offset:1024 sc1\n"
    "buffer_load_dwordx4 v[128:131], v244, s[36:39], s65 offen offset:2048 sc1\n"
    "buffer_load_dwordx4 v[132:135], v244, s[36:39], s66 offen sc1\n"
    "buffer_load_dwordx4 v[136:139], v244, s[36:39], s66 offen offset:1024 sc1\n"
    "buffer_load_dwordx4 v[140:143], v244, s[36:39], s66 offen offset:2048 sc1\n"
    "v_add_u32 v244, 0x1800, v244\n"
    "buffer_load_dwordx4 v[144:147], v245, s[40:43], s56 offen sc1\n"
    "buffer_load_dwordx4 v[148:151], v245, s[40:43], s57 offen sc1\n"
    "buffer_load_dwordx4 v[152:155], v245, s[40:43], s58 offen sc1\n"
    "buffer_load_dwordx4 v[156:159], v245, s[40:43], s59 offen sc1\n"
    "buffer_load_dwordx4 v[160:163], v245, s[40:43], s60 offen sc1\n"
    "buffer_load_dwordx4 v[164:167], v245, s[40:43], s61 offen sc1\n"
    "buffer_load_dwordx4 v[168:171], v245, s[40:43], s62 offen sc1\n"
    "buffer_load_dwordx4 v[172:175], v245, s[40:43], s63 offen sc1\n"
    "buffer_load_dwordx4 v[176:179], v245, s[40:43], s64 offen sc1\n"
    "v_add_u32 v245, 0x2400, v245\n"
    "s_add_u32 s73, s72, 0\n"
    "v_accvgpr_read_b32 v228, a0\n"
    "v_accvgpr_read_b32 v229, a1\n"
    "v_accvgpr_read_b32 v230, a2\n"
    "v_accvgpr_read_b32 v231, a3\n"
    "v_accvgpr_read_b32 v232, a4\n"
    "v_accvgpr_read_b32 v233, a5\n"
    "v_accvgpr_read_b32 v234, a6\n"
    "v_accvgpr_read_b32 v235, a7\n"
    "v_accvgpr_write_b32 a0, 0\n"
    "v_accvgpr_write_b32 a1, 0\n"
    "v_accvgpr_write_b32 a2, 0\n"
    "v_accvgpr_write_b32 a3, 0\n"
    "v_accvgpr_write_b32 a4, 0\n"
    "v_accvgpr_write_b32 a5, 0\n"
    "v_accvgpr_write_b32 a6, 0\n"
    "v_accvgpr_write_b32 a7, 0\n"
    "v_pk_mul_f32 v[228:229], v[228:229], v[214:215] op_sel:[0,0] op_sel_hi:[1,0]\n"
    "v_pk_mul_f32 v[230:231], v[230:231], v[214:215] op_sel:[0,0] op_sel_hi:[1,0]\n"
    "v_pk_mul_f32 v[232:233], v[232:233], v[214:215] op_sel:[0,0] op_sel_hi:[1,0]\n"
    "v_pk_mul_f32 v[234:235], v[234:235], v[214:215] op_sel:[0,0] op_sel_hi:[1,0]\n"
    "v_cvt_pk_bf16_f32 v220, v228, v229\n"
    "v_cvt_pk_bf16_f32 v221, v230, v231\n"
    "v_cvt_pk_bf16_f32 v222, v232, v233\n"
    "v_cvt_pk_bf16_f32 v223, v234, v235\n"
    "buffer_store_dwordx4 v[220:223], v242, s[52:55], s73 offen sc1\n"
    "v_accvgpr_read_b32 v228, a32\n"
    "v_accvgpr_read_b32 v229, a33\n"
    "v_accvgpr_read_b32 v230, a34\n"
    "v_accvgpr_read_b32 v231, a35\n"
    "v_accvgpr_read_b32 v232, a36\n"
    "v_accvgpr_read_b32 v233, a37\n"
    "v_accvgpr_read_b32 v234, a38\n"
    "v_accvgpr_read_b32 v235, a39\n"
    "v_accvgpr_write_b32 a32, 0\n"
    "v_accvgpr_write_b32 a33, 0\n"
    "v_accvgpr_write_b32 a34, 0\n"
    "v_accvgpr_write_b32 a35, 0\n"
    "v_accvgpr_write_b32 a36, 0\n"
    "v_accvgpr_write_b32 a37, 0\n"
    "v_accvgpr_write_b32 a38, 0\n"
    "v_accvgpr_write_b32 a39, 0\n"
    "v_pk_mul_f32 v[228:229], v[228:229], v[214:215] op_sel:[0,1] op_sel_hi:[1,1]\n"
    "v_pk_mul_f32 v[230:231], v[230:231], v[214:215] op_sel:[0,1] op_sel_hi:[1,1]\n"
    "v_pk_mul_f32 v[232:233], v[232:233], v[214:215] op_sel:[0,1] op_sel_hi:[1,1]\n"
    "v_pk_mul_f32 v[234:235], v[234:235], v[214:215] op_sel:[0,1] op_sel_hi:[1,1]\n"
    "v_cvt_pk_bf16_f32 v224, v228, v229\n"
    "v_cvt_pk_bf16_f32 v225, v230, v231\n"
    "v_cvt_pk_bf16_f32 v226, v232, v233\n"
    "v_cvt_pk_bf16_f32 v227, v234, v235\n"
    "buffer_store_dwordx4 v[224:227], v242, s[52:55], s73 offen offset:1024 sc1\n"
    "v_accvgpr_read_b32 v228, a64\n"
    "v_accvgpr_read_b32 v229, a65\n"
    "v_accvgpr_read_b32 v230, a66\n"
    "v_accvgpr_read_b32 v231, a67\n"
    "v_accvgpr_read_b32 v232, a68\n"
    "v_accvgpr_read_b32 v233, a69\n"
    "v_accvgpr_read_b32 v234, a70\n"
    "v_accvgpr_read_b32 v235, a71\n"
    "v_accvgpr_write_b32 a64, 0\n"
    "v_accvgpr_write_b32 a65, 0\n"
    "v_accvgpr_write_b32 a66, 0\n"
    "v_accvgpr_write_b32 a67, 0\n"
    "v_accvgpr_write_b32 a68, 0\n"
    "v_accvgpr_write_b32 a69, 0\n"
    "v_accvgpr_write_b32 a70, 0\n"
    "v_accvgpr_write_b32 a71, 0\n"
    "v_pk_mul_f32 v[228:229], v[228:229], v[216:217] op_sel:[0,0] op_sel_hi:[1,0]\n"
    "v_pk_mul_f32 v[230:231], v[230:231], v[216:217] op_sel:[0,0] op_sel_hi:[1,0]\n"
    "v_pk_mul_f32 v[232:233], v[232:233], v[216:217] op_sel:[0,0] op_sel_hi:[1,0]\n"
    "v_pk_mul_f32 v[234:235], v[234:235], v[216:217] op_sel:[0,0] op_sel_hi:[1,0]\n"
    "v_cvt_pk_bf16_f32 v220, v228, v229\n"
    "v_cvt_pk_bf16_f32 v221, v230, v231\n"
    "v_cvt_pk_bf16_f32 v222, v232, v233\n"
    "v_cvt_pk_bf16_f32 v223, v234, v235\n"
    "buffer_store_dwordx4 v[220:223], v242, s[52:55], s73 offen offset:2048 sc1\n"
    "v_accvgpr_read_b32 v228, a96\n"
    "v_accvgpr_read_b32 v229, a97\n"
    "v_accvgpr_read_b32 v230, a98\n"
    "v_accvgpr_read_b32 v231, a99\n"
    "v_accvgpr_read_b32 v232, a100\n"
    "v_accvgpr_read_b32 v233, a101\n"
    "v_accvgpr_read_b32 v234, a102\n"
    "v_accvgpr_read_b32 v235, a103\n"
    "v_accvgpr_write_b32 a96, 0\n"
    "v_accvgpr_write_b32 a97, 0\n"
    "v_accvgpr_write_b32 a98, 0\n"
    "v_accvgpr_write_b32 a99, 0\n"
    "v_accvgpr_write_b32 a100, 0\n"
    "v_accvgpr_write_b32 a101, 0\n"
    "v_accvgpr_write_b32 a102, 0\n"
    "v_accvgpr_write_b32 a103, 0\n"
    "v_pk_mul_f32 v[228:229], v[228:229], v[216:217] op_sel:[0,1] op_sel_hi:[1,1]\n"
    "v_pk_mul_f32 v[230:231], v[230:231], v[216:217] op_sel:[0,1] op_sel_hi:[1,1]\n"
    "v_pk_mul_f32 v[232:233], v[232:233], v[216:217] op_sel:[0,1] op_sel_hi:[1,1]\n"
    "v_pk_mul_f32 v[234:235], v[234:235], v[216:217] op_sel:[0,1] op_sel_hi:[1,1]\n"
    "v_cvt_pk_bf16_f32 v224, v228, v229\n"
    "v_cvt_pk_bf16_f32 v225, v230, v231\n"
    "v_cvt_pk_bf16_f32 v226, v232, v233\n"
    "v_cvt_pk_bf16_f32 v227, v234, v235\n"
    "buffer_store_dwordx4 v[224:227], v242, s[52:55], s73 offen offset:3072 sc1\n"
    "s_add_u32 s73, s72, 4096\n"
    "v_accvgpr_read_b32 v228, a128\n"
    "v_accvgpr_read_b32 v229, a129\n"
    "v_accvgpr_read_b32 v230, a130\n"
    "v_accvgpr_read_b32 v231, a131\n"
    "v_accvgpr_read_b32 v232, a132\n"
    "v_accvgpr_read_b32 v233, a133\n"
    "v_accvgpr_read_b32 v234, a134\n"
    "v_accvgpr_read_b32 v235, a135\n"
    "v_accvgpr_write_b32 a128, 0\n"
    "v_accvgpr_write_b32 a129, 0\n"
    "v_accvgpr_write_b32 a130, 0\n"
    "v_accvgpr_write_b32 a131, 0\n"
    "v_accvgpr_write_b32 a132, 0\n"
    "v_accvgpr_write_b32 a133, 0\n"
    "v_accvgpr_write_b32 a134, 0\n"
    "v_accvgpr_write_b32 a135, 0\n"
    "v_pk_mul_f32 v[228:229], v[228:229], v[218:219] op_sel:[0,0] op_sel_hi:[1,0]\n"
    "v_pk_mul_f32 v[230:231], v[230:231], v[218:219] op_sel:[0,0] op_sel_hi:[1,0]\n"
    "v_pk_mul_f32 v[232:233], v[232:233], v[218:219] op_sel:[0,0] op_sel_hi:[1,0]\n"
    "v_pk_mul_f32 v[234:235], v[234:235], v[218:219] op_sel:[0,0] op_sel_hi:[1,0]\n"
    "v_cvt_pk_bf16_f32 v220, v228, v229\n"
    "v_cvt_pk_bf16_f32 v221, v230, v231\n"
    "v_cvt_pk_bf16_f32 v222, v232, v233\n"
    "v_cvt_pk_bf16_f32 v223, v234, v235\n"
    "buffer_store_dwordx4 v[220:223], v242, s[52:55], s73 offen sc1\n"
    "v_accvgpr_read_b32 v228, a160\n"
    "v_accvgpr_read_b32 v229, a161\n"
    "v_accvgpr_read_b32 v230, a162\n"
    "v_accvgpr_read_b32 v231, a163\n"
    "v_accvgpr_read_b32 v232, a164\n"
    "v_accvgpr_read_b32 v233, a165\n"
    "v_accvgpr_read_b32 v234, a166\n"
    "v_accvgpr_read_b32 v235, a167\n"
    "v_accvgpr_write_b32 a160, 0\n"
    "v_accvgpr_write_b32 a161, 0\n"
    "v_accvgpr_write_b32 a162, 0\n"
    "v_accvgpr_write_b32 a163, 0\n"
    "v_accvgpr_write_b32 a164, 0\n"
    "v_accvgpr_write_b32 a165, 0\n"
    "v_accvgpr_write_b32 a166, 0\n"
    "v_accvgpr_write_b32 a167, 0\n"
    "v_pk_mul_f32 v[228:229], v[228:229], v[218:219] op_sel:[0,1] op_sel_hi:[1,1]\n"
    "v_pk_mul_f32 v[230:231], v[230:231], v[218:219] op_sel:[0,1] op_sel_hi:[1,1]\n"
    "v_pk_mul_f32 v[232:233], v[232:233], v[218:219] op_sel:[0,1] op_sel_hi:[1,1]\n"
    "v_pk_mul_f32 v[234:235], v[234:235], v[218:219] op_sel:[0,1] op_sel_hi:[1,1]\n"
    "v_cvt_pk_bf16_f32 v224, v228, v229\n"
    "v_cvt_pk_bf16_f32 v225, v230, v231\n"
    "v_cvt_pk_bf16_f32 v226, v232, v233\n"
    "v_cvt_pk_bf16_f32 v227, v234, v235\n"
    "buffer_store_dwordx4 v[224:227], v242, s[52:55], s73 offen offset:1024 sc1\n"
    "s_add_u32 s73, s72, 6144\n"
    "v_accvgpr_read_b32 v228, a8\n"
    "v_accvgpr_read_b32 v229, a9\n"
    "v_accvgpr_read_b32 v230, a10\n"
    "v_accvgpr_read_b32 v231, a11\n"
    "v_accvgpr_read_b32 v232, a12\n"
    "v_accvgpr_read_b32 v233, a13\n"
    "v_accvgpr_read_b32 v234, a14\n"
    "v_accvgpr_read_b32 v235, a15\n"
    "v_accvgpr_write_b32 a8, 0\n"
    "v_accvgpr_write_b32 a9, 0\n"
    "v_accvgpr_write_b32 a10, 0\n"
    "v_accvgpr_write_b32 a11, 0\n"
    "v_accvgpr_write_b32 a12, 0\n"
    "v_accvgpr_write_b32 a13, 0\n"
    "v_accvgpr_write_b32 a14, 0\n"
    "v_accvgpr_write_b32 a15, 0\n"
    "v_pk_mul_f32 v[228:229], v[228:229], v[214:215] op_sel:[0,0] op_sel_hi:[1,0]\n"
    "v_pk_mul_f32 v[230:231], v[230:231], v[214:215] op_sel:[0,0] op_sel_hi:[1,0]\n"
    "v_pk_mul_f32 v[232:233], v[232:233], v[214:215] op_sel:[0,0] op_sel_hi:[1,0]\n"
    "v_pk_mul_f32 v[234:235], v[234:235], v[214:215] op_sel:[0,0] op_sel_hi:[1,0]\n"
    "v_cvt_pk_bf16_f32 v220, v228, v229\n"
    "v_cvt_pk_bf16_f32 v221, v230, v231\n"
    "v_cvt_pk_bf16_f32 v222, v232, v233\n"
    "v_cvt_pk_bf16_f32 v223, v234, v235\n"
    "buffer_store_dwordx4 v[220:223], v242, s[52:55], s73 offen sc1\n"
    "v_accvgpr_read_b32 v228, a40\n"
    "v_accvgpr_read_b32 v229, a41\n"
    "v_accvgpr_read_b32 v230, a42\n"
    "v_accvgpr_read_b32 v231, a43\n"
    "v_accvgpr_read_b32 v232, a44\n"
    "v_accvgpr_read_b32 v233, a45\n"
    "v_accvgpr_read_b32 v234, a46\n"
    "v_accvgpr_read_b32 v235, a47\n"
    "v_accvgpr_write_b32 a40, 0\n"
    "v_accvgpr_write_b32 a41, 0\n"
    "v_accvgpr_write_b32 a42, 0\n"
    "v_accvgpr_write_b32 a43, 0\n"
    "v_accvgpr_write_b32 a44, 0\n"
    "v_accvgpr_write_b32 a45, 0\n"
    "v_accvgpr_write_b32 a46, 0\n"
    "v_accvgpr_write_b32 a47, 0\n"
    "v_pk_mul_f32 v[228:229], v[228:229], v[214:215] op_sel:[0,1] op_sel_hi:[1,1]\n"
    "v_pk_mul_f32 v[230:231], v[230:231], v[214:215] op_sel:[0,1] op_sel_hi:[1,1]\n"
    "v_pk_mul_f32 v[232:233], v[232:233], v[214:215] op_sel:[0,1] op_sel_hi:[1,1]\n"
    "v_pk_mul_f32 v[234:235], v[234:235], v[214:215] op_sel:[0,1] op_sel_hi:[1,1]\n"
    "v_cvt_pk_bf16_f32 v224, v228, v229\n"
    "v_cvt_pk_bf16_f32 v225, v230, v231\n"
    "v_cvt_pk_bf16_f32 v226, v232, v233\n"
    "v_cvt_pk_bf16_f32 v227, v234, v235\n"
    "buffer_store_dwordx4 v[224:227], v242, s[52:55], s73 offen offset:1024 sc1\n"
    "v_accvgpr_read_b32 v228, a72\n"
    "v_accvgpr_read_b32 v229, a73\n"
    "v_accvgpr_read_b32 v230, a74\n"
    "v_accvgpr_read_b32 v231, a75\n"
    "v_accvgpr_read_b32 v232, a76\n"
    "v_accvgpr_read_b32 v233, a77\n"
    "v_accvgpr_read_b32 v234, a78\n"
    "v_accvgpr_read_b32 v235, a79\n"
    "v_accvgpr_write_b32 a72, 0\n"
    "v_accvgpr_write_b32 a73, 0\n"
    "v_accvgpr_write_b32 a74, 0\n"
    "v_accvgpr_write_b32 a75, 0\n"
    "v_accvgpr_write_b32 a76, 0\n"
    "v_accvgpr_write_b32 a77, 0\n"
    "v_accvgpr_write_b32 a78, 0\n"
    "v_accvgpr_write_b32 a79, 0\n"
    "v_pk_mul_f32 v[228:229], v[228:229], v[216:217] op_sel:[0,0] op_sel_hi:[1,0]\n"
    "v_pk_mul_f32 v[230:231], v[230:231], v[216:217] op_sel:[0,0] op_sel_hi:[1,0]\n"
    "v_pk_mul_f32 v[232:233], v[232:233], v[216:217] op_sel:[0,0] op_sel_hi:[1,0]\n"
    "v_pk_mul_f32 v[234:235], v[234:235], v[216:217] op_sel:[0,0] op_sel_hi:[1,0]\n"
    "v_cvt_pk_bf16_f32 v220, v228, v229\n"
    "v_cvt_pk_bf16_f32 v221, v230, v231\n"
    "v_cvt_pk_bf16_f32 v222, v232, v233\n"
    "v_cvt_pk_bf16_f32 v223, v234, v235\n"
    "buffer_store_dwordx4 v[220:223], v242, s[52:55], s73 offen offset:2048 sc1\n"
    "v_accvgpr_read_b32 v228, a104\n"
    "v_accvgpr_read_b32 v229, a105\n"
    "v_accvgpr_read_b32 v230, a106\n"
    "v_accvgpr_read_b32 v231, a107\n"
    "v_accvgpr_read_b32 v232, a108\n"
    "v_accvgpr_read_b32 v233, a109\n"
    "v_accvgpr_read_b32 v234, a110\n"
    "v_accvgpr_read_b32 v235, a111\n"
    "v_accvgpr_write_b32 a104, 0\n"
    "v_accvgpr_write_b32 a105, 0\n"
    "v_accvgpr_write_b32 a106, 0\n"
    "v_accvgpr_write_b32 a107, 0\n"
    "v_accvgpr_write_b32 a108, 0\n"
    "v_accvgpr_write_b32 a109, 0\n"
    "v_accvgpr_write_b32 a110, 0\n"
    "v_accvgpr_write_b32 a111, 0\n"
    "v_pk_mul_f32 v[228:229], v[228:229], v[216:217] op_sel:[0,1] op_sel_hi:[1,1]\n"
    "v_pk_mul_f32 v[230:231], v[230:231], v[216:217] op_sel:[0,1] op_sel_hi:[1,1]\n"
    "v_pk_mul_f32 v[232:233], v[232:233], v[216:217] op_sel:[0,1] op_sel_hi:[1,1]\n"
    "v_pk_mul_f32 v[234:235], v[234:235], v[216:217] op_sel:[0,1] op_sel_hi:[1,1]\n"
    "v_cvt_pk_bf16_f32 v224, v228, v229\n"
    "v_cvt_pk_bf16_f32 v225, v230, v231\n"
    "v_cvt_pk_bf16_f32 v226, v232, v233\n"
    "v_cvt_pk_bf16_f32 v227, v234, v235\n"
    "buffer_store_dwordx4 v[224:227], v242, s[52:55], s73 offen offset:3072 sc1\n"
    "s_add_u32 s73, s72, 10240\n"
    "v_accvgpr_read_b32 v228, a136\n"
    "v_accvgpr_read_b32 v229, a137\n"
    "v_accvgpr_read_b32 v230, a138\n"
    "v_accvgpr_read_b32 v231, a139\n"
    "v_accvgpr_read_b32 v232, a140\n"
    "v_accvgpr_read_b32 v233, a141\n"
    "v_accvgpr_read_b32 v234, a142\n"
    "v_accvgpr_read_b32 v235, a143\n"
    "v_accvgpr_write_b32 a136, 0\n"
    "v_accvgpr_write_b32 a137, 0\n"
    "v_accvgpr_write_b32 a138, 0\n"
    "v_accvgpr_write_b32 a139, 0\n"
    "v_accvgpr_write_b32 a140, 0\n"
    "v_accvgpr_write_b32 a141, 0\n"
    "v_accvgpr_write_b32 a142, 0\n"
    "v_accvgpr_write_b32 a143, 0\n"
    "v_pk_mul_f32 v[228:229], v[228:229], v[218:219] op_sel:[0,0] op_sel_hi:[1,0]\n"
    "v_pk_mul_f32 v[230:231], v[230:231], v[218:219] op_sel:[0,0] op_sel_hi:[1,0]\n"
    "v_pk_mul_f32 v[232:233], v[232:233], v[218:219] op_sel:[0,0] op_sel_hi:[1,0]\n"
    "v_pk_mul_f32 v[234:235], v[234:235], v[218:219] op_sel:[0,0] op_sel_hi:[1,0]\n"
    "v_cvt_pk_bf16_f32 v220, v228, v229\n"
    "v_cvt_pk_bf16_f32 v221, v230, v231\n"
    "v_cvt_pk_bf16_f32 v222, v232, v233\n"
    "v_cvt_pk_bf16_f32 v223, v234, v235\n"
    "buffer_store_dwordx4 v[220:223], v242, s[52:55], s73 offen sc1\n"
    "v_accvgpr_read_b32 v228, a168\n"
    "v_accvgpr_read_b32 v229, a169\n"
    "v_accvgpr_read_b32 v230, a170\n"
    "v_accvgpr_read_b32 v231, a171\n"
    "v_accvgpr_read_b32 v232, a172\n"
    "v_accvgpr_read_b32 v233, a173\n"
    "v_accvgpr_read_b32 v234, a174\n"
    "v_accvgpr_read_b32 v235, a175\n"
    "v_accvgpr_write_b32 a168, 0\n"
    "v_accvgpr_write_b32 a169, 0\n"
    "v_accvgpr_write_b32 a170, 0\n"
    "v_accvgpr_write_b32 a171, 0\n"
    "v_accvgpr_write_b32 a172, 0\n"
    "v_accvgpr_write_b32 a173, 0\n"
    "v_accvgpr_write_b32 a174, 0\n"
    "v_accvgpr_write_b32 a175, 0\n"
    "v_pk_mul_f32 v[228:229], v[228:229], v[218:219] op_sel:[0,1] op_sel_hi:[1,1]\n"
    "v_pk_mul_f32 v[230:231], v[230:231], v[218:219] op_sel:[0,1] op_sel_hi:[1,1]\n"
    "v_pk_mul_f32 v[232:233], v[232:233], v[218:219] op_sel:[0,1] op_sel_hi:[1,1]\n"
    "v_pk_mul_f32 v[234:235], v[234:235], v[218:219] op_sel:[0,1] op_sel_hi:[1,1]\n"
    "v_cvt_pk_bf16_f32 v224, v228, v229\n"
    "v_cvt_pk_bf16_f32 v225, v230, v231\n"
    "v_cvt_pk_bf16_f32 v226, v232, v233\n"
    "v_cvt_pk_bf16_f32 v227, v234, v235\n"
    "buffer_store_dwordx4 v[224:227], v242, s[52:55], s73 offen offset:1024 sc1\n"
    "s_add_u32 s73, s72, 12288\n"
    "v_accvgpr_read_b32 v228, a16\n"
    "v_accvgpr_read_b32 v229, a17\n"
    "v_accvgpr_read_b32 v230, a18\n"
    "v_accvgpr_read_b32 v231, a19\n"
    "v_accvgpr_read_b32 v232, a20\n"
    "v_accvgpr_read_b32 v233, a21\n"
    "v_accvgpr_read_b32 v234, a22\n"
    "v_accvgpr_read_b32 v235, a23\n"
    "v_accvgpr_write_b32 a16, 0\n"
    "v_accvgpr_write_b32 a17, 0\n"
    "v_accvgpr_write_b32 a18, 0\n"
    "v_accvgpr_write_b32 a19, 0\n"
    "v_accvgpr_write_b32 a20, 0\n"
    "v_accvgpr_write_b32 a21, 0\n"
    "v_accvgpr_write_b32 a22, 0\n"
    "v_accvgpr_write_b32 a23, 0\n"
    "v_pk_mul_f32 v[228:229], v[228:229], v[214:215] op_sel:[0,0] op_sel_hi:[1,0]\n"
    "v_pk_mul_f32 v[230:231], v[230:231], v[214:215] op_sel:[0,0] op_sel_hi:[1,0]\n"
    "v_pk_mul_f32 v[232:233], v[232:233], v[214:215] op_sel:[0,0] op_sel_hi:[1,0]\n"
    "v_pk_mul_f32 v[234:235], v[234:235], v[214:215] op_sel:[0,0] op_sel_hi:[1,0]\n"
    "v_cvt_pk_bf16_f32 v220, v228, v229\n"
    "v_cvt_pk_bf16_f32 v221, v230, v231\n"
    "v_cvt_pk_bf16_f32 v222, v232, v233\n"
    "v_cvt_pk_bf16_f32 v223, v234, v235\n"
    "buffer_store_dwordx4 v[220:223], v242, s[52:55], s73 offen sc1\n"
    "v_accvgpr_read_b32 v228, a48\n"
    "v_accvgpr_read_b32 v229, a49\n"
    "v_accvgpr_read_b32 v230, a50\n"
    "v_accvgpr_read_b32 v231, a51\n"
    "v_accvgpr_read_b32 v232, a52\n"
    "v_accvgpr_read_b32 v233, a53\n"
    "v_accvgpr_read_b32 v234, a54\n"
    "v_accvgpr_read_b32 v235, a55\n"
    "v_accvgpr_write_b32 a48, 0\n"
    "v_accvgpr_write_b32 a49, 0\n"
    "v_accvgpr_write_b32 a50, 0\n"
    "v_accvgpr_write_b32 a51, 0\n"
    "v_accvgpr_write_b32 a52, 0\n"
    "v_accvgpr_write_b32 a53, 0\n"
    "v_accvgpr_write_b32 a54, 0\n"
    "v_accvgpr_write_b32 a55, 0\n"
    "v_pk_mul_f32 v[228:229], v[228:229], v[214:215] op_sel:[0,1] op_sel_hi:[1,1]\n"
    "v_pk_mul_f32 v[230:231], v[230:231], v[214:215] op_sel:[0,1] op_sel_hi:[1,1]\n"
    "v_pk_mul_f32 v[232:233], v[232:233], v[214:215] op_sel:[0,1] op_sel_hi:[1,1]\n"
    "v_pk_mul_f32 v[234:235], v[234:235], v[214:215] op_sel:[0,1] op_sel_hi:[1,1]\n"
    "v_cvt_pk_bf16_f32 v224, v228, v229\n"
    "v_cvt_pk_bf16_f32 v225, v230, v231\n"
    "v_cvt_pk_bf16_f32 v226, v232, v233\n"
    "v_cvt_pk_bf16_f32 v227, v234, v235\n"
    "buffer_store_dwordx4 v[224:227], v242, s[52:55], s73 offen offset:1024 sc1\n"
    "v_accvgpr_read_b32 v228, a80\n"
    "v_accvgpr_read_b32 v229, a81\n"
    "v_accvgpr_read_b32 v230, a82\n"
    "v_accvgpr_read_b32 v231, a83\n"
    "v_accvgpr_read_b32 v232, a84\n"
    "v_accvgpr_read_b32 v233, a85\n"
    "v_accvgpr_read_b32 v234, a86\n"
    "v_accvgpr_read_b32 v235, a87\n"
    "v_accvgpr_write_b32 a80, 0\n"
    "v_accvgpr_write_b32 a81, 0\n"
    "v_accvgpr_write_b32 a82, 0\n"
    "v_accvgpr_write_b32 a83, 0\n"
    "v_accvgpr_write_b32 a84, 0\n"
    "v_accvgpr_write_b32 a85, 0\n"
    "v_accvgpr_write_b32 a86, 0\n"
    "v_accvgpr_write_b32 a87, 0\n"
    "v_pk_mul_f32 v[228:229], v[228:229], v[216:217] op_sel:[0,0] op_sel_hi:[1,0]\n"
    "v_pk_mul_f32 v[230:231], v[230:231], v[216:217] op_sel:[0,0] op_sel_hi:[1,0]\n"
    "v_pk_mul_f32 v[232:233], v[232:233], v[216:217] op_sel:[0,0] op_sel_hi:[1,0]\n"
    "v_pk_mul_f32 v[234:235], v[234:235], v[216:217] op_sel:[0,0] op_sel_hi:[1,0]\n"
    "v_cvt_pk_bf16_f32 v220, v228, v229\n"
    "v_cvt_pk_bf16_f32 v221, v230, v231\n"
    "v_cvt_pk_bf16_f32 v222, v232, v233\n"
    "v_cvt_pk_bf16_f32 v223, v234, v235\n"
    "buffer_store_dwordx4 v[220:223], v242, s[52:55], s73 offen offset:2048 sc1\n"
    "v_accvgpr_read_b32 v228, a112\n"
    "v_accvgpr_read_b32 v229, a113\n"
    "v_accvgpr_read_b32 v230, a114\n"
    "v_accvgpr_read_b32 v231, a115\n"
    "v_accvgpr_read_b32 v232, a116\n"
    "v_accvgpr_read_b32 v233, a117\n"
    "v_accvgpr_read_b32 v234, a118\n"
    "v_accvgpr_read_b32 v235, a119\n"
    "v_accvgpr_write_b32 a112, 0\n"
    "v_accvgpr_write_b32 a113, 0\n"
    "v_accvgpr_write_b32 a114, 0\n"
    "v_accvgpr_write_b32 a115, 0\n"
    "v_accvgpr_write_b32 a116, 0\n"
    "v_accvgpr_write_b32 a117, 0\n"
    "v_accvgpr_write_b32 a118, 0\n"
    "v_accvgpr_write_b32 a119, 0\n"
    "v_pk_mul_f32 v[228:229], v[228:229], v[216:217] op_sel:[0,1] op_sel_hi:[1,1]\n"
    "v_pk_mul_f32 v[230:231], v[230:231], v[216:217] op_sel:[0,1] op_sel_hi:[1,1]\n"
    "v_pk_mul_f32 v[232:233], v[232:233], v[216:217] op_sel:[0,1] op_sel_hi:[1,1]\n"
    "v_pk_mul_f32 v[234:235], v[234:235], v[216:217] op_sel:[0,1] op_sel_hi:[1,1]\n"
    "v_cvt_pk_bf16_f32 v224, v228, v229\n"
    "v_cvt_pk_bf16_f32 v225, v230, v231\n"
    "v_cvt_pk_bf16_f32 v226, v232, v233\n"
    "v_cvt_pk_bf16_f32 v227, v234, v235\n"
    "buffer_store_dwordx4 v[224:227], v242, s[52:55], s73 offen offset:3072 sc1\n"
    "s_add_u32 s73, s72, 16384\n"
    "v_accvgpr_read_b32 v228, a144\n"
    "v_accvgpr_read_b32 v229, a145\n"
    "v_accvgpr_read_b32 v230, a146\n"
    "v_accvgpr_read_b32 v231, a147\n"
    "v_accvgpr_read_b32 v232, a148\n"
    "v_accvgpr_read_b32 v233, a149\n"
    "v_accvgpr_read_b32 v234, a150\n"
    "v_accvgpr_read_b32 v235, a151\n"
    "v_accvgpr_write_b32 a144, 0\n"
    "v_accvgpr_write_b32 a145, 0\n"
    "v_accvgpr_write_b32 a146, 0\n"
    "v_accvgpr_write_b32 a147, 0\n"
    "v_accvgpr_write_b32 a148, 0\n"
    "v_accvgpr_write_b32 a149, 0\n"
    "v_accvgpr_write_b32 a150, 0\n"
    "v_accvgpr_write_b32 a151, 0\n"
    "v_pk_mul_f32 v[228:229], v[228:229], v[218:219] op_sel:[0,0] op_sel_hi:[1,0]\n"
    "v_pk_mul_f32 v[230:231], v[230:231], v[218:219] op_sel:[0,0] op_sel_hi:[1,0]\n"
    "v_pk_mul_f32 v[232:233], v[232:233], v[218:219] op_sel:[0,0] op_sel_hi:[1,0]\n"
    "v_pk_mul_f32 v[234:235], v[234:235], v[218:219] op_sel:[0,0] op_sel_hi:[1,0]\n"
    "v_cvt_pk_bf16_f32 v220, v228, v229\n"
    "v_cvt_pk_bf16_f32 v221, v230, v231\n"
    "v_cvt_pk_bf16_f32 v222, v232, v233\n"
    "v_cvt_pk_bf16_f32 v223, v234, v235\n"
    "buffer_store_dwordx4 v[220:223], v242, s[52:55], s73 offen sc1\n"
    "v_accvgpr_read_b32 v228, a176\n"
    "v_accvgpr_read_b32 v229, a177\n"
    "v_accvgpr_read_b32 v230, a178\n"
    "v_accvgpr_read_b32 v231, a179\n"
    "v_accvgpr_read_b32 v232, a180\n"
    "v_accvgpr_read_b32 v233, a181\n"
    "v_accvgpr_read_b32 v234, a182\n"
    "v_accvgpr_read_b32 v235, a183\n"
    "v_accvgpr_write_b32 a176, 0\n"
    "v_accvgpr_write_b32 a177, 0\n"
    "v_accvgpr_write_b32 a178, 0\n"
    "v_accvgpr_write_b32 a179, 0\n"
    "v_accvgpr_write_b32 a180, 0\n"
    "v_accvgpr_write_b32 a181, 0\n"
    "v_accvgpr_write_b32 a182, 0\n"
    "v_accvgpr_write_b32 a183, 0\n"
    "v_pk_mul_f32 v[228:229], v[228:229], v[218:219] op_sel:[0,1] op_sel_hi:[1,1]\n"
    "v_pk_mul_f32 v[230:231], v[230:231], v[218:219] op_sel:[0,1] op_sel_hi:[1,1]\n"
    "v_pk_mul_f32 v[232:233], v[232:233], v[218:219] op_sel:[0,1] op_sel_hi:[1,1]\n"
    "v_pk_mul_f32 v[234:235], v[234:235], v[218:219] op_sel:[0,1] op_sel_hi:[1,1]\n"
    "v_cvt_pk_bf16_f32 v224, v228, v229\n"
    "v_cvt_pk_bf16_f32 v225, v230, v231\n"
    "v_cvt_pk_bf16_f32 v226, v232, v233\n"
    "v_cvt_pk_bf16_f32 v227, v234, v235\n"
    "buffer_store_dwordx4 v[224:227], v242, s[52:55], s73 offen offset:1024 sc1\n"
    "s_add_u32 s73, s72, 18432\n"
    "v_accvgpr_read_b32 v228, a24\n"
    "v_accvgpr_read_b32 v229, a25\n"
    "v_accvgpr_read_b32 v230, a26\n"
    "v_accvgpr_read_b32 v231, a27\n"
    "v_accvgpr_read_b32 v232, a28\n"
    "v_accvgpr_read_b32 v233, a29\n"
    "v_accvgpr_read_b32 v234, a30\n"
    "v_accvgpr_read_b32 v235, a31\n"
    "v_accvgpr_write_b32 a24, 0\n"
    "v_accvgpr_write_b32 a25, 0\n"
    "v_accvgpr_write_b32 a26, 0\n"
    "v_accvgpr_write_b32 a27, 0\n"
    "v_accvgpr_write_b32 a28, 0\n"
    "v_accvgpr_write_b32 a29, 0\n"
    "v_accvgpr_write_b32 a30, 0\n"
    "v_accvgpr_write_b32 a31, 0\n"
    "v_pk_mul_f32 v[228:229], v[228:229], v[214:215] op_sel:[0,0] op_sel_hi:[1,0]\n"
    "v_pk_mul_f32 v[230:231], v[230:231], v[214:215] op_sel:[0,0] op_sel_hi:[1,0]\n"
    "v_pk_mul_f32 v[232:233], v[232:233], v[214:215] op_sel:[0,0] op_sel_hi:[1,0]\n"
    "v_pk_mul_f32 v[234:235], v[234:235], v[214:215] op_sel:[0,0] op_sel_hi:[1,0]\n"
    "v_cvt_pk_bf16_f32 v220, v228, v229\n"
    "v_cvt_pk_bf16_f32 v221, v230, v231\n"
    "v_cvt_pk_bf16_f32 v222, v232, v233\n"
    "v_cvt_pk_bf16_f32 v223, v234, v235\n"
    "buffer_store_dwordx4 v[220:223], v242, s[52:55], s73 offen sc1\n"
    "v_accvgpr_read_b32 v228, a56\n"
    "v_accvgpr_read_b32 v229, a57\n"
    "v_accvgpr_read_b32 v230, a58\n"
    "v_accvgpr_read_b32 v231, a59\n"
    "v_accvgpr_read_b32 v232, a60\n"
    "v_accvgpr_read_b32 v233, a61\n"
    "v_accvgpr_read_b32 v234, a62\n"
    "v_accvgpr_read_b32 v235, a63\n"
    "v_accvgpr_write_b32 a56, 0\n"
    "v_accvgpr_write_b32 a57, 0\n"
    "v_accvgpr_write_b32 a58, 0\n"
    "v_accvgpr_write_b32 a59, 0\n"
    "v_accvgpr_write_b32 a60, 0\n"
    "v_accvgpr_write_b32 a61, 0\n"
    "v_accvgpr_write_b32 a62, 0\n"
    "v_accvgpr_write_b32 a63, 0\n"
    "v_pk_mul_f32 v[228:229], v[228:229], v[214:215] op_sel:[0,1] op_sel_hi:[1,1]\n"
    "v_pk_mul_f32 v[230:231], v[230:231], v[214:215] op_sel:[0,1] op_sel_hi:[1,1]\n"
    "v_pk_mul_f32 v[232:233], v[232:233], v[214:215] op_sel:[0,1] op_sel_hi:[1,1]\n"
    "v_pk_mul_f32 v[234:235], v[234:235], v[214:215] op_sel:[0,1] op_sel_hi:[1,1]\n"
    "v_cvt_pk_bf16_f32 v224, v228, v229\n"
    "v_cvt_pk_bf16_f32 v225, v230, v231\n"
    "v_cvt_pk_bf16_f32 v226, v232, v233\n"
    "v_cvt_pk_bf16_f32 v227, v234, v235\n"
    "buffer_store_dwordx4 v[224:227], v242, s[52:55], s73 offen offset:1024 sc1\n"
    "v_accvgpr_read_b32 v228, a88\n"
    "v_accvgpr_read_b32 v229, a89\n"
    "v_accvgpr_read_b32 v230, a90\n"
    "v_accvgpr_read_b32 v231, a91\n"
    "v_accvgpr_read_b32 v232, a92\n"
    "v_accvgpr_read_b32 v233, a93\n"
    "v_accvgpr_read_b32 v234, a94\n"
    "v_accvgpr_read_b32 v235, a95\n"
    "v_accvgpr_write_b32 a88, 0\n"
    "v_accvgpr_write_b32 a89, 0\n"
    "v_accvgpr_write_b32 a90, 0\n"
    "v_accvgpr_write_b32 a91, 0\n"
    "v_accvgpr_write_b32 a92, 0\n"
    "v_accvgpr_write_b32 a93, 0\n"
    "v_accvgpr_write_b32 a94, 0\n"
    "v_accvgpr_write_b32 a95, 0\n"
    "v_pk_mul_f32 v[228:229], v[228:229], v[216:217] op_sel:[0,0] op_sel_hi:[1,0]\n"
    "v_pk_mul_f32 v[230:231], v[230:231], v[216:217] op_sel:[0,0] op_sel_hi:[1,0]\n"
    "v_pk_mul_f32 v[232:233], v[232:233], v[216:217] op_sel:[0,0] op_sel_hi:[1,0]\n"
    "v_pk_mul_f32 v[234:235], v[234:235], v[216:217] op_sel:[0,0] op_sel_hi:[1,0]\n"
    "v_cvt_pk_bf16_f32 v220, v228, v229\n"
    "v_cvt_pk_bf16_f32 v221, v230, v231\n"
    "v_cvt_pk_bf16_f32 v222, v232, v233\n"
    "v_cvt_pk_bf16_f32 v223, v234, v235\n"
    "buffer_store_dwordx4 v[220:223], v242, s[52:55], s73 offen offset:2048 sc1\n"
    "v_accvgpr_read_b32 v228, a120\n"
    "v_accvgpr_read_b32 v229, a121\n"
    "v_accvgpr_read_b32 v230, a122\n"
    "v_accvgpr_read_b32 v231, a123\n"
    "v_accvgpr_read_b32 v232, a124\n"
    "v_accvgpr_read_b32 v233, a125\n"
    "v_accvgpr_read_b32 v234, a126\n"
    "v_accvgpr_read_b32 v235, a127\n"
    "v_accvgpr_write_b32 a120, 0\n"
    "v_accvgpr_write_b32 a121, 0\n"
    "v_accvgpr_write_b32 a122, 0\n"
    "v_accvgpr_write_b32 a123, 0\n"
    "v_accvgpr_write_b32 a124, 0\n"
    "v_accvgpr_write_b32 a125, 0\n"
    "v_accvgpr_write_b32 a126, 0\n"
    "v_accvgpr_write_b32 a127, 0\n"
    "v_pk_mul_f32 v[228:229], v[228:229], v[216:217] op_sel:[0,1] op_sel_hi:[1,1]\n"
    "v_pk_mul_f32 v[230:231], v[230:231], v[216:217] op_sel:[0,1] op_sel_hi:[1,1]\n"
    "v_pk_mul_f32 v[232:233], v[232:233], v[216:217] op_sel:[0,1] op_sel_hi:[1,1]\n"
    "v_pk_mul_f32 v[234:235], v[234:235], v[216:217] op_sel:[0,1] op_sel_hi:[1,1]\n"
    "v_cvt_pk_bf16_f32 v224, v228, v229\n"
    "v_cvt_pk_bf16_f32 v225, v230, v231\n"
    "v_cvt_pk_bf16_f32 v226, v232, v233\n"
    "v_cvt_pk_bf16_f32 v227, v234, v235\n"
    "buffer_store_dwordx4 v[224:227], v242, s[52:55], s73 offen offset:3072 sc1\n"
    "s_add_u32 s73, s72, 22528\n"
    "v_accvgpr_read_b32 v228, a152\n"
    "v_accvgpr_read_b32 v229, a153\n"
    "v_accvgpr_read_b32 v230, a154\n"
    "v_accvgpr_read_b32 v231, a155\n"
    "v_accvgpr_read_b32 v232, a156\n"
    "v_accvgpr_read_b32 v233, a157\n"
    "v_accvgpr_read_b32 v234, a158\n"
    "v_accvgpr_read_b32 v235, a159\n"
    "v_accvgpr_write_b32 a152, 0\n"
    "v_accvgpr_write_b32 a153, 0\n"
    "v_accvgpr_write_b32 a154, 0\n"
    "v_accvgpr_write_b32 a155, 0\n"
    "v_accvgpr_write_b32 a156, 0\n"
    "v_accvgpr_write_b32 a157, 0\n"
    "v_accvgpr_write_b32 a158, 0\n"
    "v_accvgpr_write_b32 a159, 0\n"
    "v_pk_mul_f32 v[228:229], v[228:229], v[218:219] op_sel:[0,0] op_sel_hi:[1,0]\n"
    "v_pk_mul_f32 v[230:231], v[230:231], v[218:219] op_sel:[0,0] op_sel_hi:[1,0]\n"
    "v_pk_mul_f32 v[232:233], v[232:233], v[218:219] op_sel:[0,0] op_sel_hi:[1,0]\n"
    "v_pk_mul_f32 v[234:235], v[234:235], v[218:219] op_sel:[0,0] op_sel_hi:[1,0]\n"
    "v_cvt_pk_bf16_f32 v220, v228, v229\n"
    "v_cvt_pk_bf16_f32 v221, v230, v231\n"
    "v_cvt_pk_bf16_f32 v222, v232, v233\n"
    "v_cvt_pk_bf16_f32 v223, v234, v235\n"
    "buffer_store_dwordx4 v[220:223], v242, s[52:55], s73 offen sc1\n"
    "v_accvgpr_read_b32 v228, a184\n"
    "v_accvgpr_read_b32 v229, a185\n"
    "v_accvgpr_read_b32 v230, a186\n"
    "v_accvgpr_read_b32 v231, a187\n"
    "v_accvgpr_read_b32 v232, a188\n"
    "v_accvgpr_read_b32 v233, a189\n"
    "v_accvgpr_read_b32 v234, a190\n"
    "v_accvgpr_read_b32 v235, a191\n"
    "v_accvgpr_write_b32 a184, 0\n"
    "v_accvgpr_write_b32 a185, 0\n"
    "v_accvgpr_write_b32 a186, 0\n"
    "v_accvgpr_write_b32 a187, 0\n"
    "v_accvgpr_write_b32 a188, 0\n"
    "v_accvgpr_write_b32 a189, 0\n"
    "v_accvgpr_write_b32 a190, 0\n"
    "v_accvgpr_write_b32 a191, 0\n"
    "v_pk_mul_f32 v[228:229], v[228:229], v[218:219] op_sel:[0,1] op_sel_hi:[1,1]\n"
    "v_pk_mul_f32 v[230:231], v[230:231], v[218:219] op_sel:[0,1] op_sel_hi:[1,1]\n"
    "v_pk_mul_f32 v[232:233], v[232:233], v[218:219] op_sel:[0,1] op_sel_hi:[1,1]\n"
    "v_pk_mul_f32 v[234:235], v[234:235], v[218:219] op_sel:[0,1] op_sel_hi:[1,1]\n"
    "v_cvt_pk_bf16_f32 v224, v228, v229\n"
    "v_cvt_pk_bf16_f32 v225, v230, v231\n"
    "v_cvt_pk_bf16_f32 v226, v232, v233\n"
    "v_cvt_pk_bf16_f32 v227, v234, v235\n"
    "buffer_store_dwordx4 v[224:227], v242, s[52:55], s73 offen offset:1024 sc1\n"
    "v_accvgpr_write_b32 a192, 0\n"
    "v_accvgpr_write_b32 a193, 0\n"
    "v_accvgpr_write_b32 a194, 0\n"
    "v_accvgpr_write_b32 a195, 0\n"
    "v_accvgpr_write_b32 a196, 0\n"
    "v_accvgpr_write_b32 a197, 0\n"
    "v_accvgpr_write_b32 a198, 0\n"
    "v_accvgpr_write_b32 a199, 0\n"
    "v_accvgpr_write_b32 a200, 0\n"
    "v_accvgpr_write_b32 a201, 0\n"
    "v_accvgpr_write_b32 a202, 0\n"
    "v_accvgpr_write_b32 a203, 0\n"
    "v_accvgpr_write_b32 a204, 0\n"
    "v_accvgpr_write_b32 a205, 0\n"
    "v_accvgpr_write_b32 a206, 0\n"
    "v_accvgpr_write_b32 a207, 0\n"
    "v_accvgpr_write_b32 a208, 0\n"
    "v_accvgpr_write_b32 a209, 0\n"
    "v_accvgpr_write_b32 a210, 0\n"
    "v_accvgpr_write_b32 a211, 0\n"
    "v_accvgpr_write_b32 a212, 0\n"
    "v_accvgpr_write_b32 a213, 0\n"
    "v_accvgpr_write_b32 a214, 0\n"
    "v_accvgpr_write_b32 a215, 0\n"
    "L_op%=:\n"
    "buffer_load_dwordx4 v[204:207], v245, s[40:43], s56 offen sc1\n"
    "buffer_load_dwordx4 v[208:211], v245, s[40:43], s57 offen sc1\n"
    "buffer_load_dwordx4 v[212:215], v245, s[40:43], s58 offen sc1\n"
    "buffer_load_dwordx4 v[216:219], v245, s[40:43], s59 offen sc1\n"
    "buffer_load_dwordx4 v[220:223], v245, s[40:43], s60 offen sc1\n"
    "buffer_load_dwordx4 v[224:227], v245, s[40:43], s61 offen sc1\n"
    "buffer_load_dwordx4 v[228:231], v245, s[40:43], s62 offen sc1\n"
    "buffer_load_dwordx4 v[232:235], v245, s[40:43], s63 offen sc1\n"
    "buffer_load_dwordx4 v[236:239], v245, s[40:43], s64 offen sc1\n"
    "v_add_u32 v245, 0x2400, v245\n"
    "s_mul_i32 s70, %[wv], 27648\n"
    "v_add_u32 v250, s70, v242\n"
    "s_xor_b32 s71, %[wv], 2\n"
    "s_mul_i32 s71, s71, 27648\n"
    "v_add_u32 v251, s71, v242\n"
    "s_and_b32 s71, %[wv], 1\n"
    "s_lshl_b32 s71, s71, 7\n"
    "v_mul_u32_u24 v243, 26, v255\n"
    "v_lshrrev_b32 v243, 8, v243\n"
    "v_mul_u32_u24 v253, 2144, v243\n"
    "v_add3_u32 v253, v253, v242, s71\n"
    "v_cmp_gt_u32 vcc, 60, v255\n"
    "v_bfrev_b32 v243, 2\n"
    "s_nop 0\n"
    "v_cndmask_b32 v253, v243, v253, vcc\n"
    "s_waitcnt vmcnt(63)\n"
    "L_oloop%=:\n"
    "v_mfma_f32_16x16x32_bf16 a[0:3], v[24:27], v[0:3], a[0:3]\n"
    "v_mfma_f32_16x16x32_bf16 a[36:39], v[24:27], v[4:7], a[36:39]\n"
    "v_mfma_f32_16x16x32_bf16 a[72:75], v[24:27], v[8:11], a[72:75]\n"
    "buffer_load_dwordx4 v[180:183], v244, s[36:39], s65 offen sc1\n"
    "v_mfma_f32_16x16x32_bf16 a[108:111], v[24:27], v[12:15], a[108:111]\n"
    "v_mfma_f32_16x16x32_bf16 a[144:147], v[24:27], v[16:19], a[144:147]\n"
    "v_mfma_f32_16x16x32_bf16 a[180:183], v[24:27], v[20:23], a[180:183]\n"
    "buffer_load_dwordx4 v[24:27], v245, s[40:43], s56 offen sc1\n"
    "v_mfma_f32_16x16x32_bf16 a[4:7], v[28:31], v[0:3], a[4:7]\n"
    "v_mfma_f32_16x16x32_bf16 a[40:43], v[28:31], v[4:7], a[40:43]\n"
    "v_mfma_f32_16x16x32_bf16 a[76:79], v[28:31], v[8:11], a[76:79]\n"
    "buffer_load_dwordx4 v[184:187], v244, s[36:39], s65 offen offset:1024 sc1\n"
    "v_mfma_f32_16x16x32_bf16 a[112:115], v[28:31], v[12:15], a[112:115]\n"
    "v_mfma_f32_16x16x32_bf16 a[148:151], v[28:31], v[16:19], a[148:151]\n"
    "v_mfma_f32_16x16x32_bf16 a[184:187], v[28:31], v[20:23], a[184:187]\n"
    "buffer_load_dwordx4 v[28:31], v245, s[40:43], s57 offen sc1\n"
    "v_mfma_f32_16x16x32_bf16 a[8:11], v[32:35], v[0:3], a[8:11]\n"
    "v_mfma_f32_16x16x32_bf16 a[44:47], v[32:35], v[4:7], a[44:47]\n"
    "v_mfma_f32_16x16x32_bf16 a[80:83], v[32:35], v[8:11], a[80:83]\n"
    "buffer_load_dwordx4 v[188:191], v244, s[36:39], s65 offen offset:2048 sc1\n"
    "v_mfma_f32_16x16x32_bf16 a[116:119], v[32:35], v[12:15], a[116:119]\n"
    "v_mfma_f32_16x16x32_bf16 a[152:155], v[32:35], v[16:19], a[152:155]\n"
    "v_mfma_f32_16x16x32_bf16 a[188:191], v[32:35], v[20:23], a[188:191]\n"
    "buffer_load_dwordx4 v[32:35], v245, s[40:43], s58 offen sc1\n"
    "v_mfma_f32_16x16x32_bf16 a[12:15], v[36:39], v[0:3], a[12:15]\n"
    "v_mfma_f32_16x16x32_bf16 a[48:51], v[36:39], v[4:7], a[48:51]\n"
    "v_mfma_f32_16x16x32_bf16 a[84:87], v[36:39], v[8:11], a[84:87]\n"
    "buffer_load_dwordx4 v[192:195], v244, s[36:39], s66 offen sc1\n"
    "v_mfma_f32_16x16x32_bf16 a[120:123], v[36:39], v[12:15], a[120:123]\n"
    "v_mfma_f32_16x16x32_bf16 a[156:159], v[36:39], v[16:19], a[156:159]\n"
    "v_mfma_f32_16x16x32_bf16 a[192:195], v[36:39], v[20:23], a[192:195]\n"
    "buffer_load_dwordx4 v[36:39], v245, s[40:43], s59 offen sc1\n"
    "v_mfma_f32_16x16x32_bf16 a[16:19], v[40:43], v[0:3], a[16:19]\n"
    "v_mfma_f32_16x16x32_bf16 a[52:55], v[40:43], v[4:7], a[52:55]\n"
    "v_mfma_f32_16x16x32_bf16 a[88:91], v[40:43], v[8:11], a[88:91]\n"
    "buffer_load_dwordx4 v[196:199], v244, s[36:39], s66 offen offset:1024 sc1\n"
    "v_mfma_f32_16x16x32_bf16 a[124:127], v[40:43], v[12:15], a[124:127]\n"
    "v_mfma_f32_16x16x32_bf16 a[160:163], v[40:43], v[16:19], a[160:163]\n"
    "v_mfma_f32_16x16x32_bf16 a[196:199], v[40:43], v[20:23], a[196:199]\n"
    "buffer_load_dwordx4 v[40:43], v245, s[40:43], s60 offen sc1\n"
    "v_mfma_f32_16x16x32_bf16 a[20:23], v[44:47], v[0:3], a[20:23]\n"
    "v_mfma_f32_16x16x32_bf16 a[56:59], v[44:47], v[4:7], a[56:59]\n"
    "v_mfma_f32_16x16x32_bf16 a[92:95], v[44:47], v[8:11], a[92:95]\n"
    "buffer_load_dwordx4 v[200:203], v244, s[36:39], s66 offen offset:2048 sc1\n"
    "v_mfma_f32_16x16x32_bf16 a[128:131], v[44:47], v[12:15], a[128:131]\n"
    "v_mfma_f32_16x16x32_bf16 a[164:167], v[44:47], v[16:19], a[164:167]\n"
    "v_add_u32 v244, 0x1800, v244\n"
    "v_mfma_f32_16x16x32_bf16 a[200:203], v[44:47], v[20:23], a[200:203]\n"
    "buffer_load_dwordx4 v[44:47], v245, s[40:43], s61 offen sc1\n"
    "v_mfma_f32_16x16x32_bf16 a[24:27], v[48:51], v[0:3], a[24:27]\n"
    "v_mfma_f32_16x16x32_bf16 a[60:63], v[48:51], v[4:7], a[60:63]\n"
    "v_mfma_f32_16x16x32_bf16 a[96:99], v[48:51], v[8:11], a[96:99]\n"
    "v_mfma_f32_16x16x32_bf16 a[132:135], v[48:51], v[12:15], a[132:135]\n"
    "v_mfma_f32_16x16x32_bf16 a[168:171], v[48:51], v[16:19], a[168:171]\n"
    "v_mfma_f32_16x16x32_bf16 a[204:207], v[48:51], v[20:23], a[204:207]\n"
    "buffer_load_dwordx4 v[48:51], v245, s[40:43], s62 offen sc1\n"
    "v_mfma_f32_16x16x32_bf16 a[28:31], v[52:55], v[0:3], a[28:31]\n"
    "v_mfma_f32_16x16x32_bf16 a[64:67], v[52:55], v[4:7], a[64:67]\n"
    "v_mfma_f32_16x16x32_bf16 a[100:103], v[52:55], v[8:11], a[100:103]\n"
    "v_mfma_f32_16x16x32_bf16 a[136:139], v[52:55], v[12:15], a[136:139]\n"
    "v_mfma_f32_16x16x32_bf16 a[172:175], v[52:55], v[16:19], a[172:175]\n"
    "v_mfma_f32_16x16x32_bf16 a[208:211], v[52:55], v[20:23], a[208:211]\n"
    "buffer_load_dwordx4 v[52:55], v245, s[40:43], s63 offen sc1\n"
    "v_mfma_f32_16x16x32_bf16 a[32:35], v[56:59], v[0:3], a[32:35]\n"
    "v_mfma_f32_16x16x32_bf16 a[68:71], v[56:59], v[4:7], a[68:71]\n"
    "v_mfma_f32_16x16x32_bf16 a[104:107], v[56:59], v[8:11], a[104:107]\n"
    "v_mfma_f32_16x16x32_bf16 a[140:143], v[56:59], v[12:15], a[140:143]\n"
    "v_mfma_f32_16x16x32_bf16 a[176:179], v[56:59], v[16:19], a[176:179]\n"
    "s_waitcnt vmcnt(33)\n"
    "v_mfma_f32_16x16x32_bf16 a[212:215], v[56:59], v[20:23], a[212:215]\n"
    "buffer_load_dwordx4 v[56:59], v245, s[40:43], s64 offen sc1\n"
    "v_add_u32 v245, 0x2400, v245\n"
    "v_mfma_f32_16x16x32_bf16 a[0:3], v[84:87], v[60:63], a[0:3]\n"
    "global_load_dwordx2 v[246:247], v241, %[pcnt] offset:256 sc1\n"
    "v_mfma_f32_16x16x32_bf16 a[36:39], v[84:87], v[64:67], a[36:39]\n"
    "v_mfma_f32_16x16x32_bf16 a[72:75], v[84:87], v[68:71], a[72:75]\n"
    "buffer_load_dwordx4 v[0:3], v244, s[36:39], s65 offen sc1\n"
    "v_mfma_f32_16x16x32_bf16 a[108:111], v[84:87], v[72:75], a[108:111]\n"
    "v_mfma_f32_16x16x32_bf16 a[144:147], v[84:87], v[76:79], a[144:147]\n"
    "v_mfma_f32_16x16x32_bf16 a[180:183], v[84:87], v[80:83], a[180:183]\n"
    "buffer_load_dwordx4 v[84:87], v245, s[40:43], s56 offen sc1\n"
    "v_mfma_f32_16x16x32_bf16 a[4:7], v[88:91], v[60:63], a[4:7]\n"
    "v_mfma_f32_16x16x32_bf16 a[40:43], v[88:91], v[64:67], a[40:43]\n"
    "v_mfma_f32_16x16x32_bf16 a[76:79], v[88:91], v[68:71], a[76:79]\n"
    "buffer_load_dwordx4 v[4:7], v244, s[36:39], s65 offen offset:1024 sc1\n"
    "v_mfma_f32_16x16x32_bf16 a[112:115], v[88:91], v[72:75], a[112:115]\n"
    "v_mfma_f32_16x16x32_bf16 a[148:151], v[88:91], v[76:79], a[148:151]\n"
    "v_mfma_f32_16x16x32_bf16 a[184:187], v[88:91], v[80:83], a[184:187]\n"
    "buffer_load_dwordx4 v[88:91], v245, s[40:43], s57 offen sc1\n"
    "v_mfma_f32_16x16x32_bf16 a[8:11], v[92:95], v[60:63], a[8:11]\n"
    "v_mfma_f32_16x16x32_bf16 a[44:47], v[92:95], v[64:67], a[44:47]\n"
    "v_mfma_f32_16x16x32_bf16 a[80:83], v[92:95], v[68:71], a[80:83]\n"
    "buffer_load_dwordx4 v[8:11], v244, s[36:39], s65 offen offset:2048 sc1\n"
    "v_mfma_f32_16x16x32_bf16 a[116:119], v[92:95], v[72:75], a[116:119]\n"
    "v_mfma_f32_16x16x32_bf16 a[152:155], v[92:95], v[76:79], a[152:155]\n"
    "v_mfma_f32_16x16x32_bf16 a[188:191], v[92:95], v[80:83], a[188:191]\n"
    "buffer_load_dwordx4 v[92:95], v245, s[40:43], s58 offen sc1\n"
    "v_mfma_f32_16x16x32_bf16 a[12:15], v[96:99], v[60:63], a[12:15]\n"
    "v_mfma_f32_16x16x32_bf16 a[48:51], v[96:99], v[64:67], a[48:51]\n"
    "v_mfma_f32_16x16x32_bf16 a[84:87], v[96:99], v[68:71], a[84:87]\n"
    "buffer_load_dwordx4 v[12:15], v244, s[36:39], s66 offen sc1\n"
    "v_mfma_f32_16x16x32_bf16 a[120:123], v[96:99], v[72:75], a[120:123]\n"
    "v_mfma_f32_16x16x32_bf16 a[156:159], v[96:99], v[76:79], a[156:159]\n"
    "v_mfma_f32_16x16x32_bf16 a[192:195], v[96:99], v[80:83], a[192:195]\n"
    "buffer_load_dwordx4 v[96:99], v245, s[40:43], s59 offen sc1\n"
    "v_mfma_f32_16x16x32_bf16 a[16:19], v[100:103], v[60:63], a[16:19]\n"
    "v_mfma_f32_16x16x32_bf16 a[52:55], v[100:103], v[64:67], a[52:55]\n"
    "v_mfma_f32_16x16x32_bf16 a[88:91], v[100:103], v[68:71], a[88:91]\n"
    "buffer_load_dwordx4 v[16:19], v244, s[36:39], s66 offen offset:1024 sc1\n"
    "v_mfma_f32_16x16x32_bf16 a[124:127], v[100:103], v[72:75], a[124:127]\n"
    "v_mfma_f32_16x16x32_bf16 a[160:163], v[100:103], v[76:79], a[160:163]\n"
    "v_mfma_f32_16x16x32_bf16 a[196:199], v[100:103], v[80:83], a[196:199]\n"
    "buffer_load_dwordx4 v[100:103], v245, s[40:43], s60 offen sc1\n"
    "v_mfma_f32_16x16x32_bf16 a[20:23], v[104:107], v[60:63], a[20:23]\n"
    "v_mfma_f32_16x16x32_bf16 a[56:59], v[104:107], v[64:67], a[56:59]\n"
    "v_mfma_f32_16x16x32_bf16 a[92:95], v[104:107], v[68:71], a[92:95]\n"
    "buffer_load_dwordx4 v[20:23], v244, s[36:39], s66 offen offset:2048 sc1\n"
    "v_mfma_f32_16x16x32_bf16 a[128:131], v[104:107], v[72:75], a[128:131]\n"
    "v_mfma_f32_16x16x32_bf16 a[164:167], v[104:107], v[76:79], a[164:167]\n"
    "v_add_u32 v244, 0x1800, v244\n"
    "v_mfma_f32_16x16x32_bf16 a[200:203], v[104:107], v[80:83], a[200:203]\n"
    "buffer_load_dwordx4 v[104:107], v245, s[40:43], s61 offen sc1\n"
    "v_mfma_f32_16x16x32_bf16 a[24:27], v[108:111], v[60:63], a[24:27]\n"
    "v_mfma_f32_16x16x32_bf16 a[60:63], v[108:111], v[64:67], a[60:63]\n"
    "v_mfma_f32_16x16x32_bf16 a[96:99], v[108:111], v[68:71], a[96:99]\n"
    "v_mfma_f32_16x16x32_bf16 a[132:135], v[108:111], v[72:75], a[132:135]\n"
    "v_mfma_f32_16x16x32_bf16 a[168:171], v[108:111], v[76:79], a[168:171]\n"
    "v_mfma_f32_16x16x32_bf16 a[204:207], v[108:111], v[80:83], a[204:207]\n"
    "buffer_load_dwordx4 v[108:111], v245, s[40:43], s62 offen sc1\n"
    "v_mfma_f32_16x16x32_bf16 a[28:31], v[112:115], v[60:63], a[28:31]\n"
    "v_mfma_f32_16x16x32_bf16 a[64:67], v[112:115], v[64:67], a[64:67]\n"
    "v_mfma_f32_16x16x32_bf16 a[100:103], v[112:115], v[68:71], a[100:103]\n"
    "v_mfma_f32_16x16x32_bf16 a[136:139], v[112:115], v[72:75], a[136:139]\n"
    "v_mfma_f32_16x16x32_bf16 a[172:175], v[112:115], v[76:79], a[172:175]\n"
    "s_sub_u32 s69, s69, 1\n"
    "v_mfma_f32_16x16x32_bf16 a[208:211], v[112:115], v[80:83], a[208:211]\n"
    "buffer_load_dwordx4 v[112:115], v245, s[40:43], s63 offen sc1\n"
    "v_mfma_f32_16x16x32_bf16 a[32:35], v[116:119], v[60:63], a[32:35]\n"
    "v_mfma_f32_16x16x32_bf16 a[68:71], v[116:119], v[64:67], a[68:71]\n"
    "v_mfma_f32_16x16x32_bf16 a[104:107], v[116:119], v[68:71], a[104:107]\n"
    "v_mfma_f32_16x16x32_bf16 a[140:143], v[116:119], v[72:75], a[140:143]\n"
    "v_mfma_f32_16x16x32_bf16 a[176:179], v[116:119], v[76:79], a[176:179]\n"
    "s_waitcnt vmcnt(34)\n"
    "v_mfma_f32_16x16x32_bf16 a[212:215], v[116:119], v[80:83], a[212:215]\n"
    "buffer_load_dwordx4 v[116:119], v245, s[40:43], s64 offen sc1\n"
    "v_add_u32 v245, 0x2400, v245\n"
    "v_mfma_f32_16x16x32_bf16 a[0:3], v[144:147], v[120:123], a[0:3]\n"
    "v_mfma_f32_16x16x32_bf16 a[36:39], v[144:147], v[124:127], a[36:39]\n"
    "v_mfma_f32_16x16x32_bf16 a[72:75], v[144:147], v[128:131], a[72:75]\n"
    "buffer_load_dwordx4 v[60:63], v244, s[36:39], s65 offen sc1\n"
    "v_mfma_f32_16x16x32_bf16 a[108:111], v[144:147], v[132:135], a[108:111]\n"
    "v_mfma_f32_16x16x32_bf16 a[144:147], v[144:147], v[136:139], a[144:147]\n"
    "v_mfma_f32_16x16x32_bf16 a[180:183], v[144:147], v[140:143], a[180:183]\n"
    "buffer_load_dwordx4 v[144:147], v245, s[40:43], s56 offen sc1\n"
    "v_mfma_f32_16x16x32_bf16 a[4:7], v[148:151], v[120:123], a[4:7]\n"
    "v_mfma_f32_16x16x32_bf16 a[40:43], v[148:151], v[124:127], a[40:43]\n"
    "v_mfma_f32_16x16x32_bf16 a[76:79], v[148:151], v[128:131], a[76:79]\n"
    "buffer_load_dwordx4 v[64:67], v244, s[36:39], s65 offen offset:1024 sc1\n"
    "v_mfma_f32_16x16x32_bf16 a[112:115], v[148:151], v[132:135], a[112:115]\n"
    "v_mfma_f32_16x16x32_bf16 a[148:151], v[148:151], v[136:139], a[148:151]\n"
    "v_mfma_f32_16x16x32_bf16 a[184:187], v[148:151], v[140:143], a[184:187]\n"
    "buffer_load_dwordx4 v[148:151], v245, s[40:43], s57 offen sc1\n"
    "v_mfma_f32_16x16x32_bf16 a[8:11], v[152:155], v[120:123], a[8:11]\n"
    "v_mfma_f32_16x16x32_bf16 a[44:47], v[152:155], v[124:127], a[44:47]\n"
    "v_mfma_f32_16x16x32_bf16 a[80:83], v[152:155], v[128:131], a[80:83]\n"
    "buffer_load_dwordx4 v[68:71], v244, s[36:39], s65 offen offset:2048 sc1\n"
    "v_mfma_f32_16x16x32_bf16 a[116:119], v[152:155], v[132:135], a[116:119]\n"
    "v_mfma_f32_16x16x32_bf16 a[152:155], v[152:155], v[136:139], a[152:155]\n"
    "v_mfma_f32_16x16x32_bf16 a[188:191], v[152:155], v[140:143], a[188:191]\n"
    "buffer_load_dwordx4 v[152:155], v245, s[40:43], s58 offen sc1\n"
    "v_mfma_f32_16x16x32_bf16 a[12:15], v[156:159], v[120:123], a[12:15]\n"
    "v_mfma_f32_16x16x32_bf16 a[48:51], v[156:159], v[124:127], a[48:51]\n"
    "v_mfma_f32_16x16x32_bf16 a[84:87], v[156:159], v[128:131], a[84:87]\n"
    "buffer_load_dwordx4 v[72:75], v244, s[36:39], s66 offen sc1\n"
    "v_mfma_f32_16x16x32_bf16 a[120:123], v[156:159], v[132:135], a[120:123]\n"
    "v_mfma_f32_16x16x32_bf16 a[156:159], v[156:159], v[136:139], a[156:159]\n"
    "v_mfma_f32_16x16x32_bf16 a[192:195], v[156:159], v[140:143], a[192:195]\n"
    "buffer_load_dwordx4 v[156:159], v245, s[40:43], s59 offen sc1\n"
    "v_mfma_f32_16x16x32_bf16 a[16:19], v[160:163], v[120:123], a[16:19]\n"
    "v_mfma_f32_16x16x32_bf16 a[52:55], v[160:163], v[124:127], a[52:55]\n"
    "v_mfma_f32_16x16x32_bf16 a[88:91], v[160:163], v[128:131], a[88:91]\n"
    "buffer_load_dwordx4 v[76:79], v244, s[36:39], s66 offen offset:1024 sc1\n"
    "v_mfma_f32_16x16x32_bf16 a[124:127], v[160:163], v[132:135], a[124:127]\n"
    "v_mfma_f32_16x16x32_bf16 a[160:163], v[160:163], v[136:139], a[160:163]\n"
    "v_mfma_f32_16x16x32_bf16 a[196:199], v[160:163], v[140:143], a[196:199]\n"
    "buffer_load_dwordx4 v[160:163], v245, s[40:43], s60 offen sc1\n"
    "v_mfma_f32_16x16x32_bf16 a[20:23], v[164:167], v[120:123], a[20:23]\n"
    "v_mfma_f32_16x16x32_bf16 a[56:59], v[164:167], v[124:127], a[56:59]\n"
    "v_mfma_f32_16x16x32_bf16 a[92:95], v[164:167], v[128:131], a[92:95]\n"
    "buffer_load_dwordx4 v[80:83], v244, s[36:39], s66 offen offset:2048 sc1\n"
    "v_mfma_f32_16x16x32_bf16 a[128:131], v[164:167], v[132:135], a[128:131]\n"
    "v_mfma_f32_16x16x32_bf16 a[164:167], v[164:167], v[136:139], a[164:167]\n"
    "v_add_u32 v244, 0x1800, v244\n"
    "v_mfma_f32_16x16x32_bf16 a[200:203], v[164:167], v[140:143], a[200:203]\n"
    "buffer_load_dwordx4 v[164:167], v245, s[40:43], s61 offen sc1\n"
    "v_mfma_f32_16x16x32_bf16 a[24:27], v[168:171], v[120:123], a[24:27]\n"
    "v_mfma_f32_16x16x32_bf16 a[60:63], v[168:171], v[124:127], a[60:63]\n"
    "v_mfma_f32_16x16x32_bf16 a[96:99], v[168:171], v[128:131], a[96:99]\n"
    "v_mfma_f32_16x16x32_bf16 a[132:135], v[168:171], v[132:135], a[132:135]\n"
    "v_mfma_f32_16x16x32_bf16 a[168:171], v[168:171], v[136:139], a[168:171]\n"
    "v_mfma_f32_16x16x32_bf16 a[204:207], v[168:171], v[140:143], a[204:207]\n"
    "buffer_load_dwordx4 v[168:171], v245, s[40:43], s62 offen sc1\n"
    "v_mfma_f32_16x16x32_bf16 a[28:31], v[172:175], v[120:123], a[28:31]\n"
    "v_mfma_f32_16x16x32_bf16 a[64:67], v[172:175], v[124:127], a[64:67]\n"
    "v_mfma_f32_16x16x32_bf16 a[100:103], v[172:175], v[128:131], a[100:103]\n"
    "v_mfma_f32_16x16x32_bf16 a[136:139], v[172:175], v[132:135], a[136:139]\n"
    "v_mfma_f32_16x16x32_bf16 a[172:175], v[172:175], v[136:139], a[172:175]\n"
    "v_mfma_f32_16x16x32_bf16 a[208:211], v[172:175], v[140:143], a[208:211]\n"
    "buffer_load_dwordx4 v[172:175], v245, s[40:43], s63 offen sc1\n"
    "v_mfma_f32_16x16x32_bf16 a[32:35], v[176:179], v[120:123], a[32:35]\n"
    "v_mfma_f32_16x16x32_bf16 a[68:71], v[176:179], v[124:127], a[68:71]\n"
    "v_mfma_f32_16x16x32_bf16 a[104:107], v[176:179], v[128:131], a[104:107]\n"
    "v_mfma_f32_16x16x32_bf16 a[140:143], v[176:179], v[132:135], a[140:143]\n"
    "v_mfma_f32_16x16x32_bf16 a[176:179], v[176:179], v[136:139], a[176:179]\n"
    "s_waitcnt vmcnt(34)\n"
    "v_mfma_f32_16x16x32_bf16 a[212:215], v[176:179], v[140:143], a[212:215]\n"
    "buffer_load_dwordx4 v[176:179], v245, s[40:43], s64 offen sc1\n"
    "v_add_u32 v245, 0x2400, v245\n"
    "v_mfma_f32_16x16x32_bf16 a[0:3], v[204:207], v[180:183], a[0:3]\n"
    "v_mfma_f32_16x16x32_bf16 a[36:39], v[204:207], v[184:187], a[36:39]\n"
    "v_mfma_f32_16x16x32_bf16 a[72:75], v[204:207], v[188:191], a[72:75]\n"
    "buffer_load_dwordx4 v[120:123], v244, s[36:39], s65 offen sc1\n"
    "v_mfma_f32_16x16x32_bf16 a[108:111], v[204:207], v[192:195], a[108:111]\n"
    "v_mfma_f32_16x16x32_bf16 a[144:147], v[204:207], v[196:199], a[144:147]\n"
    "v_mfma_f32_16x16x32_bf16 a[180:183], v[204:207], v[200:203], a[180:183]\n"
    "buffer_load_dwordx4 v[204:207], v245, s[40:43], s56 offen sc1\n"
    "v_mfma_f32_16x16x32_bf16 a[4:7], v[208:211], v[180:183], a[4:7]\n"
    "v_mfma_f32_16x16x32_bf16 a[40:43], v[208:211], v[184:187], a[40:43]\n"
    "v_mfma_f32_16x16x32_bf16 a[76:79], v[208:211], v[188:191], a[76:79]\n"
    "buffer_load_dwordx4 v[124:127], v244, s[36:39], s65 offen offset:1024 sc1\n"
    "v_mfma_f32_16x16x32_bf16 a[112:115], v[208:211], v[192:195], a[112:115]\n"
    "v_mfma_f32_16x16x32_bf16 a[148:151], v[208:211], v[196:199], a[148:151]\n"
    "v_mfma_f32_16x16x32_bf16 a[184:187], v[208:211], v[200:203], a[184:187]\n"
    "buffer_load_dwordx4 v[208:211], v245, s[40:43], s57 offen sc1\n"
    "v_mfma_f32_16x16x32_bf16 a[8:11], v[212:215], v[180:183], a[8:11]\n"
    "v_mfma_f32_16x16x32_bf16 a[44:47], v[212:215], v[184:187], a[44:47]\n"
    "v_mfma_f32_16x16x32_bf16 a[80:83], v[212:215], v[188:191], a[80:83]\n"
    "buffer_load_dwordx4 v[128:131], v244, s[36:39], s65 offen offset:2048 sc1\n"
    "v_mfma_f32_16x16x32_bf16 a[116:119], v[212:215], v[192:195], a[116:119]\n"
    "v_mfma_f32_16x16x32_bf16 a[152:155], v[212:215], v[196:199], a[152:155]\n"
    "v_mfma_f32_16x16x32_bf16 a[188:191], v[212:215], v[200:203], a[188:191]\n"
    "buffer_load_dwordx4 v[212:215], v245, s[40:43], s58 offen sc1\n"
    "v_mfma_f32_16x16x32_bf16 a[12:15], v[216:219], v[180:183], a[12:15]\n"
    "v_mfma_f32_16x16x32_bf16 a[48:51], v[216:219], v[184:187], a[48:51]\n"
    "v_mfma_f32_16x16x32_bf16 a[84:87], v[216:219], v[188:191], a[84:87]\n"
    "buffer_load_dwordx4 v[132:135], v244, s[36:39], s66 offen sc1\n"
    "v_mfma_f32_16x16x32_bf16 a[120:123], v[216:219], v[192:195], a[120:123]\n"
    "v_mfma_f32_16x16x32_bf16 a[156:159], v[216:219], v[196:199], a[156:159]\n"
    "v_mfma_f32_16x16x32_bf16 a[192:195], v[216:219], v[200:203], a[192:195]\n"
    "buffer_load_dwordx4 v[216:219], v245, s[40:43], s59 offen sc1\n"
    "v_mfma_f32_16x16x32_bf16 a[16:19], v[220:223], v[180:183], a[16:19]\n"
    "v_mfma_f32_16x16x32_bf16 a[52:55], v[220:223], v[184:187], a[52:55]\n"
    "v_mfma_f32_16x16x32_bf16 a[88:91], v[220:223], v[188:191], a[88:91]\n"
    "buffer_load_dwordx4 v[136:139], v244, s[36:39], s66 offen offset:1024 sc1\n"
    "v_mfma_f32_16x16x32_bf16 a[124:127], v[220:223], v[192:195], a[124:127]\n"
    "v_mfma_f32_16x16x32_bf16 a[160:163], v[220:223], v[196:199], a[160:163]\n"
    "v_mfma_f32_16x16x32_bf16 a[196:199], v[220:223], v[200:203], a[196:199]\n"
    "buffer_load_dwordx4 v[220:223], v245, s[40:43], s60 offen sc1\n"
    "v_mfma_f32_16x16x32_bf16 a[20:23], v[224:227], v[180:183], a[20:23]\n"
    "v_mfma_f32_16x16x32_bf16 a[56:59], v[224:227], v[184:187], a[56:59]\n"
    "v_mfma_f32_16x16x32_bf16 a[92:95], v[224:227], v[188:191], a[92:95]\n"
    "buffer_load_dwordx4 v[140:143], v244, s[36:39], s66 offen offset:2048 sc1\n"
    "v_mfma_f32_16x16x32_bf16 a[128:131], v[224:227], v[192:195], a[128:131]\n"
    "v_mfma_f32_16x16x32_bf16 a[164:167], v[224:227], v[196:199], a[164:167]\n"
    "v_add_u32 v244, 0x1800, v244\n"
    "v_mfma_f32_16x16x32_bf16 a[200:203], v[224:227], v[200:203], a[200:203]\n"
    "buffer_load_dwordx4 v[224:227], v245, s[40:43], s61 offen sc1\n"
    "v_mfma_f32_16x16x32_bf16 a[24:27], v[228:231], v[180:183], a[24:27]\n"
    "v_mfma_f32_16x16x32_bf16 a[60:63], v[228:231], v[184:187], a[60:63]\n"
    "v_mfma_f32_16x16x32_bf16 a[96:99], v[228:231], v[188:191], a[96:99]\n"
    "v_mfma_f32_16x16x32_bf16 a[132:135], v[228:231], v[192:195], a[132:135]\n"
    "v_mfma_f32_16x16x32_bf16 a[168:171], v[228:231], v[196:199], a[168:171]\n"
    "v_mfma_f32_16x16x32_bf16 a[204:207], v[228:231], v[200:203], a[204:207]\n"
    "buffer_load_dwordx4 v[228:231], v245, s[40:43], s62 offen sc1\n"
    "v_mfma_f32_16x16x32_bf16 a[28:31], v[232:235], v[180:183], a[28:31]\n"
    "v_mfma_f32_16x16x32_bf16 a[64:67], v[232:235], v[184:187], a[64:67]\n"
    "v_mfma_f32_16x16x32_bf16 a[100:103], v[232:235], v[188:191], a[100:103]\n"
    "v_mfma_f32_16x16x32_bf16 a[136:139], v[232:235], v[192:195], a[136:139]\n"
    "v_mfma_f32_16x16x32_bf16 a[172:175], v[232:235], v[196:199], a[172:175]\n"
    "v_mfma_f32_16x16x32_bf16 a[208:211], v[232:235], v[200:203], a[208:211]\n"
    "buffer_load_dwordx4 v[232:235], v245, s[40:43], s63 offen sc1\n"
    "v_mfma_f32_16x16x32_bf16 a[32:35], v[236:239], v[180:183], a[32:35]\n"
    "v_mfma_f32_16x16x32_bf16 a[68:71], v[236:239], v[184:187], a[68:71]\n"
    "v_mfma_f32_16x16x32_bf16 a[104:107], v[236:239], v[188:191], a[104:107]\n"
    "v_mfma_f32_16x16x32_bf16 a[140:143], v[236:239], v[192:195], a[140:143]\n"
    "v_mfma_f32_16x16x32_bf16 a[176:179], v[236:239], v[196:199], a[176:179]\n"
    "s_waitcnt vmcnt(33)\n"
    "v_mfma_f32_16x16x32_bf16 a[212:215], v[236:239], v[200:203], a[212:215]\n"
    "buffer_load_dwordx4 v[236:239], v245, s[40:43], s64 offen sc1\n"
    "v_add_u32 v245, 0x2400, v245\n"
    "s_cmp_lg_u32 s68, 0\n"
    "s_cbranch_scc1 L_pub1%=\n"
    "s_mov_b32 s68, 1\n"
    "s_cmp_lg_u64 s[78:79], 0\n"
    "s_cbranch_scc1 L_pub1%=\n"
    "s_mov_b64 exec, 3\n"
    "global_store_dwordx2 v252, v[248:249], %[pcnt] offset:256 sc1\n"
    "s_mov_b64 exec, -1\n"
    "L_pub1%=:\n"
    "s_cmp_lg_u32 s69, 0\n"
    "s_cbranch_scc1 L_oloop%=\n"
    "s_cmp_eq_u32 s68, 2\n"
    "s_cbranch_scc1 L_otail%=\n"
    "s_mov_b32 s71, 0x40000\n"
    "L_poll1%=:\n"
    "v_cmp_ne_u64 vcc, v[246:247], v[248:249]\n"
    "s_sub_u32 s71, s71, 1\n"
    "s_cbranch_scc1 L_poll1%=_ok\n"
    "s_cbranch_vccz L_poll1%=_ok\n"
    "s_sleep 8\n"
    "global_load_dwordx2 v[246:247], v241, %[pcnt] offset:256 sc1\n"
    "s_waitcnt vmcnt(0)\n"
    "s_branch L_poll1%=\n"
    "L_poll1%=_ok:\n"
    "s_barrier\n"
    "s_mov_b32 s68, 2\n"
    "s_mov_b32 s69, 4\n"
    "s_branch L_oloop%=\n"
    "L_otail%=:\n"
    "v_mfma_f32_16x16x32_bf16 a[0:3], v[24:27], v[0:3], a[0:3]\n"
    "v_mfma_f32_16x16x32_bf16 a[36:39], v[24:27], v[4:7], a[36:39]\n"
    "v_mfma_f32_16x16x32_bf16 a[72:75], v[24:27], v[8:11], a[72:75]\n"
    "buffer_load_dwordx4 v[180:183], v244, s[36:39], s65 offen sc1\n"
    "v_mfma_f32_16x16x32_bf16 a[108:111], v[24:27], v[12:15], a[108:111]\n"
    "v_mfma_f32_16x16x32_bf16 a[144:147], v[24:27], v[16:19], a[144:147]\n"
    "v_mfma_f32_16x16x32_bf16 a[180:183], v[24:27], v[20:23], a[180:183]\n"
    "v_mfma_f32_16x16x32_bf16 a[4:7], v[28:31], v[0:3], a[4:7]\n"
    "v_mfma_f32_16x16x32_bf16 a[40:43], v[28:31], v[4:7], a[40:43]\n"
    "v_mfma_f32_16x16x32_bf16 a[76:79], v[28:31], v[8:11], a[76:79]\n"
    "buffer_load_dwordx4 v[184:187], v244, s[36:39], s65 offen offset:1024 sc1\n"
    "v_mfma_f32_16x16x32_bf16 a[112:115], v[28:31], v[12:15], a[112:115]\n"
    "v_mfma_f32_16x16x32_bf16 a[148:151], v[28:31], v[16:19], a[148:151]\n"
    "v_mfma_f32_16x16x32_bf16 a[184:187], v[28:31], v[20:23], a[184:187]\n"
    "v_mfma_f32_16x16x32_bf16 a[8:11], v[32:35], v[0:3], a[8:11]\n"
    "v_mfma_f32_16x16x32_bf16 a[44:47], v[32:35], v[4:7], a[44:47]\n"
    "v_mfma_f32_16x16x32_bf16 a[80:83], v[32:35], v[8:11], a[80:83]\n"
    "buffer_load_dwordx4 v[188:191], v244, s[36:39], s65 offen offset:2048 sc1\n"
    "v_mfma_f32_16x16x32_bf16 a[116:119], v[32:35], v[12:15], a[116:119]\n"
    "v_mfma_f32_16x16x32_bf16 a[152:155], v[32:35], v[16:19], a[152:155]\n"
    "v_mfma_f32_16x16x32_bf16 a[188:191], v[32:35], v[20:23], a[188:191]\n"
    "v_mfma_f32_16x16x32_bf16 a[12:15], v[36:39], v[0:3], a[12:15]\n"
    "v_mfma_f32_16x16x32_bf16 a[48:51], v[36:39], v[4:7], a[48:51]\n"
    "v_mfma_f32_16x16x32_bf16 a[84:87], v[36:39], v[8:11], a[84:87]\n"
    "buffer_load_dwordx4 v[192:195], v244, s[36:39], s66 offen sc1\n"
    "v_mfma_f32_16x16x32_bf16 a[120:123], v[36:39], v[12:15], a[120:123]\n"
    "v_mfma_f32_16x16x32_bf16 a[156:159], v[36:39], v[16:19], a[156:159]\n"
    "v_mfma_f32_16x16x32_bf16 a[192:195], v[36:39], v[20:23], a[192:195]\n"
    "v_mfma_f32_16x16x32_bf16 a[16:19], v[40:43], v[0:3], a[16:19]\n"
    "v_mfma_f32_16x16x32_bf16 a[52:55], v[40:43], v[4:7], a[52:55]\n"
    "v_mfma_f32_16x16x32_bf16 a[88:91], v[40:43], v[8:11], a[88:91]\n"
    "buffer_load_dwordx4 v[196:199], v244, s[36:39], s66 offen offset:1024 sc1\n"
    "v_mfma_f32_16x16x32_bf16 a[124:127], v[40:43], v[12:15], a[124:127]\n"
    "v_mfma_f32_16x16x32_bf16 a[160:163], v[40:43], v[16:19], a[160:163]\n"
    "v_mfma_f32_16x16x32_bf16 a[196:199], v[40:43], v[20:23], a[196:199]\n"
    "v_mfma_f32_16x16x32_bf16 a[20:23], v[44:47], v[0:3], a[20:23]\n"
    "v_mfma_f32_16x16x32_bf16 a[56:59], v[44:47], v[4:7], a[56:59]\n"
    "v_mfma_f32_16x16x32_bf16 a[92:95], v[44:47], v[8:11], a[92:95]\n"
    "buffer_load_dwordx4 v[200:203], v244, s[36:39], s66 offen offset:2048 sc1\n"
    "v_mfma_f32_16x16x32_bf16 a[128:131], v[44:47], v[12:15], a[128:131]\n"
    "v_mfma_f32_16x16x32_bf16 a[164:167], v[44:47], v[16:19], a[164:167]\n"
    "v_add_u32 v244, 0x1800, v244\n"
    "v_mfma_f32_16x16x32_bf16 a[200:203], v[44:47], v[20:23], a[200:203]\n"
    "v_mfma_f32_16x16x32_bf16 a[24:27], v[48:51], v[0:3], a[24:27]\n"
    "v_mfma_f32_16x16x32_bf16 a[60:63], v[48:51], v[4:7], a[60:63]\n"
    "v_mfma_f32_16x16x32_bf16 a[96:99], v[48:51], v[8:11], a[96:99]\n"
    "v_mfma_f32_16x16x32_bf16 a[132:135], v[48:51], v[12:15], a[132:135]\n"
    "v_mfma_f32_16x16x32_bf16 a[168:171], v[48:51], v[16:19], a[168:171]\n"
    "v_mfma_f32_16x16x32_bf16 a[204:207], v[48:51], v[20:23], a[204:207]\n"
    "v_mfma_f32_16x16x32_bf16 a[28:31], v[52:55], v[0:3], a[28:31]\n"
    "v_mfma_f32_16x16x32_bf16 a[64:67], v[52:55], v[4:7], a[64:67]\n"
    "v_mfma_f32_16x16x32_bf16 a[100:103], v[52:55], v[8:11], a[100:103]\n"
    "v_mfma_f32_16x16x32_bf16 a[136:139], v[52:55], v[12:15], a[136:139]\n"
    "v_mfma_f32_16x16x32_bf16 a[172:175], v[52:55], v[16:19], a[172:175]\n"
    "v_mfma_f32_16x16x32_bf16 a[208:211], v[52:55], v[20:23], a[208:211]\n"
    "v_mfma_f32_16x16x32_bf16 a[32:35], v[56:59], v[0:3], a[32:35]\n"
    "v_mfma_f32_16x16x32_bf16 a[68:71], v[56:59], v[4:7], a[68:71]\n"
    "v_mfma_f32_16x16x32_bf16 a[104:107], v[56:59], v[8:11], a[104:107]\n"
    "v_mfma_f32_16x16x32_bf16 a[140:143], v[56:59], v[12:15], a[140:143]\n"
    "v_mfma_f32_16x16x32_bf16 a[176:179], v[56:59], v[16:19], a[176:179]\n"
    "s_waitcnt vmcnt(25)\n"
    "v_mfma_f32_16x16x32_bf16 a[212:215], v[56:59], v[20:23], a[212:215]\n"
    "v_mfma_f32_16x16x32_bf16 a[0:3], v[84:87], v[60:63], a[0:3]\n"
    "v_mfma_f32_16x16x32_bf16 a[36:39], v[84:87], v[64:67], a[36:39]\n"
    "v_mfma_f32_16x16x32_bf16 a[72:75], v[84:87], v[68:71], a[72:75]\n"
    "v_mfma_f32_16x16x32_bf16 a[108:111], v[84:87], v[72:75], a[108:111]\n"
    "v_mfma_f32_16x16x32_bf16 a[144:147], v[84:87], v[76:79], a[144:147]\n"
    "v_mfma_f32_16x16x32_bf16 a[180:183], v[84:87], v[80:83], a[180:183]\n"
    "v_mfma_f32_16x16x32_bf16 a[4:7], v[88:91], v[60:63], a[4:7]\n"
    "v_mfma_f32_16x16x32_bf16 a[40:43], v[88:91], v[64:67], a[40:43]\n"
    "v_mfma_f32_16x16x32_bf16 a[76:79], v[88:91], v[68:71], a[76:79]\n"
    "v_mfma_f32_16x16x32_bf16 a[112:115], v[88:91], v[72:75], a[112:115]\n"
    "v_mfma_f32_16x16x32_bf16 a[148:151], v[88:91], v[76:79], a[148:151]\n"
    "v_mfma_f32_16x16x32_bf16 a[184:187], v[88:91], v[80:83], a[184:187]\n"
    "v_mfma_f32_16x16x32_bf16 a[8:11], v[92:95], v[60:63], a[8:11]\n"
    "v_mfma_f32_16x16x32_bf16 a[44:47], v[92:95], v[64:67], a[44:47]\n"
    "v_mfma_f32_16x16x32_bf16 a[80:83], v[92:95], v[68:71], a[80:83]\n"
    "v_mfma_f32_16x16x32_bf16 a[116:119], v[92:95], v[72:75], a[116:119]\n"
    "v_mfma_f32_16x16x32_bf16 a[152:155], v[92:95], v[76:79], a[152:155]\n"
    "v_mfma_f32_16x16x32_bf16 a[188:191], v[92:95], v[80:83], a[188:191]\n"
    "v_mfma_f32_16x16x32_bf16 a[12:15], v[96:99], v[60:63], a[12:15]\n"
    "v_mfma_f32_16x16x32_bf16 a[48:51], v[96:99], v[64:67], a[48:51]\n"
    "v_mfma_f32_16x16x32_bf16 a[84:87], v[96:99], v[68:71], a[84:87]\n"
    "v_mfma_f32_16x16x32_bf16 a[120:123], v[96:99], v[72:75], a[120:123]\n"
    "v_mfma_f32_16x16x32_bf16 a[156:159], v[96:99], v[76:79], a[156:159]\n"
    "v_mfma_f32_16x16x32_bf16 a[192:195], v[96:99], v[80:83], a[192:195]\n"
    "v_mfma_f32_16x16x32_bf16 a[16:19], v[100:103], v[60:63], a[16:19]\n"
    "v_mfma_f32_16x16x32_bf16 a[52:55], v[100:103], v[64:67], a[52:55]\n"
    "v_mfma_f32_16x16x32_bf16 a[88:91], v[100:103], v[68:71], a[88:91]\n"
    "v_mfma_f32_16x16x32_bf16 a[124:127], v[100:103], v[72:75], a[124:127]\n"
    "v_mfma_f32_16x16x32_bf16 a[160:163], v[100:103], v[76:79], a[160:163]\n"
    "v_mfma_f32_16x16x32_bf16 a[196:199], v[100:103], v[80:83], a[196:199]\n"
    "v_mfma_f32_16x16x32_bf16 a[20:23], v[104:107], v[60:63], a[20:23]\n"
    "v_mfma_f32_16x16x32_bf16 a[56:59], v[104:107], v[64:67], a[56:59]\n"
    "v_mfma_f32_16x16x32_bf16 a[92:95], v[104:107], v[68:71], a[92:95]\n"
    "v_mfma_f32_16x16x32_bf16 a[128:131], v[104:107], v[72:75], a[128:131]\n"
    "v_mfma_f32_16x16x32_bf16 a[164:167], v[104:107], v[76:79], a[164:167]\n"
    "v_mfma_f32_16x16x32_bf16 a[200:203], v[104:107], v[80:83], a[200:203]\n"
    "v_mfma_f32_16x16x32_bf16 a[24:27], v[108:111], v[60:63], a[24:27]\n"
    "v_mfma_f32_16x16x32_bf16 a[60:63], v[108:111], v[64:67], a[60:63]\n"
    "v_mfma_f32_16x16x32_bf16 a[96:99], v[108:111], v[68:71], a[96:99]\n"
    "v_mfma_f32_16x16x32_bf16 a[132:135], v[108:111], v[72:75], a[132:135]\n"
    "v_mfma_f32_16x16x32_bf16 a[168:171], v[108:111], v[76:79], a[168:171]\n"
    "v_mfma_f32_16x16x32_bf16 a[204:207], v[108:111], v[80:83], a[204:207]\n"
    "v_mfma_f32_16x16x32_bf16 a[28:31], v[112:115], v[60:63], a[28:31]\n"
    "v_mfma_f32_16x16x32_bf16 a[64:67], v[112:115], v[64:67], a[64:67]\n"
    "v_mfma_f32_16x16x32_bf16 a[100:103], v[112:115], v[68:71], a[100:103]\n"
    "v_mfma_f32_16x16x32_bf16 a[136:139], v[112:115], v[72:75], a[136:139]\n"
    "v_mfma_f32_16x16x32_bf16 a[172:175], v[112:115], v[76:79], a[172:175]\n"
    "v_mfma_f32_16x16x32_bf16 a[208:211], v[112:115], v[80:83], a[208:211]\n"
    "v_mfma_f32_16x16x32_bf16 a[32:35], v[116:119], v[60:63], a[32:35]\n"
    "v_mfma_f32_16x16x32_bf16 a[68:71], v[116:119], v[64:67], a[68:71]\n"
    "v_mfma_f32_16x16x32_bf16 a[104:107], v[116:119], v[68:71], a[104:107]\n"
    "v_mfma_f32_16x16x32_bf16 a[140:143], v[116:119], v[72:75], a[140:143]\n"
    "v_mfma_f32_16x16x32_bf16 a[176:179], v[116:119], v[76:79], a[176:179]\n"
    "s_waitcnt vmcnt(0)\n"
    "v_mfma_f32_16x16x32_bf16 a[212:215], v[116:119], v[80:83], a[212:215]\n"
    "v_mfma_f32_16x16x32_bf16 a[108:111], v[144:147], v[132:135], a[108:111]\n"
    "v_mfma_f32_16x16x32_bf16 a[108:111], v[204:207], v[192:195], a[108:111]\n"
    "v_mfma_f32_16x16x32_bf16 a[112:115], v[148:151], v[132:135], a[112:115]\n"
    "v_mfma_f32_16x16x32_bf16 a[112:115], v[208:211], v[192:195], a[112:115]\n"
    "v_mfma_f32_16x16x32_bf16 a[116:119], v[152:155], v[132:135], a[116:119]\n"
    "v_mfma_f32_16x16x32_bf16 a[116:119], v[212:215], v[192:195], a[116:119]\n"
    "v_mfma_f32_16x16x32_bf16 a[120:123], v[156:159], v[132:135], a[120:123]\n"
    "v_mfma_f32_16x16x32_bf16 a[120:123], v[216:219], v[192:195], a[120:123]\n"
    "v_mfma_f32_16x16x32_bf16 a[124:127], v[160:163], v[132:135], a[124:127]\n"
    "v_mfma_f32_16x16x32_bf16 a[124:127], v[220:223], v[192:195], a[124:127]\n"
    "v_mfma_f32_16x16x32_bf16 a[128:131], v[164:167], v[132:135], a[128:131]\n"
    "v_mfma_f32_16x16x32_bf16 a[128:131], v[224:227], v[192:195], a[128:131]\n"
    "v_mfma_f32_16x16x32_bf16 a[132:135], v[168:171], v[132:135], a[132:135]\n"
    "v_mfma_f32_16x16x32_bf16 a[132:135], v[228:231], v[192:195], a[132:135]\n"
    "v_mfma_f32_16x16x32_bf16 a[136:139], v[172:175], v[132:135], a[136:139]\n"
    "v_mfma_f32_16x16x32_bf16 a[136:139], v[232:235], v[192:195], a[136:139]\n"
    "v_mfma_f32_16x16x32_bf16 a[140:143], v[176:179], v[132:135], a[140:143]\n"
    "v_mfma_f32_16x16x32_bf16 a[140:143], v[236:239], v[192:195], a[140:143]\n"
    "v_mfma_f32_16x16x32_bf16 a[144:147], v[144:147], v[136:139], a[144:147]\n"
    "v_mfma_f32_16x16x32_bf16 a[144:147], v[204:207], v[196:199], a[144:147]\n"
    "v_mfma_f32_16x16x32_bf16 a[148:151], v[148:151], v[136:139], a[148:151]\n"
    "v_mfma_f32_16x16x32_bf16 a[148:151], v[208:211], v[196:199], a[148:151]\n"
    "v_mfma_f32_16x16x32_bf16 a[152:155], v[152:155], v[136:139], a[152:155]\n"
    "ds_write_b128 v250, a[108:111] offset:0\n"
    "v_mfma_f32_16x16x32_bf16 a[152:155], v[212:215], v[196:199], a[152:155]\n"
    "v_mfma_f32_16x16x32_bf16 a[156:159], v[156:159], v[136:139], a[156:159]\n"
    "v_mfma_f32_16x16x32_bf16 a[156:159], v[216:219], v[196:199], a[156:159]\n"
    "ds_write_b128 v250, a[112:115] offset:1024\n"
    "v_mfma_f32_16x16x32_bf16 a[160:163], v[160:163], v[136:139], a[160:163]\n"
    "v_mfma_f32_16x16x32_bf16 a[160:163], v[220:223], v[196:199], a[160:163]\n"
    "v_mfma_f32_16x16x32_bf16 a[164:167], v[164:167], v[136:139], a[164:167]\n"
    "ds_write_b128 v250, a[116:119] offset:2048\n"
    "v_mfma_f32_16x16x32_bf16 a[164:167], v[224:227], v[196:199], a[164:167]\n"
    "v_mfma_f32_16x16x32_bf16 a[168:171], v[168:171], v[136:139], a[168:171]\n"
    "v_mfma_f32_16x16x32_bf16 a[168:171], v[228:231], v[196:199], a[168:171]\n"
    "ds_write_b128 v250, a[120:123] offset:3072\n"
    "v_mfma_f32_16x16x32_bf16 a[172:175], v[172:175], v[136:139], a[172:175]\n"
    "v_mfma_f32_16x16x32_bf16 a[172:175], v[232:235], v[196:199], a[172:175]\n"
    "v_mfma_f32_16x16x32_bf16 a[176:179], v[176:179], v[136:139], a[176:179]\n"
    "ds_write_b128 v250, a[124:127] offset:4096\n"
    "v_mfma_f32_16x16x32_bf16 a[176:179], v[236:239], v[196:199], a[176:179]\n"
    "v_mfma_f32_16x16x32_bf16 a[180:183], v[144:147], v[140:143], a[180:183]\n"
    "v_mfma_f32_16x16x32_bf16 a[180:183], v[204:207], v[200:203], a[180:183]\n"
    "ds_write_b128 v250, a[128:131] offset:5120\n"
    "v_mfma_f32_16x16x32_bf16 a[184:187], v[148:151], v[140:143], a[184:187]\n"
    "v_mfma_f32_16x16x32_bf16 a[184:187], v[208:211], v[200:203], a[184:187]\n"
    "v_mfma_f32_16x16x32_bf16 a[188:191], v[152:155], v[140:143], a[188:191]\n"
    "ds_write_b128 v250, a[132:135] offset:6144\n"
    "v_mfma_f32_16x16x32_bf16 a[188:191], v[212:215], v[200:203], a[188:191]\n"
    "v_mfma_f32_16x16x32_bf16 a[192:195], v[156:159], v[140:143], a[192:195]\n"
    "v_mfma_f32_16x16x32_bf16 a[192:195], v[216:219], v[200:203], a[192:195]\n"
    "ds_write_b128 v250, a[136:139] offset:7168\n"
    "v_mfma_f32_16x16x32_bf16 a[196:199], v[160:163], v[140:143], a[196:199]\n"
    "v_mfma_f32_16x16x32_bf16 a[196:199], v[220:223], v[200:203], a[196:199]\n"
    "v_mfma_f32_16x16x32_bf16 a[200:203], v[164:167], v[140:143], a[200:203]\n"
    "ds_write_b128 v250, a[140:143] offset:8192\n"
    "v_mfma_f32_16x16x32_bf16 a[200:203], v[224:227], v[200:203], a[200:203]\n"
    "v_mfma_f32_16x16x32_bf16 a[204:207], v[168:171], v[140:143], a[204:207]\n"
    "v_mfma_f32_16x16x32_bf16 a[204:207], v[228:231], v[200:203], a[204:207]\n"
    "ds_write_b128 v250, a[144:147] offset:9216\n"
    "v_mfma_f32_16x16x32_bf16 a[208:211], v[172:175], v[140:143], a[208:211]\n"
    "v_mfma_f32_16x16x32_bf16 a[208:211], v[232:235], v[200:203], a[208:211]\n"
    "v_mfma_f32_16x16x32_bf16 a[212:215], v[176:179], v[140:143], a[212:215]\n"
    "ds_write_b128 v250, a[148:151] offset:10240\n"
    "v_mfma_f32_16x16x32_bf16 a[212:215], v[236:239], v[200:203], a[212:215]\n"
    "v_mfma_f32_16x16x32_bf16 a[0:3], v[144:147], v[120:123], a[0:3]\n"
    "v_mfma_f32_16x16x32_bf16 a[0:3], v[204:207], v[180:183], a[0:3]\n"
    "ds_write_b128 v250, a[152:155] offset:11264\n"
    "v_mfma_f32_16x16x32_bf16 a[4:7], v[148:151], v[120:123], a[4:7]\n"
    "v_mfma_f32_16x16x32_bf16 a[4:7], v[208:211], v[180:183], a[4:7]\n"
    "v_mfma_f32_16x16x32_bf16 a[8:11], v[152:155], v[120:123], a[8:11]\n"
    "ds_write_b128 v250, a[156:159] offset:12288\n"
    "v_mfma_f32_16x16x32_bf16 a[8:11], v[212:215], v[180:183], a[8:11]\n"
    "v_mfma_f32_16x16x32_bf16 a[12:15], v[156:159], v[120:123], a[12:15]\n"
    "v_mfma_f32_16x16x32_bf16 a[12:15], v[216:219], v[180:183], a[12:15]\n"
    "ds_write_b128 v250, a[160:163] offset:13312\n"
    "v_mfma_f32_16x16x32_bf16 a[16:19], v[160:163], v[120:123], a[16:19]\n"
    "v_accvgpr_read_b32 v0, a0\n"
    "v_accvgpr_read_b32 v1, a1\n"
    "v_mfma_f32_16x16x32_bf16 a[16:19], v[220:223], v[180:183], a[16:19]\n"
    "v_accvgpr_read_b32 v2, a2\n"
    "v_accvgpr_read_b32 v3, a3\n"
    "v_mfma_f32_16x16x32_bf16 a[20:23], v[164:167], v[120:123], a[20:23]\n"
    "ds_write_b128 v250, a[164:167] offset:14336\n"
    "v_accvgpr_read_b32 v4, a4\n"
    "v_accvgpr_read_b32 v5, a5\n"
    "v_mfma_f32_16x16x32_bf16 a[20:23], v[224:227], v[180:183], a[20:23]\n"
    "v_accvgpr_read_b32 v6, a6\n"
    "v_accvgpr_read_b32 v7, a7\n"
    "v_mfma_f32_16x16x32_bf16 a[24:27], v[168:171], v[120:123], a[24:27]\n"
    "v_accvgpr_read_b32 v8, a8\n"
    "v_accvgpr_read_b32 v9, a9\n"
    "v_mfma_f32_16x16x32_bf16 a[24:27], v[228:231], v[180:183], a[24:27]\n"
    "ds_write_b128 v250, a[168:171] offset:15360\n"
    "v_accvgpr_read_b32 v10, a10\n"
    "v_accvgpr_read_b32 v11, a11\n"
    "v_mfma_f32_16x16x32_bf16 a[28:31], v[172:175], v[120:123], a[28:31]\n"
    "v_accvgpr_read_b32 v12, a12\n"
    "v_accvgpr_read_b32 v13, a13\n"
    "v_mfma_f32_16x16x32_bf16 a[28:31], v[232:235], v[180:183], a[28:31]\n"
    "v_accvgpr_read_b32 v14, a14\n"
    "v_accvgpr_read_b32 v15, a15\n"
    "v_mfma_f32_16x16x32_bf16 a[32:35], v[176:179], v[120:123], a[32:35]\n"
    "ds_write_b128 v250, a[172:175] offset:16384\n"
    "v_accvgpr_read_b32 v16, a16\n"
    "v_accvgpr_read_b32 v17, a17\n"
    "v_mfma_f32_16x16x32_bf16 a[32:35], v[236:239], v[180:183], a[32:35]\n"
    "v_accvgpr_read_b32 v18, a18\n"
    "v_accvgpr_read_b32 v19, a19\n"
    "v_mfma_f32_16x16x32_bf16 a[36:39], v[144:147], v[124:127], a[36:39]\n"
    "v_accvgpr_read_b32 v20, a20\n"
    "v_accvgpr_read_b32 v21, a21\n"
    "v_mfma_f32_16x16x32_bf16 a[36:39], v[204:207], v[184:187], a[36:39]\n"
    "ds_write_b128 v250, a[176:179] offset:17408\n"
    "v_accvgpr_read_b32 v22, a22\n"
    "v_accvgpr_read_b32 v23, a23\n"
    "v_mfma_f32_16x16x32_bf16 a[40:43], v[148:151], v[124:127], a[40:43]\n"
    "v_accvgpr_read_b32 v24, a24\n"
    "v_accvgpr_read_b32 v25, a25\n"
    "v_mfma_f32_16x16x32_bf16 a[40:43], v[208:211], v[184:187], a[40:43]\n"
    "v_accvgpr_read_b32 v26, a26\n"
    "v_accvgpr_read_b32 v27, a27\n"
    "v_mfma_f32_16x16x32_bf16 a[44:47], v[152:155], v[124:127], a[44:47]\n"
    "ds_write_b128 v250, a[180:183] offset:18432\n"
    "v_accvgpr_read_b32 v28, a28\n"
    "v_accvgpr_read_b32 v29, a29\n"
    "v_mfma_f32_16x16x32_bf16 a[44:47], v[212:215], v[184:187], a[44:47]\n"
    "v_accvgpr_read_b32 v30, a30\n"
    "v_accvgpr_read_b32 v31, a31\n"
    "v_mfma_f32_16x16x32_bf16 a[48:51], v[156:159], v[124:127], a[48:51]\n"
    "v_accvgpr_read_b32 v32, a32\n"
    "v_accvgpr_read_b32 v33, a33\n"
    "v_mfma_f32_16x16x32_bf16 a[48:51], v[216:219], v[184:187], a[48:51]\n"
    "ds_write_b128 v250, a[184:187] offset:19456\n"
    "v_accvgpr_read_b32 v34, a34\n"
    "v_accvgpr_read_b32 v35, a35\n"
    "v_mfma_f32_16x16x32_bf16 a[52:55], v[160:163], v[124:127], a[52:55]\n"
    "v_accvgpr_read_b32 v36, a36\n"
    "v_accvgpr_read_b32 v37, a37\n"
    "v_mfma_f32_16x16x32_bf16 a[52:55], v[220:223], v[184:187], a[52:55]\n"
    "v_accvgpr_read_b32 v38, a38\n"
    "v_accvgpr_read_b32 v39, a39\n"
    "v_mfma_f32_16x16x32_bf16 a[56:59], v[164:167], v[124:127], a[56:59]\n"
    "ds_write_b128 v250, a[188:191] offset:20480\n"
    "v_accvgpr_read_b32 v40, a40\n"
    "v_accvgpr_read_b32 v41, a41\n"
    "v_mfma_f32_16x16x32_bf16 a[56:59], v[224:227], v[184:187], a[56:59]\n"
    "v_accvgpr_read_b32 v42, a42\n"
    "v_accvgpr_read_b32 v43, a43\n"
    "v_mfma_f32_16x16x32_bf16 a[60:63], v[168:171], v[124:127], a[60:63]\n"
    "v_accvgpr_read_b32 v44, a44\n"
    "v_accvgpr_read_b32 v45, a45\n"
    "v_mfma_f32_16x16x32_bf16 a[60:63], v[228:231], v[184:187], a[60:63]\n"
    "ds_write_b128 v250, a[192:195] offset:21504\n"
    "v_accvgpr_read_b32 v46, a46\n"
    "v_accvgpr_read_b32 v47, a47\n"
    "v_mfma_f32_16x16x32_bf16 a[64:67], v[172:175], v[124:127], a[64:67]\n"
    "v_accvgpr_read_b32 v48, a48\n"
    "v_accvgpr_read_b32 v49, a49\n"
    "v_mfma_f32_16x16x32_bf16 a[64:67], v[232:235], v[184:187], a[64:67]\n"
    "v_accvgpr_read_b32 v50, a50\n"
    "v_accvgpr_read_b32 v51, a51\n"
    "v_mfma_f32_16x16x32_bf16 a[68:71], v[176:179], v[124:127], a[68:71]\n"
    "ds_write_b128 v250, a[196:199] offset:22528\n"
    "v_accvgpr_read_b32 v52, a52\n"
    "v_accvgpr_read_b32 v53, a53\n"
    "v_mfma_f32_16x16x32_bf16 a[68:71], v[236:239], v[184:187], a[68:71]\n"
    "v_accvgpr_read_b32 v54, a54\n"
    "v_accvgpr_read_b32 v55, a55\n"
    "v_mfma_f32_16x16x32_bf16 a[72:75], v[144:147], v[128:131], a[72:75]\n"
    "v_accvgpr_read_b32 v56, a56\n"
    "v_accvgpr_read_b32 v57, a57\n"
    "v_mfma_f32_16x16x32_bf16 a[72:75], v[204:207], v[188:191], a[72:75]\n"
    "ds_write_b128 v250, a[200:203] offset:23552\n"
    "v_accvgpr_read_b32 v58, a58\n"
    "v_accvgpr_read_b32 v59, a59\n"
    "v_mfma_f32_16x16x32_bf16 a[76:79], v[148:151], v[128:131], a[76:79]\n"
    "v_accvgpr_read_b32 v60, a60\n"
    "v_accvgpr_read_b32 v61, a61\n"
    "v_mfma_f32_16x16x32_bf16 a[76:79], v[208:211], v[188:191], a[76:79]\n"
    "v_accvgpr_read_b32 v62, a62\n"
    "v_accvgpr_read_b32 v63, a63\n"
    "v_mfma_f32_16x16x32_bf16 a[80:83], v[152:155], v[128:131], a[80:83]\n"
    "ds_write_b128 v250, a[204:207] offset:24576\n"
    "v_accvgpr_read_b32 v64, a64\n"
    "v_accvgpr_read_b32 v65, a65\n"
    "v_mfma_f32_16x16x32_bf16 a[80:83], v[212:215], v[188:191], a[80:83]\n"
    "v_accvgpr_read_b32 v66, a66\n"
    "v_accvgpr_read_b32 v67, a67\n"
    "v_mfma_f32_16x16x32_bf16 a[84:87], v[156:159], v[128:131], a[84:87]\n"
    "v_accvgpr_read_b32 v68, a68\n"
    "v_accvgpr_read_b32 v69, a69\n"
    "v_mfma_f32_16x16x32_bf16 a[84:87], v[216:219], v[188:191], a[84:87]\n"
    "ds_write_b128 v250, a[208:211] offset:25600\n"
    "v_accvgpr_read_b32 v70, a70\n"
    "v_accvgpr_read_b32 v71, a71\n"
    "v_mfma_f32_16x16x32_bf16 a[88:91], v[160:163], v[128:131], a[88:91]\n"
    "v_accvgpr_read_b32 v72, a72\n"
    "v_accvgpr_read_b32 v73, a73\n"
    "v_mfma_f32_16x16x32_bf16 a[88:91], v[220:223], v[188:191], a[88:91]\n"
    "v_accvgpr_read_b32 v74, a74\n"
    "v_accvgpr_read_b32 v75, a75\n"
    "v_mfma_f32_16x16x32_bf16 a[92:95], v[164:167], v[128:131], a[92:95]\n"
    "ds_write_b128 v250, a[212:215] offset:26624\n"
    "v_accvgpr_read_b32 v76, a76\n"
    "v_accvgpr_read_b32 v77, a77\n"
    "v_mfma_f32_16x16x32_bf16 a[92:95], v[224:227], v[188:191], a[92:95]\n"
    "v_accvgpr_read_b32 v78, a78\n"
    "v_accvgpr_read_b32 v79, a79\n"
    "v_mfma_f32_16x16x32_bf16 a[96:99], v[168:171], v[128:131], a[96:99]\n"
    "v_accvgpr_read_b32 v80, a80\n"
    "v_accvgpr_read_b32 v81, a81\n"
    "v_mfma_f32_16x16x32_bf16 a[96:99], v[228:231], v[188:191], a[96:99]\n"
    "v_accvgpr_read_b32 v82, a82\n"
    "v_accvgpr_read_b32 v83, a83\n"
    "v_mfma_f32_16x16x32_bf16 a[100:103], v[172:175], v[128:131], a[100:103]\n"
    "v_accvgpr_read_b32 v84, a84\n"
    "v_accvgpr_read_b32 v85, a85\n"
    "v_mfma_f32_16x16x32_bf16 a[100:103], v[232:235], v[188:191], a[100:103]\n"
    "v_accvgpr_read_b32 v86, a86\n"
    "v_accvgpr_read_b32 v87, a87\n"
    "v_mfma_f32_16x16x32_bf16 a[104:107], v[176:179], v[128:131], a[104:107]\n"
    "v_mfma_f32_16x16x32_bf16 a[104:107], v[236:239], v[188:191], a[104:107]\n"
    "s_waitcnt lgkmcnt(0)\n"
    "s_barrier\n"
    "ds_read_b128 v[124:127], v251 offset:4096\n"
    "ds_read_b128 v[128:131], v251 offset:5120\n"
    "ds_read_b128 v[132:135], v251 offset:6144\n"
    "ds_read_b128 v[136:139], v251 offset:7168\n"
    "ds_read_b128 v[140:143], v251 offset:8192\n"
    "ds_read_b128 v[160:163], v251 offset:13312\n"
    "ds_read_b128 v[164:167], v251 offset:14336\n"
    "ds_read_b128 v[168:171], v251 offset:15360\n"
    "ds_read_b128 v[172:175], v251 offset:16384\n"
    "v_accvgpr_read_b32 v88, a88\n"
    "v_accvgpr_read_b32 v89, a89\n"
    "v_accvgpr_read_b32 v90, a90\n"
    "v_accvgpr_read_b32 v91, a91\n"
    "v_accvgpr_read_b32 v92, a92\n"
    "v_accvgpr_read_b32 v93, a93\n"
    "v_accvgpr_read_b32 v94, a94\n"
    "v_accvgpr_read_b32 v95, a95\n"
    "v_accvgpr_read_b32 v96, a96\n"
    "v_accvgpr_read_b32 v97, a97\n"
    "v_accvgpr_read_b32 v98, a98\n"
    "v_accvgpr_read_b32 v99, a99\n"
    "v_accvgpr_read_b32 v100, a100\n"
    "v_accvgpr_read_b32 v101, a101\n"
    "v_accvgpr_read_b32 v102, a102\n"
    "v_accvgpr_read_b32 v103, a103\n"
    "v_accvgpr_read_b32 v104, a104\n"
    "v_accvgpr_read_b32 v105, a105\n"
    "v_accvgpr_read_b32 v106, a106\n"
    "v_accvgpr_read_b32 v107, a107\n"
    "s_xor_b32 s71, %[wv], 3\n"
    "s_mul_i32 s71, s71, 27648\n"
    "v_add_u32 v245, s71, v242\n"
    "s_mul_i32 s70, %[wv], 27648\n"
    "s_and_b32 s71, %[wv], 1\n"
    "s_lshl_b32 s71, s71, 7\n"
    "s_add_u32 s70, s70, s71\n"
    "s_add_u32 s70, s70, 0x4000\n"
    "v_bfe_u32 v244, v255, 0, 4\n"
    "v_mul_u32_u24 v244, 160, v244\n"
    "v_bfe_u32 v243, v255, 4, 2\n"
    "v_lshl_add_u32 v244, v243, 3, v244\n"
    "v_add_u32 v244, s70, v244\n"
    "ds_read_b128 v[176:179], v251 offset:17408\n"
    "ds_read_b128 v[196:199], v251 offset:22528\n"
    "ds_read_b128 v[200:203], v251 offset:23552\n"
    "ds_read_b128 v[204:207], v251 offset:24576\n"
    "ds_read_b128 v[208:211], v251 offset:25600\n"
    "ds_read_b128 v[212:215], v251 offset:26624\n"
    "ds_read_b128 v[108:111], v251 offset:0\n"
    "ds_read_b128 v[112:115], v251 offset:1024\n"
    "ds_read_b128 v[116:119], v251 offset:2048\n"
    "s_waitcnt lgkmcnt(9)\n"
    "v_pk_add_f32 v[16:17], v[16:17], v[124:125]\n"
    "v_pk_add_f32 v[18:19], v[18:19], v[126:127]\n"
    "v_pk_add_f32 v[20:21], v[20:21], v[128:129]\n"
    "v_pk_add_f32 v[22:23], v[22:23], v[130:131]\n"
    "v_pk_add_f32 v[24:25], v[24:25], v[132:133]\n"
    "v_pk_add_f32 v[26:27], v[26:27], v[134:135]\n"
    "v_pk_add_f32 v[28:29], v[28:29], v[136:137]\n"
    "v_pk_add_f32 v[30:31], v[30:31], v[138:139]\n"
    "v_pk_add_f32 v[32:33], v[32:33], v[140:141]\n"
    "v_pk_add_f32 v[34:35], v[34:35], v[142:143]\n"
    "v_pk_add_f32 v[52:53], v[52:53], v[160:161]\n"
    "v_pk_add_f32 v[54:55], v[54:55], v[162:163]\n"
    "v_pk_add_f32 v[56:57], v[56:57], v[164:165]\n"
    "v_pk_add_f32 v[58:59], v[58:59], v[166:167]\n"
    "v_pk_add_f32 v[60:61], v[60:61], v[168:169]\n"
    "v_pk_add_f32 v[62:63], v[62:63], v[170:171]\n"
    "v_pk_add_f32 v[64:65], v[64:65], v[172:173]\n"
    "v_pk_add_f32 v[66:67], v[66:67], v[174:175]\n"
    "ds_read_b128 v[120:123], v251 offset:3072\n"
    "ds_read_b128 v[144:147], v251 offset:9216\n"
    "ds_read_b128 v[148:151], v251 offset:10240\n"
    "ds_read_b128 v[152:155], v251 offset:11264\n"
    "ds_read_b128 v[156:159], v251 offset:12288\n"
    "ds_read_b128 v[180:183], v251 offset:18432\n"
    "ds_read_b128 v[184:187], v251 offset:19456\n"
    "ds_read_b128 v[188:191], v251 offset:20480\n"
    "ds_read_b128 v[192:195], v251 offset:21504\n"
    "s_waitcnt lgkmcnt(9)\n"
    "v_pk_add_f32 v[68:69], v[68:69], v[176:177]\n"
    "v_pk_add_f32 v[70:71], v[70:71], v[178:179]\n"
    "v_pk_add_f32 v[88:89], v[88:89], v[196:197]\n"
    "v_pk_add_f32 v[90:91], v[90:91], v[198:199]\n"
    "v_pk_add_f32 v[92:93], v[92:93], v[200:201]\n"
    "v_pk_add_f32 v[94:95], v[94:95], v[202:203]\n"
    "v_pk_add_f32 v[96:97], v[96:97], v[204:205]\n"
    "v_pk_add_f32 v[98:99], v[98:99], v[206:207]\n"
    "v_pk_add_f32 v[100:101], v[100:101], v[208:209]\n"
    "v_pk_add_f32 v[102:103], v[102:103], v[210:211]\n"
    "v_pk_add_f32 v[104:105], v[104:105], v[212:213]\n"
    "v_pk_add_f32 v[106:107], v[106:107], v[214:215]\n"
    "v_pk_add_f32 v[0:1], v[0:1], v[108:109]\n"
    "v_pk_add_f32 v[2:3], v[2:3], v[110:111]\n"
    "v_pk_add_f32 v[4:5], v[4:5], v[112:113]\n"
    "v_pk_add_f32 v[6:7], v[6:7], v[114:115]\n"
    "v_pk_add_f32 v[8:9], v[8:9], v[116:117]\n"
    "v_pk_add_f32 v[10:11], v[10:11], v[118:119]\n"
    "s_waitcnt lgkmcnt(0)\n"
    "ds_write_b128 v251, v[16:19] offset:0\n"
    "ds_write_b128 v251, v[20:23] offset:1024\n"
    "ds_write_b128 v251, v[24:27] offset:2048\n"
    "ds_write_b128 v251, v[28:31] offset:3072\n"
    "ds_write_b128 v251, v[32:35] offset:4096\n"
    "ds_write_b128 v251, v[52:55] offset:5120\n"
    "ds_write_b128 v251, v[56:59] offset:6144\n"
    "ds_write_b128 v251, v[60:63] offset:7168\n"
    "ds_write_b128 v251, v[64:67] offset:8192\n"
    "ds_write_b128 v251, v[68:71] offset:9216\n"
    "ds_write_b128 v251, v[88:91] offset:10240\n"
    "ds_write_b128 v251, v[92:95] offset:11264\n"
    "ds_write_b128 v251, v[96:99] offset:12288\n"
    "ds_write_b128 v251, v[100:103] offset:13312\n"
    "ds_write_b128 v251, v[104:107] offset:14336\n"
    "v_pk_add_f32 v[12:13], v[12:13], v[120:121]\n"
    "v_pk_add_f32 v[14:15], v[14:15], v[122:123]\n"
    "v_pk_add_f32 v[36:37], v[36:37], v[144:145]\n"
    "v_pk_add_f32 v[38:39], v[38:39], v[146:147]\n"
    "v_pk_add_f32 v[40:41], v[40:41], v[148:149]\n"
    "v_pk_add_f32 v[42:43], v[42:43], v[150:151]\n"
    "v_pk_add_f32 v[44:45], v[44:45], v[152:153]\n"
    "v_pk_add_f32 v[46:47], v[46:47], v[154:155]\n"
    "v_pk_add_f32 v[48:49], v[48:49], v[156:157]\n"
    "v_pk_add_f32 v[50:51], v[50:51], v[158:159]\n"
    "v_pk_add_f32 v[72:73], v[72:73], v[180:181]\n"
    "v_pk_add_f32 v[74:75], v[74:75], v[182:183]\n"
    "v_pk_add_f32 v[76:77], v[76:77], v[184:185]\n"
    "v_pk_add_f32 v[78:79], v[78:79], v[186:187]\n"
    "v_pk_add_f32 v[80:81], v[80:81], v[188:189]\n"
    "v_pk_add_f32 v[82:83], v[82:83], v[190:191]\n"
    "v_pk_add_f32 v[84:85], v[84:85], v[192:193]\n"
    "v_pk_add_f32 v[86:87], v[86:87], v[194:195]\n"
    "s_bitcmp1_b32 %[wv], 0\n"
    "s_cselect_b32 s70, -32, 32\n"
    "v_mov_b32 v220, v244\n"
    "v_add_u32 v221, s70, v220\n"
    "v_add_u32 v222, s70, v221\n"
    "v_add_u32 v223, s70, v222\n"
    "v_add_u32 v224, s70, v223\n"
    "s_waitcnt lgkmcnt(0)\n"
    "s_barrier\n"
    "ds_read_b128 v[108:111], v245 offset:4096\n"
    "ds_read_b128 v[112:115], v245 offset:3072\n"
    "ds_read_b128 v[116:119], v245 offset:2048\n"
    "ds_read_b128 v[120:123], v245 offset:1024\n"
    "ds_read_b128 v[124:127], v245 offset:0\n"
    "ds_read_b128 v[128:131], v245 offset:9216\n"
    "ds_read_b128 v[132:135], v245 offset:8192\n"
    "ds_read_b128 v[136:139], v245 offset:7168\n"
    "ds_read_b128 v[140:143], v245 offset:6144\n"
    "ds_read_b128 v[144:147], v245 offset:5120\n"
    "ds_read_b128 v[148:151], v245 offset:14336\n"
    "ds_read_b128 v[152:155], v245 offset:13312\n"
    "ds_read_b128 v[156:159], v245 offset:12288\n"
    "ds_read_b128 v[160:163], v245 offset:11264\n"
    "ds_read_b128 v[164:167], v245 offset:10240\n"
    "s_waitcnt lgkmcnt(10)\n"
    "v_pk_add_f32 v[0:1], v[0:1], v[108:109]\n"
    "v_pk_add_f32 v[2:3], v[2:3], v[110:111]\n"
    "v_cvt_pk_bf16_f32 v180, v0, v1\n"
    "v_cvt_pk_bf16_f32 v181, v2, v3\n"
    "ds_write_b64 v220, v[180:181] offset:0\n"
    "v_pk_add_f32 v[4:5], v[4:5], v[112:113]\n"
    "v_pk_add_f32 v[6:7], v[6:7], v[114:115]\n"
    "v_cvt_pk_bf16_f32 v182, v4, v5\n"
    "v_cvt_pk_bf16_f32 v183, v6, v7\n"
    "ds_write_b64 v221, v[182:183] offset:0\n"
    "v_pk_add_f32 v[8:9], v[8:9], v[116:117]\n"
    "v_pk_add_f32 v[10:11], v[10:11], v[118:119]\n"
    "v_cvt_pk_bf16_f32 v184, v8, v9\n"
    "v_cvt_pk_bf16_f32 v185, v10, v11\n"
    "ds_write_b64 v222, v[184:185] offset:0\n"
    "v_pk_add_f32 v[12:13], v[12:13], v[120:121]\n"
    "v_pk_add_f32 v[14:15], v[14:15], v[122:123]\n"
    "v_cvt_pk_bf16_f32 v186, v12, v13\n"
    "v_cvt_pk_bf16_f32 v187, v14, v15\n"
    "ds_write_b64 v223, v[186:187] offset:0\n"
    "v_pk_add_f32 v[16:17], v[16:17], v[124:125]\n"
    "v_pk_add_f32 v[18:19], v[18:19], v[126:127]\n"
    "v_cvt_pk_bf16_f32 v188, v16, v17\n"
    "v_cvt_pk_bf16_f32 v189, v18, v19\n"
    "ds_write_b64 v224, v[188:189] offset:0\n"
    "s_waitcnt lgkmcnt(10)\n"
    "v_pk_add_f32 v[36:37], v[36:37], v[128:129]\n"
    "v_pk_add_f32 v[38:39], v[38:39], v[130:131]\n"
    "v_cvt_pk_bf16_f32 v190, v36, v37\n"
    "v_cvt_pk_bf16_f32 v191, v38, v39\n"
    "ds_write_b64 v220, v[190:191] offset:2560\n"
    "v_pk_add_f32 v[40:41], v[40:41], v[132:133]\n"
    "v_pk_add_f32 v[42:43], v[42:43], v[134:135]\n"
    "v_cvt_pk_bf16_f32 v192, v40, v41\n"
    "v_cvt_pk_bf16_f32 v193, v42, v43\n"
    "ds_write_b64 v221, v[192:193] offset:2560\n"
    "v_pk_add_f32 v[44:45], v[44:45], v[136:137]\n"
    "v_pk_add_f32 v[46:47], v[46:47], v[138:139]\n"
    "v_cvt_pk_bf16_f32 v194, v44, v45\n"
    "v_cvt_pk_bf16_f32 v195, v46, v47\n"
    "ds_write_b64 v222, v[194:195] offset:2560\n"
    "v_pk_add_f32 v[48:49], v[48:49], v[140:141]\n"
    "v_pk_add_f32 v[50:51], v[50:51], v[142:143]\n"
    "v_cvt_pk_bf16_f32 v196, v48, v49\n"
    "v_cvt_pk_bf16_f32 v197, v50, v51\n"
    "ds_write_b64 v223, v[196:197] offset:2560\n"
    "v_pk_add_f32 v[52:53], v[52:53], v[144:145]\n"
    "v_pk_add_f32 v[54:55], v[54:55], v[146:147]\n"
    "v_cvt_pk_bf16_f32 v198, v52, v53\n"
    "v_cvt_pk_bf16_f32 v199, v54, v55\n"
    "ds_write_b64 v224, v[198:199] offset:2560\n"
    "s_waitcnt lgkmcnt(10)\n"
    "v_pk_add_f32 v[72:73], v[72:73], v[148:149]\n"
    "v_pk_add_f32 v[74:75], v[74:75], v[150:151]\n"
    "v_cvt_pk_bf16_f32 v200, v72, v73\n"
    "v_cvt_pk_bf16_f32 v201, v74, v75\n"
    "ds_write_b64 v220, v[200:201] offset:5120\n"
    "v_pk_add_f32 v[76:77], v[76:77], v[152:153]\n"
    "v_pk_add_f32 v[78:79], v[78:79], v[154:155]\n"
    "v_cvt_pk_bf16_f32 v202, v76, v77\n"
    "v_cvt_pk_bf16_f32 v203, v78, v79\n"
    "ds_write_b64 v221, v[202:203] offset:5120\n"
    "v_pk_add_f32 v[80:81], v[80:81], v[156:157]\n"
    "v_pk_add_f32 v[82:83], v[82:83], v[158:159]\n"
    "v_cvt_pk_bf16_f32 v204, v80, v81\n"
    "v_cvt_pk_bf16_f32 v205, v82, v83\n"
    "ds_write_b64 v222, v[204:205] offset:5120\n"
    "v_pk_add_f32 v[84:85], v[84:85], v[160:161]\n"
    "v_pk_add_f32 v[86:87], v[86:87], v[162:163]\n"
    "v_cvt_pk_bf16_f32 v206, v84, v85\n"
    "v_cvt_pk_bf16_f32 v207, v86, v87\n"
    "ds_write_b64 v223, v[206:207] offset:5120\n"
    "v_pk_add_f32 v[88:89], v[88:89], v[164:165]\n"
    "v_pk_add_f32 v[90:91], v[90:91], v[166:167]\n"
    "v_cvt_pk_bf16_f32 v208, v88, v89\n"
    "v_cvt_pk_bf16_f32 v209, v90, v91\n"
    "ds_write_b64 v224, v[208:209] offset:5120\n"
    "s_waitcnt lgkmcnt(0)\n"
    "ds_read_b128 v[0:3], v250 offset:16384\n"
    "ds_read_b128 v[4:7], v250 offset:17344\n"
    "ds_read_b128 v[8:11], v250 offset:18304\n"
    "ds_read_b128 v[12:15], v250 offset:19264\n"
    "ds_read_b128 v[16:19], v250 offset:20224\n"
    "ds_read_b128 v[20:23], v250 offset:21184\n"
    "ds_read_b128 v[24:27], v250 offset:22144\n"
    "ds_read_b128 v[28:31], v250 offset:23104\n"
    "s_add_u32 s70, s67, 0\n"
    "s_waitcnt lgkmcnt(7)\n"
    "buffer_store_dwordx4 v[0:3], v253, s[44:47], s70 offen\n"
    "s_add_u32 s70, s67, 13824\n"
    "s_waitcnt lgkmcnt(6)\n"
    "buffer_store_dwordx4 v[4:7], v253, s[44:47], s70 offen\n"
    "s_add_u32 s70, s67, 27648\n"
    "s_waitcnt lgkmcnt(5)\n"
    "buffer_store_dwordx4 v[8:11], v253, s[44:47], s70 offen\n"
    "s_add_u32 s70, s67, 41472\n"
    "s_waitcnt lgkmcnt(4)\n"
    "buffer_store_dwordx4 v[12:15], v253, s[44:47], s70 offen\n"
    "s_add_u32 s70, s67, 55296\n"
    "s_waitcnt lgkmcnt(3)\n"
    "buffer_store_dwordx4 v[16:19], v253, s[44:47], s70 offen\n"
    "s_add_u32 s70, s67, 69120\n"
    "s_waitcnt lgkmcnt(2)\n"
    "buffer_store_dwordx4 v[20:23], v253, s[44:47], s70 offen\n"
    "s_add_u32 s70, s67, 82944\n"
    "s_waitcnt lgkmcnt(1)\n"
    "buffer_store_dwordx4 v[24:27], v253, s[44:47], s70 offen\n"
    "s_add_u32 s70, s67, 96768\n"
    "s_waitcnt lgkmcnt(0)\n"
    "buffer_store_dwordx4 v[28:31], v253, s[44:47], s70 offen\n"
    "s_waitcnt vmcnt(0)\n"
    "L_exit%=:\n"
    "s_waitcnt vmcnt(0) lgkmcnt(0)\n"
    "s_cmp_lg_u64 s[78:79], 0\n"
    "s_cselect_b32 %[flag], s83, 0\n"
    : [flag] "=&s"(flag)
    : [pk] "s"(pk), [pv] "s"(pv), [pw] "s"(pw), [px] "s"(px), [po] "s"(po), [pwo] "s"(pwo), [pwt] "s"(pwt), [pa] "s"(pa), [pb] "s"(pb), [pc] "s"(pc), [pcnt] "s"(pcnt), [wv] "s"(w), [oo] "s"(oo), [tw] "s"(tw), [uo] "s"(uo), [uw] "s"(uw), [co] "s"(co), [cw] "s"(cw), [rot] "s"(rot), [eo] "s"(eo), [ct] "s"(ct)
    :
      "v0", "v1", "v2", "v3", "v4", "v5", "v6", "v7", "v8", "v9", "v10", "v11", "v12", "v13", "v14", "v15", "v16", "v17", "v18", "v19", "v20", "v21", "v22", "v23",
      "v24", "v25", "v26", "v27", "v28", "v29", "v30", "v31", "v32", "v33", "v34", "v35", "v36", "v37", "v38", "v39", "v40", "v41", "v42", "v43", "v44", "v45", "v46", "v47",
      "v48", "v49", "v50", "v51", "v52", "v53", "v54", "v55", "v56", "v57", "v58", "v59", "v60", "v61", "v62", "v63", "v64", "v65", "v66", "v67", "v68", "v69", "v70", "v71",
      "v72", "v73", "v74", "v75", "v76", "v77", "v78", "v79", "v80", "v81", "v82", "v83", "v84", "v85", "v86", "v87", "v88", "v89", "v90", "v91", "v92", "v93", "v94", "v95",
      "v96", "v97", "v98", "v99", "v100", "v101", "v102", "v103", "v104", "v105", "v106", "v107", "v108", "v109", "v110", "v111", "v112", "v113", "v114", "v115", "v116", "v117", "v118", "v119",
      "v120", "v121", "v122", "v123", "v124", "v125", "v126", "v127", "v128", "v129", "v130", "v131", "v132", "v133", "v134", "v135", "v136", "v137", "v138", "v139", "v140", "v141", "v142", "v143",
      "v144", "v145", "v146", "v147", "v148", "v149", "v150", "v151", "v152", "v153", "v154", "v155", "v156", "v157", "v158", "v159", "v160", "v161", "v162", "v163", "v164", "v165", "v166", "v167",
      "v168", "v169", "v170", "v171", "v172", "v173", "v174", "v175", "v176", "v177", "v178", "v179", "v180", "v181", "v182", "v183", "v184", "v185", "v186", "v187", "v188", "v189", "v190", "v191",
      "v192", "v193", "v194", "v195", "v196", "v197", "v198", "v199", "v200", "v201", "v202", "v203", "v204", "v205", "v206", "v207", "v208", "v209", "v210", "v211", "v212", "v213", "v214", "v215",
      "v216", "v217", "v218", "v219", "v220", "v221", "v222", "v223", "v224", "v225", "v226", "v227", "v228", "v229", "v230", "v231", "v232", "v233", "v234", "v235", "v236", "v237", "v238", "v239",
      "v240", "v241", "v242", "v243", "v244", "v245", "v246", "v247", "v248", "v249", "v250", "v251", "v252", "v253", "v254", "v255", "a0", "a1", "a2", "a3", "a4", "a5", "a6", "a7",
      "a8", "a9", "a10", "a11", "a12", "a13", "a14", "a15", "a16", "a17", "a18", "a19", "a20", "a21", "a22", "a23", "a24", "a25", "a26", "a27", "a28", "a29", "a30", "a31",
      "a32", "a33", "a34", "a35", "a36", "a37", "a38", "a39", "a40", "a41", "a42", "a43", "a44", "a45", "a46", "a47", "a48", "a49", "a50", "a51", "a52", "a53", "a54", "a55",
      "a56", "a57", "a58", "a59", "a60", "a61", "a62", "a63", "a64", "a65", "a66", "a67", "a68", "a69", "a70", "a71", "a72", "a73", "a74", "a75", "a76", "a77", "a78", "a79",
      "a80", "a81", "a82", "a83", "a84", "a85", "a86", "a87", "a88", "a89", "a90", "a91", "a92", "a93", "a94", "a95", "a96", "a97", "a98", "a99", "a100", "a101", "a102", "a103",
      "a104", "a105", "a106", "a107", "a108", "a109", "a110", "a111", "a112", "a113", "a114", "a115", "a116", "a117", "a118", "a119", "a120", "a121", "a122", "a123", "a124", "a125", "a126", "a127",
      "a128", "a129", "a130", "a131", "a132", "a133", "a134", "a135", "a136", "a137", "a138", "a139", "a140", "a141", "a142", "a143", "a144", "a145", "a146", "a147", "a148", "a149", "a150", "a151",
      "a152", "a153", "a154", "a155", "a156", "a157", "a158", "a159", "a160", "a161", "a162", "a163", "a164", "a165", "a166", "a167", "a168", "a169", "a170", "a171", "a172", "a173", "a174", "a175",
      "a176", "a177", "a178", "a179", "a180", "a181", "a182", "a183", "a184", "a185", "a186", "a187", "a188", "a189", "a190", "a191", "a192", "a193", "a194", "a195", "a196", "a197", "a198", "a199",
      "a200", "a201", "a202", "a203", "a204", "a205", "a206", "a207", "a208", "a209", "a210", "a211", "a212", "a213", "a214", "a215", "a216", "a217", "a218", "a219", "a220", "a221", "a222", "a223",
      "a224", "a225", "a226", "a227", "a228", "a229", "a230", "a231", "a232", "a233", "a234", "a235", "a236", "a237", "a238", "a239", "a240", "a241", "a242", "a243", "a244", "a245", "a246", "a247",
      "a248", "a249", "a250", "a251", "a252", "a253", "a254", "a255", "s36", "s37", "s38", "s39", "s40", "s41", "s42", "s43", "s44", "s45", "s46", "s47", "s48", "s49", "s50", "s51",
      "s52", "s53", "s54", "s55", "s56", "s57", "s58", "s59", "s60", "s61", "s62", "s63", "s64", "s65", "s66", "s67", "s68", "s69", "s70", "s71", "s72", "s73", "s74", "s75",
      "s76", "s77", "s78", "s79", "s80", "s81", "s82", "s83", "s84", "s85", "s86", "s87", "s88", "s89", "s90", "s91", "s92", "s93", "vcc", "scc", "memory"
  );
  if (flag != 0) {
    unsigned lane, fw = w, fo = oo;
    const char *fx = px, *fq = pw, *fk = pk, *fv = pv; char* fp = po;
    asm volatile("v_mbcnt_lo_u32_b32 %0, -1, 0\n\tv_mbcnt_hi_u32_b32 %0, -1, %0" : "=v"(lane), "+s"(fw), "+s"(fo), "+s"(fx), "+s"(fq), "+s"(fk), "+s"(fv), "+s"(fp));
#pragma unroll 1
    for (int r = (flag & 1) ? 0 : 1; r < 2; ++r)
#pragma unroll 1
      for (int qb = 0; qb < 6; ++qb)
        slow_rows((const u16*)fx + (96 * (fw & 1) + 16 * qb) * DM, (const u16*)fq + ((fw & 2) + r) * DH, (const u16*)fk + r * SK * DH, (const u16*)fv + r * SK * DH,
                  fp + fo + r * (16 * 6144) + 1024 * qb, lane);
    __builtin_amdgcn_fence(__ATOMIC_RELEASE, "agent");
    asm volatile("s_waitcnt vmcnt(0)" ::: "memory");
    const u64 tag = __hip_atomic_load((u64*)(pcnt + eo), __ATOMIC_RELAXED, __HIP_MEMORY_SCOPE_AGENT) + 1;
    const unsigned tlo = rfl((unsigned)tag), thi = rfl((unsigned)(tag >> 32));
    if (lane == 0) {
#pragma unroll 1
      for (int r = (flag & 1) ? 0 : 1; r < 2; ++r) {
        __hip_atomic_store((u64*)(pcnt + uo + 256 * r), tag, __ATOMIC_RELAXED, __HIP_MEMORY_SCOPE_AGENT);
        __hip_atomic_store((u64*)(pcnt + uw + 256 * r), tag, __ATOMIC_RELAXED, __HIP_MEMORY_SCOPE_AGENT);
      }
    }
    asm volatile(
    "s_nop 4\n"
    "v_mbcnt_lo_u32_b32 v255, -1, 0\n"
    "v_mbcnt_hi_u32_b32 v255, -1, v255\n"
    "v_lshlrev_b32 v242, 4, v255\n"
    "v_mov_b32 v248, %[tlo]\n"
    "v_mov_b32 v249, %[thi]\n"
    "s_mov_b32 vcc_lo, 0\n"
    "s_mov_b32 vcc_hi, -1\n"
    "v_bfe_u32 v241, v255, 0, 5\n"
    "v_lshlrev_b32 v241, 3, v241\n"
    "v_add_u32 v241, %[cw], v241\n"
    "v_bfe_u32 v253, v255, 0, 2\n"
    "v_lshlrev_b32 v253, 3, v253\n"
    "v_add_u32 v253, %[co], v253\n"
    "v_cndmask_b32 v241, v241, v253, vcc\n"
    "s_mov_b64 s[78:79], -1\n"
    "global_load_dwordx2 v[246:247], v241, %[pcnt] offset:0 sc1\n"
    "s_waitcnt vmcnt(0)\n"
    "s_mov_b32 s60, 0x40000\n"
    "L_poll0r%=:\n"
    "v_cmp_ne_u64 vcc, v[246:247], v[248:249]\n"
    "s_sub_u32 s60, s60, 1\n"
    "s_cbranch_scc1 L_poll0r%=_ok\n"
    "s_cbranch_vccz L_poll0r%=_ok\n"
    "s_sleep 8\n"
    "global_load_dwordx2 v[246:247], v241, %[pcnt] offset:0 sc1\n"
    "s_waitcnt vmcnt(0)\n"
    "s_branch L_poll0r%=\n"
    "L_poll0r%=_ok:\n"
    "s_barrier\n"
    "s_mov_b64 s[36:37], %[pa]\n"
    "s_mov_b32 s38, 196608\n"
    "s_mov_b32 s39, 0x20000\n"
    "s_mov_b64 s[40:41], %[pb]\n"
    "s_mov_b32 s42, 294912\n"
    "s_mov_b32 s43, 0x20000\n"
    "s_mov_b64 s[44:45], %[pc]\n"
    "s_mov_b32 s46, 221184\n"
    "s_mov_b32 s47, 0x20000\n"
    "s_bitcmp1_b32 %[wv], 0\n"
    "s_cselect_b32 s56, 8, 0\n"
    "s_cselect_b32 s57, 7, 1\n"
    "s_cselect_b32 s58, 6, 2\n"
    "s_cselect_b32 s59, 5, 3\n"
    "s_cselect_b32 s60, 4, 4\n"
    "s_cselect_b32 s61, 3, 5\n"
    "s_cselect_b32 s62, 2, 6\n"
    "s_cselect_b32 s63, 1, 7\n"
    "s_cselect_b32 s64, 0, 8\n"
    "s_lshl_b32 s56, s56, 10\n"
    "s_lshl_b32 s57, s57, 10\n"
    "s_lshl_b32 s58, s58, 10\n"
    "s_lshl_b32 s59, s59, 10\n"
    "s_lshl_b32 s60, s60, 10\n"
    "s_lshl_b32 s61, s61, 10\n"
    "s_lshl_b32 s62, s62, 10\n"
    "s_lshl_b32 s63, s63, 10\n"
    "s_lshl_b32 s64, s64, 10\n"
    "s_bitcmp1_b32 %[wv], 1\n"
    "s_cselect_b32 s65, 0xc00, 0\n"
    "s_cselect_b32 s66, 0, 0xc00\n"
    "s_cselect_b32 s67, 110592, 0\n"
    "s_mov_b32 s69, 3\n"
    "s_mov_b32 s68, 0\n"
    "v_mov_b32 v244, v242\n"
    "v_mov_b32 v245, v242\n"
    "buffer_load_dwordx4 v[0:3], v244, s[36:39], s65 offen sc1\n"
    "buffer_load_dwordx4 v[4:7], v244, s[36:39], s65 offen offset:1024 sc1\n"
    "buffer_load_dwordx4 v[8:11], v244, s[36:39], s65 offen offset:2048 sc1\n"
    "buffer_load_dwordx4 v[12:15], v244, s[36:39], s66 offen sc1\n"
    "buffer_load_dwordx4 v[16:19], v244, s[36:39], s66 offen offset:1024 sc1\n"
    "buffer_load_dwordx4 v[20:23], v244, s[36:39], s66 offen offset:2048 sc1\n"
    "v_add_u32 v244, 0x1800, v244\n"
    "buffer_load_dwordx4 v[24:27], v245, s[40:43], s56 offen sc1\n"
    "buffer_load_dwordx4 v[28:31], v245, s[40:43], s57 offen sc1\n"
    "buffer_load_dwordx4 v[32:35], v245, s[40:43], s58 offen sc1\n"
    "buffer_load_dwordx4 v[36:39], v245, s[40:43], s59 offen sc1\n"
    "buffer_load_dwordx4 v[40:43], v245, s[40:43], s60 offen sc1\n"
    "buffer_load_dwordx4 v[44:47], v245, s[40:43], s61 offen sc1\n"
    "buffer_load_dwordx4 v[48:51], v245, s[40:43], s62 offen sc1\n"
    "buffer_load_dwordx4 v[52:55], v245, s[40:43], s63 offen sc1\n"
    "buffer_load_dwordx4 v[56:59], v245, s[40:43], s64 offen sc1\n"
    "v_add_u32 v245, 0x2400, v245\n"
    "buffer_load_dwordx4 v[60:63], v244, s[36:39], s65 offen sc1\n"
    "buffer_load_dwordx4 v[64:67], v244, s[36:39], s65 offen offset:1024 sc1\n"
    "buffer_load_dwordx4 v[68:71], v244, s[36:39], s65 offen offset:2048 sc1\n"
    "buffer_load_dwordx4 v[72:75], v244, s[36:39], s66 offen sc1\n"
    "buffer_load_dwordx4 v[76:79], v244, s[36:39], s66 offen offset:1024 sc1\n"
    "buffer_load_dwordx4 v[80:83], v244, s[36:39], s66 offen offset:2048 sc1\n"
    "v_add_u32 v244, 0x1800, v244\n"
    "buffer_load_dwordx4 v[84:87], v245, s[40:43], s56 offen sc1\n"
    "buffer_load_dwordx4 v[88:91], v245, s[40:43], s57 offen sc1\n"
    "buffer_load_dwordx4 v[92:95], v245, s[40:43], s58 offen sc1\n"
    "buffer_load_dwordx4 v[96:99], v245, s[40:43], s59 offen sc1\n"
    "buffer_load_dwordx4 v[100:103], v245, s[40:43], s60 offen sc1\n"
    "buffer_load_dwordx4 v[104:107], v245, s[40:43], s61 offen sc1\n"
    "buffer_load_dwordx4 v[108:111], v245, s[40:43], s62 offen sc1\n"
    "buffer_load_dwordx4 v[112:115], v245, s[40:43], s63 offen sc1\n"
    "buffer_load_dwordx4 v[116:119], v245, s[40:43], s64 offen sc1\n"
    "v_add_u32 v245, 0x2400, v245\n"
    "buffer_load_dwordx4 v[120:123], v244, s[36:39], s65 offen sc1\n"
    "buffer_load_dwordx4 v[124:127], v244, s[36:39], s65 offen offset:1024 sc1\n"
    "buffer_load_dwordx4 v[128:131], v244, s[36:39], s65 offen offset:2048 sc1\n"
    "buffer_load_dwordx4 v[132:135], v244, s[36:39], s66 offen sc1\n"
    "buffer_load_dwordx4 v[136:139], v244, s[36:39], s66 offen offset:1024 sc1\n"
    "buffer_load_dwordx4 v[140:143], v244, s[36:39], s66 offen offset:2048 sc1\n"
    "v_add_u32 v244, 0x1800, v244\n"
    "buffer_load_dwordx4 v[144:147], v245, s[40:43], s56 offen sc1\n"
    "buffer_load_dwordx4 v[148:151], v245, s[40:43], s57 offen sc1\n"
    "buffer_load_dwordx4 v[152:155], v245, s[40:43], s58 offen sc1\n"
    "buffer_load_dwordx4 v[156:159], v245, s[40:43], s59 offen sc1\n"
    "buffer_load_dwordx4 v[160:163], v245, s[40:43], s60 offen sc1\n"
    "buffer_load_dwordx4 v[164:167], v245, s[40:43], s61 offen sc1\n"
    "buffer_load_dwordx4 v[168:171], v245, s[40:43], s62 offen sc1\n"
    "buffer_load_dwordx4 v[172:175], v245, s[40:43], s63 offen sc1\n"
    "buffer_load_dwordx4 v[176:179], v245, s[40:43], s64 offen sc1\n"
    "v_add_u32 v245, 0x2400, v245\n"
    "v_accvgpr_write_b32 a0, 0\n"
    "v_accvgpr_write_b32 a1, 0\n"
    "v_accvgpr_write_b32 a2, 0\n"
    "v_accvgpr_write_b32 a3, 0\n"
    "v_accvgpr_write_b32 a4, 0\n"
    "v_accvgpr_write_b32 a5, 0\n"
    "v_accvgpr_write_b32 a6, 0\n"
    "v_accvgpr_write_b32 a7, 0\n"
    "v_accvgpr_write_b32 a8, 0\n"
    "v_accvgpr_write_b32 a9, 0\n"
    "v_accvgpr_write_b32 a10, 0\n"
    "v_accvgpr_write_b32 a11, 0\n"
    "v_accvgpr_write_b32 a12, 0\n"
    "v_accvgpr_write_b32 a13, 0\n"
    "v_accvgpr_write_b32 a14, 0\n"
    "v_accvgpr_write_b32 a15, 0\n"
    "v_accvgpr_write_b32 a16, 0\n"
    "v_accvgpr_write_b32 a17, 0\n"
    "v_accvgpr_write_b32 a18, 0\n"
    "v_accvgpr_write_b32 a19, 0\n"
    "v_accvgpr_write_b32 a20, 0\n"
    "v_accvgpr_write_b32 a21, 0\n"
    "v_accvgpr_write_b32 a22, 0\n"
    "v_accvgpr_write_b32 a23, 0\n"
    "v_accvgpr_write_b32 a24, 0\n"
    "v_accvgpr_write_b32 a25, 0\n"
    "v_accvgpr_write_b32 a26, 0\n"
    "v_accvgpr_write_b32 a27, 0\n"
    "v_accvgpr_write_b32 a28, 0\n"
    "v_accvgpr_write_b32 a29, 0\n"
    "v_accvgpr_write_b32 a30, 0\n"
    "v_accvgpr_write_b32 a31, 0\n"
    "v_accvgpr_write_b32 a32, 0\n"
    "v_accvgpr_write_b32 a33, 0\n"
    "v_accvgpr_write_b32 a34, 0\n"
    "v_accvgpr_write_b32 a35, 0\n"
    "v_accvgpr_write_b32 a36, 0\n"
    "v_accvgpr_write_b32 a37, 0\n"
    "v_accvgpr_write_b32 a38, 0\n"
    "v_accvgpr_write_b32 a39, 0\n"
    "v_accvgpr_write_b32 a40, 0\n"
    "v_accvgpr_write_b32 a41, 0\n"
    "v_accvgpr_write_b32 a42, 0\n"
    "v_accvgpr_write_b32 a43, 0\n"
    "v_accvgpr_write_b32 a44, 0\n"
    "v_accvgpr_write_b32 a45, 0\n"
    "v_accvgpr_write_b32 a46, 0\n"
    "v_accvgpr_write_b32 a47, 0\n"
    "v_accvgpr_write_b32 a48, 0\n"
    "v_accvgpr_write_b32 a49, 0\n"
    "v_accvgpr_write_b32 a50, 0\n"
    "v_accvgpr_write_b32 a51, 0\n"
    "v_accvgpr_write_b32 a52, 0\n"
    "v_accvgpr_write_b32 a53, 0\n"
    "v_accvgpr_write_b32 a54, 0\n"
    "v_accvgpr_write_b32 a55, 0\n"
    "v_accvgpr_write_b32 a56, 0\n"
    "v_accvgpr_write_b32 a57, 0\n"
    "v_accvgpr_write_b32 a58, 0\n"
    "v_accvgpr_write_b32 a59, 0\n"
    "v_accvgpr_write_b32 a60, 0\n"
    "v_accvgpr_write_b32 a61, 0\n"
    "v_accvgpr_write_b32 a62, 0\n"
    "v_accvgpr_write_b32 a63, 0\n"
    "v_accvgpr_write_b32 a64, 0\n"
    "v_accvgpr_write_b32 a65, 0\n"
    "v_accvgpr_write_b32 a66, 0\n"
    "v_accvgpr_write_b32 a67, 0\n"
    "v_accvgpr_write_b32 a68, 0\n"
    "v_accvgpr_write_b32 a69, 0\n"
    "v_accvgpr_write_b32 a70, 0\n"
    "v_accvgpr_write_b32 a71, 0\n"
    "v_accvgpr_write_b32 a72, 0\n"
    "v_accvgpr_write_b32 a73, 0\n"
    "v_accvgpr_write_b32 a74, 0\n"
    "v_accvgpr_write_b32 a75, 0\n"
    "v_accvgpr_write_b32 a76, 0\n"
    "v_accvgpr_write_b32 a77, 0\n"
    "v_accvgpr_write_b32 a78, 0\n"
    "v_accvgpr_write_b32 a79, 0\n"
    "v_accvgpr_write_b32 a80, 0\n"
    "v_accvgpr_write_b32 a81, 0\n"
    "v_accvgpr_write_b32 a82, 0\n"
    "v_accvgpr_write_b32 a83, 0\n"
    "v_accvgpr_write_b32 a84, 0\n"
    "v_accvgpr_write_b32 a85, 0\n"
    "v_accvgpr_write_b32 a86, 0\n"
    "v_accvgpr_write_b32 a87, 0\n"
    "v_accvgpr_write_b32 a88, 0\n"
    "v_accvgpr_write_b32 a89, 0\n"
    "v_accvgpr_write_b32 a90, 0\n"
    "v_accvgpr_write_b32 a91, 0\n"
    "v_accvgpr_write_b32 a92, 0\n"
    "v_accvgpr_write_b32 a93, 0\n"
    "v_accvgpr_write_b32 a94, 0\n"
    "v_accvgpr_write_b32 a95, 0\n"
    "v_accvgpr_write_b32 a96, 0\n"
    "v_accvgpr_write_b32 a97, 0\n"
    "v_accvgpr_write_b32 a98, 0\n"
    "v_accvgpr_write_b32 a99, 0\n"
    "v_accvgpr_write_b32 a100, 0\n"
    "v_accvgpr_write_b32 a101, 0\n"
    "v_accvgpr_write_b32 a102, 0\n"
    "v_accvgpr_write_b32 a103, 0\n"
    "v_accvgpr_write_b32 a104, 0\n"
    "v_accvgpr_write_b32 a105, 0\n"
    "v_accvgpr_write_b32 a106, 0\n"
    "v_accvgpr_write_b32 a107, 0\n"
    "v_accvgpr_write_b32 a108, 0\n"
    "v_accvgpr_write_b32 a109, 0\n"
    "v_accvgpr_write_b32 a110, 0\n"
    "v_accvgpr_write_b32 a111, 0\n"
    "v_accvgpr_write_b32 a112, 0\n"
    "v_accvgpr_write_b32 a113, 0\n"
    "v_accvgpr_write_b32 a114, 0\n"
    "v_accvgpr_write_b32 a115, 0\n"
    "v_accvgpr_write_b32 a116, 0\n"
    "v_accvgpr_write_b32 a117, 0\n"
    "v_accvgpr_write_b32 a118, 0\n"
    "v_accvgpr_write_b32 a119, 0\n"
    "v_accvgpr_write_b32 a120, 0\n"
    "v_accvgpr_write_b32 a121, 0\n"
    "v_accvgpr_write_b32 a122, 0\n"
    "v_accvgpr_write_b32 a123, 0\n"
    "v_accvgpr_write_b32 a124, 0\n"
    "v_accvgpr_write_b32 a125, 0\n"
    "v_accvgpr_write_b32 a126, 0\n"
    "v_accvgpr_write_b32 a127, 0\n"
    "v_accvgpr_write_b32 a128, 0\n"
    "v_accvgpr_write_b32 a129, 0\n"
    "v_accvgpr_write_b32 a130, 0\n"
    "v_accvgpr_write_b32 a131, 0\n"
    "v_accvgpr_write_b32 a132, 0\n"
    "v_accvgpr_write_b32 a133, 0\n"
    "v_accvgpr_write_b32 a134, 0\n"
    "v_accvgpr_write_b32 a135, 0\n"
    "v_accvgpr_write_b32 a136, 0\n"
    "v_accvgpr_write_b32 a137, 0\n"
    "v_accvgpr_write_b32 a138, 0\n"
    "v_accvgpr_write_b32 a139, 0\n"
    "v_accvgpr_write_b32 a140, 0\n"
    "v_accvgpr_write_b32 a141, 0\n"
    "v_accvgpr_write_b32 a142, 0\n"
    "v_accvgpr_write_b32 a143, 0\n"
    "v_accvgpr_write_b32 a144, 0\n"
    "v_accvgpr_write_b32 a145, 0\n"
    "v_accvgpr_write_b32 a146, 0\n"
    "v_accvgpr_write_b32 a147, 0\n"
    "v_accvgpr_write_b32 a148, 0\n"
    "v_accvgpr_write_b32 a149, 0\n"
    "v_accvgpr_write_b32 a150, 0\n"
    "v_accvgpr_write_b32 a151, 0\n"
    "v_accvgpr_write_b32 a152, 0\n"
    "v_accvgpr_write_b32 a153, 0\n"
    "v_accvgpr_write_b32 a154, 0\n"
    "v_accvgpr_write_b32 a155, 0\n"
    "v_accvgpr_write_b32 a156, 0\n"
    "v_accvgpr_write_b32 a157, 0\n"
    "v_accvgpr_write_b32 a158, 0\n"
    "v_accvgpr_write_b32 a159, 0\n"
    "v_accvgpr_write_b32 a160, 0\n"
    "v_accvgpr_write_b32 a161, 0\n"
    "v_accvgpr_write_b32 a162, 0\n"
    "v_accvgpr_write_b32 a163, 0\n"
    "v_accvgpr_write_b32 a164, 0\n"
    "v_accvgpr_write_b32 a165, 0\n"
    "v_accvgpr_write_b32 a166, 0\n"
    "v_accvgpr_write_b32 a167, 0\n"
    "v_accvgpr_write_b32 a168, 0\n"
    "v_accvgpr_write_b32 a169, 0\n"
    "v_accvgpr_write_b32 a170, 0\n"
    "v_accvgpr_write_b32 a171, 0\n"
    "v_accvgpr_write_b32 a172, 0\n"
    "v_accvgpr_write_b32 a173, 0\n"
    "v_accvgpr_write_b32 a174, 0\n"
    "v_accvgpr_write_b32 a175, 0\n"
    "v_accvgpr_write_b32 a176, 0\n"
    "v_accvgpr_write_b32 a177, 0\n"
    "v_accvgpr_write_b32 a178, 0\n"
    "v_accvgpr_write_b32 a179, 0\n"
    "v_accvgpr_write_b32 a180, 0\n"
    "v_accvgpr_write_b32 a181, 0\n"
    "v_accvgpr_write_b32 a182, 0\n"
    "v_accvgpr_write_b32 a183, 0\n"
    "v_accvgpr_write_b32 a184, 0\n"
    "v_accvgpr_write_b32 a185, 0\n"
    "v_accvgpr_write_b32 a186, 0\n"
    "v_accvgpr_write_b32 a187, 0\n"
    "v_accvgpr_write_b32 a188, 0\n"
    "v_accvgpr_write_b32 a189, 0\n"
    "v_accvgpr_write_b32 a190, 0\n"
    "v_accvgpr_write_b32 a191, 0\n"
    "v_accvgpr_write_b32 a192, 0\n"
    "v_accvgpr_write_b32 a193, 0\n"
    "v_accvgpr_write_b32 a194, 0\n"
    "v_accvgpr_write_b32 a195, 0\n"
    "v_accvgpr_write_b32 a196, 0\n"
    "v_accvgpr_write_b32 a197, 0\n"
    "v_accvgpr_write_b32 a198, 0\n"
    "v_accvgpr_write_b32 a199, 0\n"
    "v_accvgpr_write_b32 a200, 0\n"
    "v_accvgpr_write_b32 a201, 0\n"
    "v_accvgpr_write_b32 a202, 0\n"
    "v_accvgpr_write_b32 a203, 0\n"
    "v_accvgpr_write_b32 a204, 0\n"
    "v_accvgpr_write_b32 a205, 0\n"
    "v_accvgpr_write_b32 a206, 0\n"
    "v_accvgpr_write_b32 a207, 0\n"
    "v_accvgpr_write_b32 a208, 0\n"
    "v_accvgpr_write_b32 a209, 0\n"
    "v_accvgpr_write_b32 a210, 0\n"
    "v_accvgpr_write_b32 a211, 0\n"
    "v_accvgpr_write_b32 a212, 0\n"
    "v_accvgpr_write_b32 a213, 0\n"
    "v_accvgpr_write_b32 a214, 0\n"
    "v_accvgpr_write_b32 a215, 0\n"
    "s_waitcnt vmcnt(0)\n"
    "L_op%=:\n"
    "buffer_load_dwordx4 v[204:207], v245, s[40:43], s56 offen sc1\n"
    "buffer_load_dwordx4 v[208:211], v245, s[40:43], s57 offen sc1\n"
    "buffer_load_dwordx4 v[212:215], v245, s[40:43], s58 offen sc1\n"
    "buffer_load_dwordx4 v[216:219], v245, s[40:43], s59 offen sc1\n"
    "buffer_load_dwordx4 v[220:223], v245, s[40:43], s60 offen sc1\n"
    "buffer_load_dwordx4 v[224:227], v245, s[40:43], s61 offen sc1\n"
    "buffer_load_dwordx4 v[228:231], v245, s[40:43], s62 offen sc1\n"
    "buffer_load_dwordx4 v[232:235], v245, s[40:43], s63 offen sc1\n"
    "buffer_load_dwordx4 v[236:239], v245, s[40:43], s64 offen sc1\n"
    "v_add_u32 v245, 0x2400, v245\n"
    "s_mul_i32 s70, %[wv], 27648\n"
    "v_add_u32 v250, s70, v242\n"
    "s_xor_b32 s71, %[wv], 2\n"
    "s_mul_i32 s71, s71, 27648\n"
    "v_add_u32 v251, s71, v242\n"
    "s_and_b32 s71, %[wv], 1\n"
    "s_lshl_b32 s71, s71, 7\n"
    "v_mul_u32_u24 v243, 26, v255\n"
    "v_lshrrev_b32 v243, 8, v243\n"
    "v_mul_u32_u24 v253, 2144, v243\n"
    "v_add3_u32 v253, v253, v242, s71\n"
    "v_cmp_gt_u32 vcc, 60, v255\n"
    "v_bfrev_b32 v243, 2\n"
    "s_nop 0\n"
    "v_cndmask_b32 v253, v243, v253, vcc\n"
    "s_waitcnt vmcnt(63)\n"
    "L_oloop%=:\n"
    "v_mfma_f32_16x16x32_bf16 a[0:3], v[24:27], v[0:3], a[0:3]\n"
    "v_mfma_f32_16x16x32_bf16 a[36:39], v[24:27], v[4:7], a[36:39]\n"
    "v_mfma_f32_16x16x32_bf16 a[72:75], v[24:27], v[8:11], a[72:75]\n"
    "buffer_load_dwordx4 v[180:183], v244, s[36:39], s65 offen sc1\n"
    "v_mfma_f32_16x16x32_bf16 a[108:111], v[24:27], v[12:15], a[108:111]\n"
    "v_mfma_f32_16x16x32_bf16 a[144:147], v[24:27], v[16:19], a[144:147]\n"
    "v_mfma_f32_16x16x32_bf16 a[180:183], v[24:27], v[20:23], a[180:183]\n"
    "buffer_load_dwordx4 v[24:27], v245, s[40:43], s56 offen sc1\n"
    "v_mfma_f32_16x16x32_bf16 a[4:7], v[28:31], v[0:3], a[4:7]\n"
    "v_mfma_f32_16x16x32_bf16 a[40:43], v[28:31], v[4:7], a[40:43]\n"
    "v_mfma_f32_16x16x32_bf16 a[76:79], v[28:31], v[8:11], a[76:79]\n"
    "buffer_load_dwordx4 v[184:187], v244, s[36:39], s65 offen offset:1024 sc1\n"
    "v_mfma_f32_16x16x32_bf16 a[112:115], v[28:31], v[12:15], a[112:115]\n"
    "v_mfma_f32_16x16x32_bf16 a[148:151], v[28:31], v[16:19], a[148:151]\n"
    "v_mfma_f32_16x16x32_bf16 a[184:187], v[28:31], v[20:23], a[184:187]\n"
    "buffer_load_dwordx4 v[28:31], v245, s[40:43], s57 offen sc1\n"
    "v_mfma_f32_16x16x32_bf16 a[8:11], v[32:35], v[0:3], a[8:11]\n"
    "v_mfma_f32_16x16x32_bf16 a[44:47], v[32:35], v[4:7], a[44:47]\n"
    "v_mfma_f32_16x16x32_bf16 a[80:83], v[32:35], v[8:11], a[80:83]\n"
    "buffer_load_dwordx4 v[188:191], v244, s[36:39], s65 offen offset:2048 sc1\n"
    "v_mfma_f32_16x16x32_bf16 a[116:119], v[32:35], v[12:15], a[116:119]\n"
    "v_mfma_f32_16x16x32_bf16 a[152:155], v[32:35], v[16:19], a[152:155]\n"
    "v_mfma_f32_16x16x32_bf16 a[188:191], v[32:35], v[20:23], a[188:191]\n"
    "buffer_load_dwordx4 v[32:35], v245, s[40:43], s58 offen sc1\n"
    "v_mfma_f32_16x16x32_bf16 a[12:15], v[36:39], v[0:3], a[12:15]\n"
    "v_mfma_f32_16x16x32_bf16 a[48:51], v[36:39], v[4:7], a[48:51]\n"
    "v_mfma_f32_16x16x32_bf16 a[84:87], v[36:39], v[8:11], a[84:87]\n"
    "buffer_load_dwordx4 v[192:195], v244, s[36:39], s66 offen sc1\n"
    "v_mfma_f32_16x16x32_bf16 a[120:123], v[36:39], v[12:15], a[120:123]\n"
    "v_mfma_f32_16x16x32_bf16 a[156:159], v[36:39], v[16:19], a[156:159]\n"
    "v_mfma_f32_16x16x32_bf16 a[192:195], v[36:39], v[20:23], a[192:195]\n"
    "buffer_load_dwordx4 v[36:39], v245, s[40:43], s59 offen sc1\n"
    "v_mfma_f32_16x16x32_bf16 a[16:19], v[40:43], v[0:3], a[16:19]\n"
    "v_mfma_f32_16x16x32_bf16 a[52:55], v[40:43], v[4:7], a[52:55]\n"
    "v_mfma_f32_16x16x32_bf16 a[88:91], v[40:43], v[8:11], a[88:91]\n"
    "buffer_load_dwordx4 v[196:199], v244, s[36:39], s66 offen offset:1024 sc1\n"
    "v_mfma_f32_16x16x32_bf16 a[124:127], v[40:43], v[12:15], a[124:127]\n"
    "v_mfma_f32_16x16x32_bf16 a[160:163], v[40:43], v[16:19], a[160:163]\n"
    "v_mfma_f32_16x16x32_bf16 a[196:199], v[40:43], v[20:23], a[196:199]\n"
    "buffer_load_dwordx4 v[40:43], v245, s[40:43], s60 offen sc1\n"
    "v_mfma_f32_16x16x32_bf16 a[20:23], v[44:47], v[0:3], a[20:23]\n"
    "v_mfma_f32_16x16x32_bf16 a[56:59], v[44:47], v[4:7], a[56:59]\n"
    "v_mfma_f32_16x16x32_bf16 a[92:95], v[44:47], v[8:11], a[92:95]\n"
    "buffer_load_dwordx4 v[200:203], v244, s[36:39], s66 offen offset:2048 sc1\n"
    "v_mfma_f32_16x16x32_bf16 a[128:131], v[44:47], v[12:15], a[128:131]\n"
    "v_mfma_f32_16x16x32_bf16 a[164:167], v[44:47], v[16:19], a[164:167]\n"
    "v_add_u32 v244, 0x1800, v244\n"
    "v_mfma_f32_16x16x32_bf16 a[200:203], v[44:47], v[20:23], a[200:203]\n"
    "buffer_load_dwordx4 v[44:47], v245, s[40:43], s61 offen sc1\n"
    "v_mfma_f32_16x16x32_bf16 a[24:27], v[48:51], v[0:3], a[24:27]\n"
    "v_mfma_f32_16x16x32_bf16 a[60:63], v[48:51], v[4:7], a[60:63]\n"
    "v_mfma_f32_16x16x32_bf16 a[96:99], v[48:51], v[8:11], a[96:99]\n"
    "v_mfma_f32_16x16x32_bf16 a[132:135], v[48:51], v[12:15], a[132:135]\n"
    "v_mfma_f32_16x16x32_bf16 a[168:171], v[48:51], v[16:19], a[168:171]\n"
    "v_mfma_f32_16x16x32_bf16 a[204:207], v[48:51], v[20:23], a[204:207]\n"
    "buffer_load_dwordx4 v[48:51], v245, s[40:43], s62 offen sc1\n"
    "v_mfma_f32_16x16x32_bf16 a[28:31], v[52:55], v[0:3], a[28:31]\n"
    "v_mfma_f32_16x16x32_bf16 a[64:67], v[52:55], v[4:7], a[64:67]\n"
    "v_mfma_f32_16x16x32_bf16 a[100:103], v[52:55], v[8:11], a[100:103]\n"
    "v_mfma_f32_16x16x32_bf16 a[136:139], v[52:55], v[12:15], a[136:139]\n"
    "v_mfma_f32_16x16x32_bf16 a[172:175], v[52:55], v[16:19], a[172:175]\n"
    "v_mfma_f32_16x16x32_bf16 a[208:211], v[52:55], v[20:23], a[208:211]\n"
    "buffer_load_dwordx4 v[52:55], v245, s[40:43], s63 offen sc1\n"
    "v_mfma_f32_16x16x32_bf16 a[32:35], v[56:59], v[0:3], a[32:35]\n"
    "v_mfma_f32_16x16x32_bf16 a[68:71], v[56:59], v[4:7], a[68:71]\n"
    "v_mfma_f32_16x16x32_bf16 a[104:107], v[56:59], v[8:11], a[104:107]\n"
    "v_mfma_f32_16x16x32_bf16 a[140:143], v[56:59], v[12:15], a[140:143]\n"
    "v_mfma_f32_16x16x32_bf16 a[176:179], v[56:59], v[16:19], a[176:179]\n"
    "s_waitcnt vmcnt(33)\n"
    "v_mfma_f32_16x16x32_bf16 a[212:215], v[56:59], v[20:23], a[212:215]\n"
    "buffer_load_dwordx4 v[56:59], v245, s[40:43], s64 offen sc1\n"
    "v_add_u32 v245, 0x2400, v245\n"
    "v_mfma_f32_16x16x32_bf16 a[0:3], v[84:87], v[60:63], a[0:3]\n"
    "global_load_dwordx2 v[246:247], v241, %[pcnt] offset:256 sc1\n"
    "v_mfma_f32_16x16x32_bf16 a[36:39], v[84:87], v[64:67], a[36:39]\n"
    "v_mfma_f32_16x16x32_bf16 a[72:75], v[84:87], v[68:71], a[72:75]\n"
    "buffer_load_dwordx4 v[0:3], v244, s[36:39], s65 offen sc1\n"
    "v_mfma_f32_16x16x32_bf16 a[108:111], v[84:87], v[72:75], a[108:111]\n"
    "v_mfma_f32_16x16x32_bf16 a[144:147], v[84:87], v[76:79], a[144:147]\n"
    "v_mfma_f32_16x16x32_bf16 a[180:183], v[84:87], v[80:83], a[180:183]\n"
    "buffer_load_dwordx4 v[84:87], v245, s[40:43], s56 offen sc1\n"
    "v_mfma_f32_16x16x32_bf16 a[4:7], v[88:91], v[60:63], a[4:7]\n"
    "v_mfma_f32_16x16x32_bf16 a[40:43], v[88:91], v[64:67], a[40:43]\n"
    "v_mfma_f32_16x16x32_bf16 a[76:79], v[88:91], v[68:71], a[76:79]\n"
    "buffer_load_dwordx4 v[4:7], v244, s[36:39], s65 offen offset:1024 sc1\n"
    "v_mfma_f32_16x16x32_bf16 a[112:115], v[88:91], v[72:75], a[112:115]\n"
    "v_mfma_f32_16x16x32_bf16 a[148:151], v[88:91], v[76:79], a[148:151]\n"
    "v_mfma_f32_16x16x32_bf16 a[184:187], v[88:91], v[80:83], a[184:187]\n"
    "buffer_load_dwordx4 v[88:91], v245, s[40:43], s57 offen sc1\n"
    "v_mfma_f32_16x16x32_bf16 a[8:11], v[92:95], v[60:63], a[8:11]\n"
    "v_mfma_f32_16x16x32_bf16 a[44:47], v[92:95], v[64:67], a[44:47]\n"
    "v_mfma_f32_16x16x32_bf16 a[80:83], v[92:95], v[68:71], a[80:83]\n"
    "buffer_load_dwordx4 v[8:11], v244, s[36:39], s65 offen offset:2048 sc1\n"
    "v_mfma_f32_16x16x32_bf16 a[116:119], v[92:95], v[72:75], a[116:119]\n"
    "v_mfma_f32_16x16x32_bf16 a[152:155], v[92:95], v[76:79], a[152:155]\n"
    "v_mfma_f32_16x16x32_bf16 a[188:191], v[92:95], v[80:83], a[188:191]\n"
    "buffer_load_dwordx4 v[92:95], v245, s[40:43], s58 offen sc1\n"
    "v_mfma_f32_16x16x32_bf16 a[12:15], v[96:99], v[60:63], a[12:15]\n"
    "v_mfma_f32_16x16x32_bf16 a[48:51], v[96:99], v[64:67], a[48:51]\n"
    "v_mfma_f32_16x16x32_bf16 a[84:87], v[96:99], v[68:71], a[84:87]\n"
    "buffer_load_dwordx4 v[12:15], v244, s[36:39], s66 offen sc1\n"
    "v_mfma_f32_16x16x32_bf16 a[120:123], v[96:99], v[72:75], a[120:123]\n"
    "v_mfma_f32_16x16x32_bf16 a[156:159], v[96:99], v[76:79], a[156:159]\n"
    "v_mfma_f32_16x16x32_bf16 a[192:195], v[96:99], v[80:83], a[192:195]\n"
    "buffer_load_dwordx4 v[96:99], v245, s[40:43], s59 offen sc1\n"
    "v_mfma_f32_16x16x32_bf16 a[16:19], v[100:103], v[60:63], a[16:19]\n"
    "v_mfma_f32_16x16x32_bf16 a[52:55], v[100:103], v[64:67], a[52:55]\n"
    "v_mfma_f32_16x16x32_bf16 a[88:91], v[100:103], v[68:71], a[88:91]\n"
    "buffer_load_dwordx4 v[16:19], v244, s[36:39], s66 offen offset:1024 sc1\n"
    "v_mfma_f32_16x16x32_bf16 a[124:127], v[100:103], v[72:75], a[124:127]\n"
    "v_mfma_f32_16x16x32_bf16 a[160:163], v[100:103], v[76:79], a[160:163]\n"
    "v_mfma_f32_16x16x32_bf16 a[196:199], v[100:103], v[80:83], a[196:199]\n"
    "buffer_load_dwordx4 v[100:103], v245, s[40:43], s60 offen sc1\n"
    "v_mfma_f32_16x16x32_bf16 a[20:23], v[104:107], v[60:63], a[20:23]\n"
    "v_mfma_f32_16x16x32_bf16 a[56:59], v[104:107], v[64:67], a[56:59]\n"
    "v_mfma_f32_16x16x32_bf16 a[92:95], v[104:107], v[68:71], a[92:95]\n"
    "buffer_load_dwordx4 v[20:23], v244, s[36:39], s66 offen offset:2048 sc1\n"
    "v_mfma_f32_16x16x32_bf16 a[128:131], v[104:107], v[72:75], a[128:131]\n"
    "v_mfma_f32_16x16x32_bf16 a[164:167], v[104:107], v[76:79], a[164:167]\n"
    "v_add_u32 v244, 0x1800, v244\n"
    "v_mfma_f32_16x16x32_bf16 a[200:203], v[104:107], v[80:83], a[200:203]\n"
    "buffer_load_dwordx4 v[104:107], v245, s[40:43], s61 offen sc1\n"
    "v_mfma_f32_16x16x32_bf16 a[24:27], v[108:111], v[60:63], a[24:27]\n"
    "v_mfma_f32_16x16x32_bf16 a[60:63], v[108:111], v[64:67], a[60:63]\n"
    "v_mfma_f32_16x16x32_bf16 a[96:99], v[108:111], v[68:71], a[96:99]\n"
    "v_mfma_f32_16x16x32_bf16 a[132:135], v[108:111], v[72:75], a[132:135]\n"
    "v_mfma_f32_16x16x32_bf16 a[168:171], v[108:111], v[76:79], a[168:171]\n"
    "v_mfma_f32_16x16x32_bf16 a[204:207], v[108:111], v[80:83], a[204:207]\n"
    "buffer_load_dwordx4 v[108:111], v245, s[40:43], s62 offen sc1\n"
    "v_mfma_f32_16x16x32_bf16 a[28:31], v[112:115], v[60:63], a[28:31]\n"
    "v_mfma_f32_16x16x32_bf16 a[64:67], v[112:115], v[64:67], a[64:67]\n"
    "v_mfma_f32_16x16x32_bf16 a[100:103], v[112:115], v[68:71], a[100:103]\n"
    "v_mfma_f32_16x16x32_bf16 a[136:139], v[112:115], v[72:75], a[136:139]\n"
    "v_mfma_f32_16x16x32_bf16 a[172:175], v[112:115], v[76:79], a[172:175]\n"
    "s_sub_u32 s69, s69, 1\n"
    "v_mfma_f32_16x16x32_bf16 a[208:211], v[112:115], v[80:83], a[208:211]\n"
    "buffer_load_dwordx4 v[112:115], v245, s[40:43], s63 offen sc1\n"
    "v_mfma_f32_16x16x32_bf16 a[32:35], v[116:119], v[60:63], a[32:35]\n"
    "v_mfma_f32_16x16x32_bf16 a[68:71], v[116:119], v[64:67], a[68:71]\n"
    "v_mfma_f32_16x16x32_bf16 a[104:107], v[116:119], v[68:71], a[104:107]\n"
    "v_mfma_f32_16x16x32_bf16 a[140:143], v[116:119], v[72:75], a[140:143]\n"
    "v_mfma_f32_16x16x32_bf16 a[176:179], v[116:119], v[76:79], a[176:179]\n"
    "s_waitcnt vmcnt(34)\n"
    "v_mfma_f32_16x16x32_bf16 a[212:215], v[116:119], v[80:83], a[212:215]\n"
    "buffer_load_dwordx4 v[116:119], v245, s[40:43], s64 offen sc1\n"
    "v_add_u32 v245, 0x2400, v245\n"
    "v_mfma_f32_16x16x32_bf16 a[0:3], v[144:147], v[120:123], a[0:3]\n"
    "v_mfma_f32_16x16x32_bf16 a[36:39], v[144:147], v[124:127], a[36:39]\n"
    "v_mfma_f32_16x16x32_bf16 a[72:75], v[144:147], v[128:131], a[72:75]\n"
    "buffer_load_dwordx4 v[60:63], v244, s[36:39], s65 offen sc1\n"
    "v_mfma_f32_16x16x32_bf16 a[108:111], v[144:147], v[132:135], a[108:111]\n"
    "v_mfma_f32_16x16x32_bf16 a[144:147], v[144:147], v[136:139], a[144:147]\n"
    "v_mfma_f32_16x16x32_bf16 a[180:183], v[144:147], v[140:143], a[180:183]\n"
    "buffer_load_dwordx4 v[144:147], v245, s[40:43], s56 offen sc1\n"
    "v_mfma_f32_16x16x32_bf16 a[4:7], v[148:151], v[120:123], a[4:7]\n"
    "v_mfma_f32_16x16x32_bf16 a[40:43], v[148:151], v[124:127], a[40:43]\n"
    "v_mfma_f32_16x16x32_bf16 a[76:79], v[148:151], v[128:131], a[76:79]\n"
    "buffer_load_dwordx4 v[64:67], v244, s[36:39], s65 offen offset:1024 sc1\n"
    "v_mfma_f32_16x16x32_bf16 a[112:115], v[148:151], v[132:135], a[112:115]\n"
    "v_mfma_f32_16x16x32_bf16 a[148:151], v[148:151], v[136:139], a[148:151]\n"
    "v_mfma_f32_16x16x32_bf16 a[184:187], v[148:151], v[140:143], a[184:187]\n"
    "buffer_load_dwordx4 v[148:151], v245, s[40:43], s57 offen sc1\n"
    "v_mfma_f32_16x16x32_bf16 a[8:11], v[152:155], v[120:123], a[8:11]\n"
    "v_mfma_f32_16x16x32_bf16 a[44:47], v[152:155], v[124:127], a[44:47]\n"
    "v_mfma_f32_16x16x32_bf16 a[80:83], v[152:155], v[128:131], a[80:83]\n"
    "buffer_load_dwordx4 v[68:71], v244, s[36:39], s65 offen offset:2048 sc1\n"
    "v_mfma_f32_16x16x32_bf16 a[116:119], v[152:155], v[132:135], a[116:119]\n"
    "v_mfma_f32_16x16x32_bf16 a[152:155], v[152:155], v[136:139], a[152:155]\n"
    "v_mfma_f32_16x16x32_bf16 a[188:191], v[152:155], v[140:143], a[188:191]\n"
    "buffer_load_dwordx4 v[152:155], v245, s[40:43], s58 offen sc1\n"
    "v_mfma_f32_16x16x32_bf16 a[12:15], v[156:159], v[120:123], a[12:15]\n"
    "v_mfma_f32_16x16x32_bf16 a[48:51], v[156:159], v[124:127], a[48:51]\n"
    "v_mfma_f32_16x16x32_bf16 a[84:87], v[156:159], v[128:131], a[84:87]\n"
    "buffer_load_dwordx4 v[72:75], v244, s[36:39], s66 offen sc1\n"
    "v_mfma_f32_16x16x32_bf16 a[120:123], v[156:159], v[132:135], a[120:123]\n"
    "v_mfma_f32_16x16x32_bf16 a[156:159], v[156:159], v[136:139], a[156:159]\n"
    "v_mfma_f32_16x16x32_bf16 a[192:195], v[156:159], v[140:143], a[192:195]\n"
    "buffer_load_dwordx4 v[156:159], v245, s[40:43], s59 offen sc1\n"
    "v_mfma_f32_16x16x32_bf16 a[16:19], v[160:163], v[120:123], a[16:19]\n"
    "v_mfma_f32_16x16x32_bf16 a[52:55], v[160:163], v[124:127], a[52:55]\n"
    "v_mfma_f32_16x16x32_bf16 a[88:91], v[160:163], v[128:131], a[88:91]\n"
    "buffer_load_dwordx4 v[76:79], v244, s[36:39], s66 offen offset:1024 sc1\n"
    "v_mfma_f32_16x16x32_bf16 a[124:127], v[160:163], v[132:135], a[124:127]\n"
    "v_mfma_f32_16x16x32_bf16 a[160:163], v[160:163], v[136:139], a[160:163]\n"
    "v_mfma_f32_16x16x32_bf16 a[196:199], v[160:163], v[140:143], a[196:199]\n"
    "buffer_load_dwordx4 v[160:163], v245, s[40:43], s60 offen sc1\n"
    "v_mfma_f32_16x16x32_bf16 a[20:23], v[164:167], v[120:123], a[20:23]\n"
    "v_mfma_f32_16x16x32_bf16 a[56:59], v[164:167], v[124:127], a[56:59]\n"
    "v_mfma_f32_16x16x32_bf16 a[92:95], v[164:167], v[128:131], a[92:95]\n"
    "buffer_load_dwordx4 v[80:83], v244, s[36:39], s66 offen offset:2048 sc1\n"
    "v_mfma_f32_16x16x32_bf16 a[128:131], v[164:167], v[132:135], a[128:131]\n"
    "v_mfma_f32_16x16x32_bf16 a[164:167], v[164:167], v[136:139], a[164:167]\n"
    "v_add_u32 v244, 0x1800, v244\n"
    "v_mfma_f32_16x16x32_bf16 a[200:203], v[164:167], v[140:143], a[200:203]\n"
    "buffer_load_dwordx4 v[164:167], v245, s[40:43], s61 offen sc1\n"
    "v_mfma_f32_16x16x32_bf16 a[24:27], v[168:171], v[120:123], a[24:27]\n"
    "v_mfma_f32_16x16x32_bf16 a[60:63], v[168:171], v[124:127], a[60:63]\n"
    "v_mfma_f32_16x16x32_bf16 a[96:99], v[168:171], v[128:131], a[96:99]\n"
    "v_mfma_f32_16x16x32_bf16 a[132:135], v[168:171], v[132:135], a[132:135]\n"
    "v_mfma_f32_16x16x32_bf16 a[168:171], v[168:171], v[136:139], a[168:171]\n"
    "v_mfma_f32_16x16x32_bf16 a[204:207], v[168:171], v[140:143], a[204:207]\n"
    "buffer_load_dwordx4 v[168:171], v245, s[40:43], s62 offen sc1\n"
    "v_mfma_f32_16x16x32_bf16 a[28:31], v[172:175], v[120:123], a[28:31]\n"
    "v_mfma_f32_16x16x32_bf16 a[64:67], v[172:175], v[124:127], a[64:67]\n"
    "v_mfma_f32_16x16x32_bf16 a[100:103], v[172:175], v[128:131], a[100:103]\n"
    "v_mfma_f32_16x16x32_bf16 a[136:139], v[172:175], v[132:135], a[136:139]\n"
    "v_mfma_f32_16x16x32_bf16 a[172:175], v[172:175], v[136:139], a[172:175]\n"
    "v_mfma_f32_16x16x32_bf16 a[208:211], v[172:175], v[140:143], a[208:211]\n"
    "buffer_load_dwordx4 v[172:175], v245, s[40:43], s63 offen sc1\n"
    "v_mfma_f32_16x16x32_bf16 a[32:35], v[176:179], v[120:123], a[32:35]\n"
    "v_mfma_f32_16x16x32_bf16 a[68:71], v[176:179], v[124:127], a[68:71]\n"
    "v_mfma_f32_16x16x32_bf16 a[104:107], v[176:179], v[128:131], a[104:107]\n"
    "v_mfma_f32_16x16x32_bf16 a[140:143], v[176:179], v[132:135], a[140:143]\n"
    "v_mfma_f32_16x16x32_bf16 a[176:179], v[176:179], v[136:139], a[176:179]\n"
    "s_waitcnt vmcnt(34)\n"
    "v_mfma_f32_16x16x32_bf16 a[212:215], v[176:179], v[140:143], a[212:215]\n"
    "buffer_load_dwordx4 v[176:179], v245, s[40:43], s64 offen sc1\n"
    "v_add_u32 v245, 0x2400, v245\n"
    "v_mfma_f32_16x16x32_bf16 a[0:3], v[204:207], v[180:183], a[0:3]\n"
    "v_mfma_f32_16x16x32_bf16 a[36:39], v[204:207], v[184:187], a[36:39]\n"
    "v_mfma_f32_16x16x32_bf16 a[72:75], v[204:207], v[188:191], a[72:75]\n"
    "buffer_load_dwordx4 v[120:123], v244, s[36:39], s65 offen sc1\n"
    "v_mfma_f32_16x16x32_bf16 a[108:111], v[204:207], v[192:195], a[108:111]\n"
    "v_mfma_f32_16x16x32_bf16 a[144:147], v[204:207], v[196:199], a[144:147]\n"
    "v_mfma_f32_16x16x32_bf16 a[180:183], v[204:207], v[200:203], a[180:183]\n"
    "buffer_load_dwordx4 v[204:207], v245, s[40:43], s56 offen sc1\n"
    "v_mfma_f32_16x16x32_bf16 a[4:7], v[208:211], v[180:183], a[4:7]\n"
    "v_mfma_f32_16x16x32_bf16 a[40:43], v[208:211], v[184:187], a[40:43]\n"
    "v_mfma_f32_16x16x32_bf16 a[76:79], v[208:211], v[188:191], a[76:79]\n"
    "buffer_load_dwordx4 v[124:127], v244, s[36:39], s65 offen offset:1024 sc1\n"
    "v_mfma_f32_16x16x32_bf16 a[112:115], v[208:211], v[192:195], a[112:115]\n"
    "v_mfma_f32_16x16x32_bf16 a[148:151], v[208:211], v[196:199], a[148:151]\n"
    "v_mfma_f32_16x16x32_bf16 a[184:187], v[208:211], v[200:203], a[184:187]\n"
    "buffer_load_dwordx4 v[208:211], v245, s[40:43], s57 offen sc1\n"
    "v_mfma_f32_16x16x32_bf16 a[8:11], v[212:215], v[180:183], a[8:11]\n"
    "v_mfma_f32_16x16x32_bf16 a[44:47], v[212:215], v[184:187], a[44:47]\n"
    "v_mfma_f32_16x16x32_bf16 a[80:83], v[212:215], v[188:191], a[80:83]\n"
    "buffer_load_dwordx4 v[128:131], v244, s[36:39], s65 offen offset:2048 sc1\n"
    "v_mfma_f32_16x16x32_bf16 a[116:119], v[212:215], v[192:195], a[116:119]\n"
    "v_mfma_f32_16x16x32_bf16 a[152:155], v[212:215], v[196:199], a[152:155]\n"
    "v_mfma_f32_16x16x32_bf16 a[188:191], v[212:215], v[200:203], a[188:191]\n"
    "buffer_load_dwordx4 v[212:215], v245, s[40:43], s58 offen sc1\n"
    "v_mfma_f32_16x16x32_bf16 a[12:15], v[216:219], v[180:183], a[12:15]\n"
    "v_mfma_f32_16x16x32_bf16 a[48:51], v[216:219], v[184:187], a[48:51]\n"
    "v_mfma_f32_16x16x32_bf16 a[84:87], v[216:219], v[188:191], a[84:87]\n"
    "buffer_load_dwordx4 v[132:135], v244, s[36:39], s66 offen sc1\n"
    "v_mfma_f32_16x16x32_bf16 a[120:123], v[216:219], v[192:195], a[120:123]\n"
    "v_mfma_f32_16x16x32_bf16 a[156:159], v[216:219], v[196:199], a[156:159]\n"
    "v_mfma_f32_16x16x32_bf16 a[192:195], v[216:219], v[200:203], a[192:195]\n"
    "buffer_load_dwordx4 v[216:219], v245, s[40:43], s59 offen sc1\n"
    "v_mfma_f32_16x16x32_bf16 a[16:19], v[220:223], v[180:183], a[16:19]\n"
    "v_mfma_f32_16x16x32_bf16 a[52:55], v[220:223], v[184:187], a[52:55]\n"
    "v_mfma_f32_16x16x32_bf16 a[88:91], v[220:223], v[188:191], a[88:91]\n"
    "buffer_load_dwordx4 v[136:139], v244, s[36:39], s66 offen offset:1024 sc1\n"
    "v_mfma_f32_16x16x32_bf16 a[124:127], v[220:223], v[192:195], a[124:127]\n"
    "v_mfma_f32_16x16x32_bf16 a[160:163], v[220:223], v[196:199], a[160:163]\n"
    "v_mfma_f32_16x16x32_bf16 a[196:199], v[220:223], v[200:203], a[196:199]\n"
    "buffer_load_dwordx4 v[220:223], v245, s[40:43], s60 offen sc1\n"
    "v_mfma_f32_16x16x32_bf16 a[20:23], v[224:227], v[180:183], a[20:23]\n"
    "v_mfma_f32_16x16x32_bf16 a[56:59], v[224:227], v[184:187], a[56:59]\n"
    "v_mfma_f32_16x16x32_bf16 a[92:95], v[224:227], v[188:191], a[92:95]\n"
    "buffer_load_dwordx4 v[140:143], v244, s[36:39], s66 offen offset:2048 sc1\n"
    "v_mfma_f32_16x16x32_bf16 a[128:131], v[224:227], v[192:195], a[128:131]\n"
    "v_mfma_f32_16x16x32_bf16 a[164:167], v[224:227], v[196:199], a[164:167]\n"
    "v_add_u32 v244, 0x1800, v244\n"
    "v_mfma_f32_16x16x32_bf16 a[200:203], v[224:227], v[200:203], a[200:203]\n"
    "buffer_load_dwordx4 v[224:227], v245, s[40:43], s61 offen sc1\n"
    "v_mfma_f32_16x16x32_bf16 a[24:27], v[228:231], v[180:183], a[24:27]\n"
    "v_mfma_f32_16x16x32_bf16 a[60:63], v[228:231], v[184:187], a[60:63]\n"
    "v_mfma_f32_16x16x32_bf16 a[96:99], v[228:231], v[188:191], a[96:99]\n"
    "v_mfma_f32_16x16x32_bf16 a[132:135], v[228:231], v[192:195], a[132:135]\n"
    "v_mfma_f32_16x16x32_bf16 a[168:171], v[228:231], v[196:199], a[168:171]\n"
    "v_mfma_f32_16x16x32_bf16 a[204:207], v[228:231], v[200:203], a[204:207]\n"
    "buffer_load_dwordx4 v[228:231], v245, s[40:43], s62 offen sc1\n"
    "v_mfma_f32_16x16x32_bf16 a[28:31], v[232:235], v[180:183], a[28:31]\n"
    "v_mfma_f32_16x16x32_bf16 a[64:67], v[232:235], v[184:187], a[64:67]\n"
    "v_mfma_f32_16x16x32_bf16 a[100:103], v[232:235], v[188:191], a[100:103]\n"
    "v_mfma_f32_16x16x32_bf16 a[136:139], v[232:235], v[192:195], a[136:139]\n"
    "v_mfma_f32_16x16x32_bf16 a[172:175], v[232:235], v[196:199], a[172:175]\n"
    "v_mfma_f32_16x16x32_bf16 a[208:211], v[232:235], v[200:203], a[208:211]\n"
    "buffer_load_dwordx4 v[232:235], v245, s[40:43], s63 offen sc1\n"
    "v_mfma_f32_16x16x32_bf16 a[32:35], v[236:239], v[180:183], a[32:35]\n"
    "v_mfma_f32_16x16x32_bf16 a[68:71], v[236:239], v[184:187], a[68:71]\n"
    "v_mfma_f32_16x16x32_bf16 a[104:107], v[236:239], v[188:191], a[104:107]\n"
    "v_mfma_f32_16x16x32_bf16 a[140:143], v[236:239], v[192:195], a[140:143]\n"
    "v_mfma_f32_16x16x32_bf16 a[176:179], v[236:239], v[196:199], a[176:179]\n"
    "s_waitcnt vmcnt(33)\n"
    "v_mfma_f32_16x16x32_bf16 a[212:215], v[236:239], v[200:203], a[212:215]\n"
    "buffer_load_dwordx4 v[236:239], v245, s[40:43], s64 offen sc1\n"
    "v_add_u32 v245, 0x2400, v245\n"
    "s_cmp_lg_u32 s68, 0\n"
    "s_cbranch_scc1 L_pub1%=\n"
    "s_mov_b32 s68, 1\n"
    "s_cmp_lg_u64 s[78:79], 0\n"
    "s_cbranch_scc1 L_pub1%=\n"
    "s_mov_b64 exec, 3\n"
    "global_store_dwordx2 v252, v[248:249], %[pcnt] offset:256 sc1\n"
    "s_mov_b64 exec, -1\n"
    "L_pub1%=:\n"
    "s_cmp_lg_u32 s69, 0\n"
    "s_cbranch_scc1 L_oloop%=\n"
    "s_cmp_eq_u32 s68, 2\n"
    "s_cbranch_scc1 L_otail%=\n"
    "s_mov_b32 s71, 0x40000\n"
    "L_poll1%=:\n"
    "v_cmp_ne_u64 vcc, v[246:247], v[248:249]\n"
    "s_sub_u32 s71, s71, 1\n"
    "s_cbranch_scc1 L_poll1%=_ok\n"
    "s_cbranch_vccz L_poll1%=_ok\n"
    "s_sleep 8\n"
    "global_load_dwordx2 v[246:247], v241, %[pcnt] offset:256 sc1\n"
    "s_waitcnt vmcnt(0)\n"
    "s_branch L_poll1%=\n"
    "L_poll1%=_ok:\n"
    "s_barrier\n"
    "s_mov_b32 s68, 2\n"
    "s_mov_b32 s69, 4\n"
    "s_branch L_oloop%=\n"
    "L_otail%=:\n"
    "v_mfma_f32_16x16x32_bf16 a[0:3], v[24:27], v[0:3], a[0:3]\n"
    "v_mfma_f32_16x16x32_bf16 a[36:39], v[24:27], v[4:7], a[36:39]\n"
    "v_mfma_f32_16x16x32_bf16 a[72:75], v[24:27], v[8:11], a[72:75]\n"
    "buffer_load_dwordx4 v[180:183], v244, s[36:39], s65 offen sc1\n"
    "v_mfma_f32_16x16x32_bf16 a[108:111], v[24:27], v[12:15], a[108:111]\n"
    "v_mfma_f32_16x16x32_bf16 a[144:147], v[24:27], v[16:19], a[144:147]\n"
    "v_mfma_f32_16x16x32_bf16 a[180:183], v[24:27], v[20:23], a[180:183]\n"
    "v_mfma_f32_16x16x32_bf16 a[4:7], v[28:31], v[0:3], a[4:7]\n"
    "v_mfma_f32_16x16x32_bf16 a[40:43], v[28:31], v[4:7], a[40:43]\n"
    "v_mfma_f32_16x16x32_bf16 a[76:79], v[28:31], v[8:11], a[76:79]\n"
    "buffer_load_dwordx4 v[184:187], v244, s[36:39], s65 offen offset:1024 sc1\n"
    "v_mfma_f32_16x16x32_bf16 a[112:115], v[28:31], v[12:15], a[112:115]\n"
    "v_mfma_f32_16x16x32_bf16 a[148:151], v[28:31], v[16:19], a[148:151]\n"
    "v_mfma_f32_16x16x32_bf16 a[184:187], v[28:31], v[20:23], a[184:187]\n"
    "v_mfma_f32_16x16x32_bf16 a[8:11], v[32:35], v[0:3], a[8:11]\n"
    "v_mfma_f32_16x16x32_bf16 a[44:47], v[32:35], v[4:7], a[44:47]\n"
    "v_mfma_f32_16x16x32_bf16 a[80:83], v[32:35], v[8:11], a[80:83]\n"
    "buffer_load_dwordx4 v[188:191], v244, s[36:39], s65 offen offset:2048 sc1\n"
    "v_mfma_f32_16x16x32_bf16 a[116:119], v[32:35], v[12:15], a[116:119]\n"
    "v_mfma_f32_16x16x32_bf16 a[152:155], v[32:35], v[16:19], a[152:155]\n"
    "v_mfma_f32_16x16x32_bf16 a[188:191], v[32:35], v[20:23], a[188:191]\n"
    "v_mfma_f32_16x16x32_bf16 a[12:15], v[36:39], v[0:3], a[12:15]\n"
    "v_mfma_f32_16x16x32_bf16 a[48:51], v[36:39], v[4:7], a[48:51]\n"
    "v_mfma_f32_16x16x32_bf16 a[84:87], v[36:39], v[8:11], a[84:87]\n"
    "buffer_load_dwordx4 v[192:195], v244, s[36:39], s66 offen sc1\n"
    "v_mfma_f32_16x16x32_bf16 a[120:123], v[36:39], v[12:15], a[120:123]\n"
    "v_mfma_f32_16x16x32_bf16 a[156:159], v[36:39], v[16:19], a[156:159]\n"
    "v_mfma_f32_16x16x32_bf16 a[192:195], v[36:39], v[20:23], a[192:195]\n"
    "v_mfma_f32_16x16x32_bf16 a[16:19], v[40:43], v[0:3], a[16:19]\n"
    "v_mfma_f32_16x16x32_bf16 a[52:55], v[40:43], v[4:7], a[52:55]\n"
    "v_mfma_f32_16x16x32_bf16 a[88:91], v[40:43], v[8:11], a[88:91]\n"
    "buffer_load_dwordx4 v[196:199], v244, s[36:39], s66 offen offset:1024 sc1\n"
    "v_mfma_f32_16x16x32_bf16 a[124:127], v[40:43], v[12:15], a[124:127]\n"
    "v_mfma_f32_16x16x32_bf16 a[160:163], v[40:43], v[16:19], a[160:163]\n"
    "v_mfma_f32_16x16x32_bf16 a[196:199], v[40:43], v[20:23], a[196:199]\n"
    "v_mfma_f32_16x16x32_bf16 a[20:23], v[44:47], v[0:3], a[20:23]\n"
    "v_mfma_f32_16x16x32_bf16 a[56:59], v[44:47], v[4:7], a[56:59]\n"
    "v_mfma_f32_16x16x32_bf16 a[92:95], v[44:47], v[8:11], a[92:95]\n"
    "buffer_load_dwordx4 v[200:203], v244, s[36:39], s66 offen offset:2048 sc1\n"
    "v_mfma_f32_16x16x32_bf16 a[128:131], v[44:47], v[12:15], a[128:131]\n"
    "v_mfma_f32_16x16x32_bf16 a[164:167], v[44:47], v[16:19], a[164:167]\n"
    "v_add_u32 v244, 0x1800, v244\n"
    "v_mfma_f32_16x16x32_bf16 a[200:203], v[44:47], v[20:23], a[200:203]\n"
    "v_mfma_f32_16x16x32_bf16 a[24:27], v[48:51], v[0:3], a[24:27]\n"
    "v_mfma_f32_16x16x32_bf16 a[60:63], v[48:51], v[4:7], a[60:63]\n"
    "v_mfma_f32_16x16x32_bf16 a[96:99], v[48:51], v[8:11], a[96:99]\n"
    "v_mfma_f32_16x16x32_bf16 a[132:135], v[48:51], v[12:15], a[132:135]\n"
    "v_mfma_f32_16x16x32_bf16 a[168:171], v[48:51], v[16:19], a[168:171]\n"
    "v_mfma_f32_16x16x32_bf16 a[204:207], v[48:51], v[20:23], a[204:207]\n"
    "v_mfma_f32_16x16x32_bf16 a[28:31], v[52:55], v[0:3], a[28:31]\n"
    "v_mfma_f32_16x16x32_bf16 a[64:67], v[52:55], v[4:7], a[64:67]\n"
    "v_mfma_f32_16x16x32_bf16 a[100:103], v[52:55], v[8:11], a[100:103]\n"
    "v_mfma_f32_16x16x32_bf16 a[136:139], v[52:55], v[12:15], a[136:139]\n"
    "v_mfma_f32_16x16x32_bf16 a[172:175], v[52:55], v[16:19], a[172:175]\n"
    "v_mfma_f32_16x16x32_bf16 a[208:211], v[52:55], v[20:23], a[208:211]\n"
    "v_mfma_f32_16x16x32_bf16 a[32:35], v[56:59], v[0:3], a[32:35]\n"
    "v_mfma_f32_16x16x32_bf16 a[68:71], v[56:59], v[4:7], a[68:71]\n"
    "v_mfma_f32_16x16x32_bf16 a[104:107], v[56:59], v[8:11], a[104:107]\n"
    "v_mfma_f32_16x16x32_bf16 a[140:143], v[56:59], v[12:15], a[140:143]\n"
    "v_mfma_f32_16x16x32_bf16 a[176:179], v[56:59], v[16:19], a[176:179]\n"
    "s_waitcnt vmcnt(25)\n"
    "v_mfma_f32_16x16x32_bf16 a[212:215], v[56:59], v[20:23], a[212:215]\n"
    "v_mfma_f32_16x16x32_bf16 a[0:3], v[84:87], v[60:63], a[0:3]\n"
    "v_mfma_f32_16x16x32_bf16 a[36:39], v[84:87], v[64:67], a[36:39]\n"
    "v_mfma_f32_16x16x32_bf16 a[72:75], v[84:87], v[68:71], a[72:75]\n"
    "v_mfma_f32_16x16x32_bf16 a[108:111], v[84:87], v[72:75], a[108:111]\n"
    "v_mfma_f32_16x16x32_bf16 a[144:147], v[84:87], v[76:79], a[144:147]\n"
    "v_mfma_f32_16x16x32_bf16 a[180:183], v[84:87], v[80:83], a[180:183]\n"
    "v_mfma_f32_16x16x32_bf16 a[4:7], v[88:91], v[60:63], a[4:7]\n"
    "v_mfma_f32_16x16x32_bf16 a[40:43], v[88:91], v[64:67], a[40:43]\n"
    "v_mfma_f32_16x16x32_bf16 a[76:79], v[88:91], v[68:71], a[76:79]\n"
    "v_mfma_f32_16x16x32_bf16 a[112:115], v[88:91], v[72:75], a[112:115]\n"
    "v_mfma_f32_16x16x32_bf16 a[148:151], v[88:91], v[76:79], a[148:151]\n"
    "v_mfma_f32_16x16x32_bf16 a[184:187], v[88:91], v[80:83], a[184:187]\n"
    "v_mfma_f32_16x16x32_bf16 a[8:11], v[92:95], v[60:63], a[8:11]\n"
    "v_mfma_f32_16x16x32_bf16 a[44:47], v[92:95], v[64:67], a[44:47]\n"
    "v_mfma_f32_16x16x32_bf16 a[80:83], v[92:95], v[68:71], a[80:83]\n"
    "v_mfma_f32_16x16x32_bf16 a[116:119], v[92:95], v[72:75], a[116:119]\n"
    "v_mfma_f32_16x16x32_bf16 a[152:155], v[92:95], v[76:79], a[152:155]\n"
    "v_mfma_f32_16x16x32_bf16 a[188:191], v[92:95], v[80:83], a[188:191]\n"
    "v_mfma_f32_16x16x32_bf16 a[12:15], v[96:99], v[60:63], a[12:15]\n"
    "v_mfma_f32_16x16x32_bf16 a[48:51], v[96:99], v[64:67], a[48:51]\n"
    "v_mfma_f32_16x16x32_bf16 a[84:87], v[96:99], v[68:71], a[84:87]\n"
    "v_mfma_f32_16x16x32_bf16 a[120:123], v[96:99], v[72:75], a[120:123]\n"
    "v_mfma_f32_16x16x32_bf16 a[156:159], v[96:99], v[76:79], a[156:159]\n"
    "v_mfma_f32_16x16x32_bf16 a[192:195], v[96:99], v[80:83], a[192:195]\n"
    "v_mfma_f32_16x16x32_bf16 a[16:19], v[100:103], v[60:63], a[16:19]\n"
    "v_mfma_f32_16x16x32_bf16 a[52:55], v[100:103], v[64:67], a[52:55]\n"
    "v_mfma_f32_16x16x32_bf16 a[88:91], v[100:103], v[68:71], a[88:91]\n"
    "v_mfma_f32_16x16x32_bf16 a[124:127], v[100:103], v[72:75], a[124:127]\n"
    "v_mfma_f32_16x16x32_bf16 a[160:163], v[100:103], v[76:79], a[160:163]\n"
    "v_mfma_f32_16x16x32_bf16 a[196:199], v[100:103], v[80:83], a[196:199]\n"
    "v_mfma_f32_16x16x32_bf16 a[20:23], v[104:107], v[60:63], a[20:23]\n"
    "v_mfma_f32_16x16x32_bf16 a[56:59], v[104:107], v[64:67], a[56:59]\n"
    "v_mfma_f32_16x16x32_bf16 a[92:95], v[104:107], v[68:71], a[92:95]\n"
    "v_mfma_f32_16x16x32_bf16 a[128:131], v[104:107], v[72:75], a[128:131]\n"
    "v_mfma_f32_16x16x32_bf16 a[164:167], v[104:107], v[76:79], a[164:167]\n"
    "v_mfma_f32_16x16x32_bf16 a[200:203], v[104:107], v[80:83], a[200:203]\n"
    "v_mfma_f32_16x16x32_bf16 a[24:27], v[108:111], v[60:63], a[24:27]\n"
    "v_mfma_f32_16x16x32_bf16 a[60:63], v[108:111], v[64:67], a[60:63]\n"
    "v_mfma_f32_16x16x32_bf16 a[96:99], v[108:111], v[68:71], a[96:99]\n"
    "v_mfma_f32_16x16x32_bf16 a[132:135], v[108:111], v[72:75], a[132:135]\n"
    "v_mfma_f32_16x16x32_bf16 a[168:171], v[108:111], v[76:79], a[168:171]\n"
    "v_mfma_f32_16x16x32_bf16 a[204:207], v[108:111], v[80:83], a[204:207]\n"
    "v_mfma_f32_16x16x32_bf16 a[28:31], v[112:115], v[60:63], a[28:31]\n"
    "v_mfma_f32_16x16x32_bf16 a[64:67], v[112:115], v[64:67], a[64:67]\n"
    "v_mfma_f32_16x16x32_bf16 a[100:103], v[112:115], v[68:71], a[100:103]\n"
    "v_mfma_f32_16x16x32_bf16 a[136:139], v[112:115], v[72:75], a[136:139]\n"
    "v_mfma_f32_16x16x32_bf16 a[172:175], v[112:115], v[76:79], a[172:175]\n"
    "v_mfma_f32_16x16x32_bf16 a[208:211], v[112:115], v[80:83], a[208:211]\n"
    "v_mfma_f32_16x16x32_bf16 a[32:35], v[116:119], v[60:63], a[32:35]\n"
    "v_mfma_f32_16x16x32_bf16 a[68:71], v[116:119], v[64:67], a[68:71]\n"
    "v_mfma_f32_16x16x32_bf16 a[104:107], v[116:119], v[68:71], a[104:107]\n"
    "v_mfma_f32_16x16x32_bf16 a[140:143], v[116:119], v[72:75], a[140:143]\n"
    "v_mfma_f32_16x16x32_bf16 a[176:179], v[116:119], v[76:79], a[176:179]\n"
    "s_waitcnt vmcnt(0)\n"
    "v_mfma_f32_16x16x32_bf16 a[212:215], v[116:119], v[80:83], a[212:215]\n"
    "v_mfma_f32_16x16x32_bf16 a[108:111], v[144:147], v[132:135], a[108:111]\n"
    "v_mfma_f32_16x16x32_bf16 a[108:111], v[204:207], v[192:195], a[108:111]\n"
    "v_mfma_f32_16x16x32_bf16 a[112:115], v[148:151], v[132:135], a[112:115]\n"
    "v_mfma_f32_16x16x32_bf16 a[112:115], v[208:211], v[192:195], a[112:115]\n"
    "v_mfma_f32_16x16x32_bf16 a[116:119], v[152:155], v[132:135], a[116:119]\n"
    "v_mfma_f32_16x16x32_bf16 a[116:119], v[212:215], v[192:195], a[116:119]\n"
    "v_mfma_f32_16x16x32_bf16 a[120:123], v[156:159], v[132:135], a[120:123]\n"
    "v_mfma_f32_16x16x32_bf16 a[120:123], v[216:219], v[192:195], a[120:123]\n"
    "v_mfma_f32_16x16x32_bf16 a[124:127], v[160:163], v[132:135], a[124:127]\n"
    "v_mfma_f32_16x16x32_bf16 a[124:127], v[220:223], v[192:195], a[124:127]\n"
    "v_mfma_f32_16x16x32_bf16 a[128:131], v[164:167], v[132:135], a[128:131]\n"
    "v_mfma_f32_16x16x32_bf16 a[128:131], v[224:227], v[192:195], a[128:131]\n"
    "v_mfma_f32_16x16x32_bf16 a[132:135], v[168:171], v[132:135], a[132:135]\n"
    "v_mfma_f32_16x16x32_bf16 a[132:135], v[228:231], v[192:195], a[132:135]\n"
    "v_mfma_f32_16x16x32_bf16 a[136:139], v[172:175], v[132:135], a[136:139]\n"
    "v_mfma_f32_16x16x32_bf16 a[136:139], v[232:235], v[192:195], a[136:139]\n"
    "v_mfma_f32_16x16x32_bf16 a[140:143], v[176:179], v[132:135], a[140:143]\n"
    "v_mfma_f32_16x16x32_bf16 a[140:143], v[236:239], v[192:195], a[140:143]\n"
    "v_mfma_f32_16x16x32_bf16 a[144:147], v[144:147], v[136:139], a[144:147]\n"
    "v_mfma_f32_16x16x32_bf16 a[144:147], v[204:207], v[196:199], a[144:147]\n"
    "v_mfma_f32_16x16x32_bf16 a[148:151], v[148:151], v[136:139], a[148:151]\n"
    "v_mfma_f32_16x16x32_bf16 a[148:151], v[208:211], v[196:199], a[148:151]\n"
    "v_mfma_f32_16x16x32_bf16 a[152:155], v[152:155], v[136:139], a[152:155]\n"
    "ds_write_b128 v250, a[108:111] offset:0\n"
    "v_mfma_f32_16x16x32_bf16 a[152:155], v[212:215], v[196:199], a[152:155]\n"
    "v_mfma_f32_16x16x32_bf16 a[156:159], v[156:159], v[136:139], a[156:159]\n"
    "v_mfma_f32_16x16x32_bf16 a[156:159], v[216:219], v[196:199], a[156:159]\n"
    "ds_write_b128 v250, a[112:115] offset:1024\n"
    "v_mfma_f32_16x16x32_bf16 a[160:163], v[160:163], v[136:139], a[160:163]\n"
    "v_mfma_f32_16x16x32_bf16 a[160:163], v[220:223], v[196:199], a[160:163]\n"
    "v_mfma_f32_16x16x32_bf16 a[164:167], v[164:167], v[136:139], a[164:167]\n"
    "ds_write_b128 v250, a[116:119] offset:2048\n"
    "v_mfma_f32_16x16x32_bf16 a[164:167], v[224:227], v[196:199], a[164:167]\n"
    "v_mfma_f32_16x16x32_bf16 a[168:171], v[168:171], v[136:139], a[168:171]\n"
    "v_mfma_f32_16x16x32_bf16 a[168:171], v[228:231], v[196:199], a[168:171]\n"
    "ds_write_b128 v250, a[120:123] offset:3072\n"
    "v_mfma_f32_16x16x32_bf16 a[172:175], v[172:175], v[136:139], a[172:175]\n"
    "v_mfma_f32_16x16x32_bf16 a[172:175], v[232:235], v[196:199], a[172:175]\n"
    "v_mfma_f32_16x16x32_bf16 a[176:179], v[176:179], v[136:139], a[176:179]\n"
    "ds_write_b128 v250, a[124:127] offset:4096\n"
    "v_mfma_f32_16x16x32_bf16 a[176:179], v[236:239], v[196:199], a[176:179]\n"
    "v_mfma_f32_16x16x32_bf16 a[180:183], v[144:147], v[140:143], a[180:183]\n"
    "v_mfma_f32_16x16x32_bf16 a[180:183], v[204:207], v[200:203], a[180:183]\n"
    "ds_write_b128 v250, a[128:131] offset:5120\n"
    "v_mfma_f32_16x16x32_bf16 a[184:187], v[148:151], v[140:143], a[184:187]\n"
    "v_mfma_f32_16x16x32_bf16 a[184:187], v[208:211], v[200:203], a[184:187]\n"
    "v_mfma_f32_16x16x32_bf16 a[188:191], v[152:155], v[140:143], a[188:191]\n"
    "ds_write_b128 v250, a[132:135] offset:6144\n"
    "v_mfma_f32_16x16x32_bf16 a[188:191], v[212:215], v[200:203], a[188:191]\n"
    "v_mfma_f32_16x16x32_bf16 a[192:195], v[156:159], v[140:143], a[192:195]\n"
    "v_mfma_f32_16x16x32_bf16 a[192:195], v[216:219], v[200:203], a[192:195]\n"
    "ds_write_b128 v250, a[136:139] offset:7168\n"
    "v_mfma_f32_16x16x32_bf16 a[196:199], v[160:163], v[140:143], a[196:199]\n"
    "v_mfma_f32_16x16x32_bf16 a[196:199], v[220:223], v[200:203], a[196:199]\n"
    "v_mfma_f32_16x16x32_bf16 a[200:203], v[164:167], v[140:143], a[200:203]\n"
    "ds_write_b128 v250, a[140:143] offset:8192\n"
    "v_mfma_f32_16x16x32_bf16 a[200:203], v[224:227], v[200:203], a[200:203]\n"
    "v_mfma_f32_16x16x32_bf16 a[204:207], v[168:171], v[140:143], a[204:207]\n"
    "v_mfma_f32_16x16x32_bf16 a[204:207], v[228:231], v[200:203], a[204:207]\n"
    "ds_write_b128 v250, a[144:147] offset:9216\n"
    "v_mfma_f32_16x16x32_bf16 a[208:211], v[172:175], v[140:143], a[208:211]\n"
    "v_mfma_f32_16x16x32_bf16 a[208:211], v[232:235], v[200:203], a[208:211]\n"
    "v_mfma_f32_16x16x32_bf16 a[212:215], v[176:179], v[140:143], a[212:215]\n"
    "ds_write_b128 v250, a[148:151] offset:10240\n"
    "v_mfma_f32_16x16x32_bf16 a[212:215], v[236:239], v[200:203], a[212:215]\n"
    "v_mfma_f32_16x16x32_bf16 a[0:3], v[144:147], v[120:123], a[0:3]\n"
    "v_mfma_f32_16x16x32_bf16 a[0:3], v[204:207], v[180:183], a[0:3]\n"
    "ds_write_b128 v250, a[152:155] offset:11264\n"
    "v_mfma_f32_16x16x32_bf16 a[4:7], v[148:151], v[120:123], a[4:7]\n"
    "v_mfma_f32_16x16x32_bf16 a[4:7], v[208:211], v[180:183], a[4:7]\n"
    "v_mfma_f32_16x16x32_bf16 a[8:11], v[152:155], v[120:123], a[8:11]\n"
    "ds_write_b128 v250, a[156:159] offset:12288\n"
    "v_mfma_f32_16x16x32_bf16 a[8:11], v[212:215], v[180:183], a[8:11]\n"
    "v_mfma_f32_16x16x32_bf16 a[12:15], v[156:159], v[120:123], a[12:15]\n"
    "v_mfma_f32_16x16x32_bf16 a[12:15], v[216:219], v[180:183], a[12:15]\n"
    "ds_write_b128 v250, a[160:163] offset:13312\n"
    "v_mfma_f32_16x16x32_bf16 a[16:19], v[160:163], v[120:123], a[16:19]\n"
    "v_accvgpr_read_b32 v0, a0\n"
    "v_accvgpr_read_b32 v1, a1\n"
    "v_mfma_f32_16x16x32_bf16 a[16:19], v[220:223], v[180:183], a[16:19]\n"
    "v_accvgpr_read_b32 v2, a2\n"
    "v_accvgpr_read_b32 v3, a3\n"
    "v_mfma_f32_16x16x32_bf16 a[20:23], v[164:167], v[120:123], a[20:23]\n"
    "ds_write_b128 v250, a[164:167] offset:14336\n"
    "v_accvgpr_read_b32 v4, a4\n"
    "v_accvgpr_read_b32 v5, a5\n"
    "v_mfma_f32_16x16x32_bf16 a[20:23], v[224:227], v[180:183], a[20:23]\n"
    "v_accvgpr_read_b32 v6, a6\n"
    "v_accvgpr_read_b32 v7, a7\n"
    "v_mfma_f32_16x16x32_bf16 a[24:27], v[168:171], v[120:123], a[24:27]\n"
    "v_accvgpr_read_b32 v8, a8\n"
    "v_accvgpr_read_b32 v9, a9\n"
    "v_mfma_f32_16x16x32_bf16 a[24:27], v[228:231], v[180:183], a[24:27]\n"
    "ds_write_b128 v250, a[168:171] offset:15360\n"
    "v_accvgpr_read_b32 v10, a10\n"
    "v_accvgpr_read_b32 v11, a11\n"
    "v_mfma_f32_16x16x32_bf16 a[28:31], v[172:175], v[120:123], a[28:31]\n"
    "v_accvgpr_read_b32 v12, a12\n"
    "v_accvgpr_read_b32 v13, a13\n"
    "v_mfma_f32_16x16x32_bf16 a[28:31], v[232:235], v[180:183], a[28:31]\n"
    "v_accvgpr_read_b32 v14, a14\n"
    "v_accvgpr_read_b32 v15, a15\n"
    "v_mfma_f32_16x16x32_bf16 a[32:35], v[176:179], v[120:123], a[32:35]\n"
    "ds_write_b128 v250, a[172:175] offset:16384\n"
    "v_accvgpr_read_b32 v16, a16\n"
    "v_accvgpr_read_b32 v17, a17\n"
    "v_mfma_f32_16x16x32_bf16 a[32:35], v[236:239], v[180:183], a[32:35]\n"
    "v_accvgpr_read_b32 v18, a18\n"
    "v_accvgpr_read_b32 v19, a19\n"
    "v_mfma_f32_16x16x32_bf16 a[36:39], v[144:147], v[124:127], a[36:39]\n"
    "v_accvgpr_read_b32 v20, a20\n"
    "v_accvgpr_read_b32 v21, a21\n"
    "v_mfma_f32_16x16x32_bf16 a[36:39], v[204:207], v[184:187], a[36:39]\n"
    "ds_write_b128 v250, a[176:179] offset:17408\n"
    "v_accvgpr_read_b32 v22, a22\n"
    "v_accvgpr_read_b32 v23, a23\n"
    "v_mfma_f32_16x16x32_bf16 a[40:43], v[148:151], v[124:127], a[40:43]\n"
    "v_accvgpr_read_b32 v24, a24\n"
    "v_accvgpr_read_b32 v25, a25\n"
    "v_mfma_f32_16x16x32_bf16 a[40:43], v[208:211], v[184:187], a[40:43]\n"
    "v_accvgpr_read_b32 v26, a26\n"
    "v_accvgpr_read_b32 v27, a27\n"
    "v_mfma_f32_16x16x32_bf16 a[44:47], v[152:155], v[124:127], a[44:47]\n"
    "ds_write_b128 v250, a[180:183] offset:18432\n"
    "v_accvgpr_read_b32 v28, a28\n"
    "v_accvgpr_read_b32 v29, a29\n"
    "v_mfma_f32_16x16x32_bf16 a[44:47], v[212:215], v[184:187], a[44:47]\n"
    "v_accvgpr_read_b32 v30, a30\n"
    "v_accvgpr_read_b32 v31, a31\n"
    "v_mfma_f32_16x16x32_bf16 a[48:51], v[156:159], v[124:127], a[48:51]\n"
    "v_accvgpr_read_b32 v32, a32\n"
    "v_accvgpr_read_b32 v33, a33\n"
    "v_mfma_f32_16x16x32_bf16 a[48:51], v[216:219], v[184:187], a[48:51]\n"
    "ds_write_b128 v250, a[184:187] offset:19456\n"
    "v_accvgpr_read_b32 v34, a34\n"
    "v_accvgpr_read_b32 v35, a35\n"
    "v_mfma_f32_16x16x32_bf16 a[52:55], v[160:163], v[124:127], a[52:55]\n"
    "v_accvgpr_read_b32 v36, a36\n"
    "v_accvgpr_read_b32 v37, a37\n"
    "v_mfma_f32_16x16x32_bf16 a[52:55], v[220:223], v[184:187], a[52:55]\n"
    "v_accvgpr_read_b32 v38, a38\n"
    "v_accvgpr_read_b32 v39, a39\n"
    "v_mfma_f32_16x16x32_bf16 a[56:59], v[164:167], v[124:127], a[56:59]\n"
    "ds_write_b128 v250, a[188:191] offset:20480\n"
    "v_accvgpr_read_b32 v40, a40\n"
    "v_accvgpr_read_b32 v41, a41\n"
    "v_mfma_f32_16x16x32_bf16 a[56:59], v[224:227], v[184:187], a[56:59]\n"
    "v_accvgpr_read_b32 v42, a42\n"
    "v_accvgpr_read_b32 v43, a43\n"
    "v_mfma_f32_16x16x32_bf16 a[60:63], v[168:171], v[124:127], a[60:63]\n"
    "v_accvgpr_read_b32 v44, a44\n"
    "v_accvgpr_read_b32 v45, a45\n"
    "v_mfma_f32_16x16x32_bf16 a[60:63], v[228:231], v[184:187], a[60:63]\n"
    "ds_write_b128 v250, a[192:195] offset:21504\n"
    "v_accvgpr_read_b32 v46, a46\n"
    "v_accvgpr_read_b32 v47, a47\n"
    "v_mfma_f32_16x16x32_bf16 a[64:67], v[172:175], v[124:127], a[64:67]\n"
    "v_accvgpr_read_b32 v48, a48\n"
    "v_accvgpr_read_b32 v49, a49\n"
    "v_mfma_f32_16x16x32_bf16 a[64:67], v[232:235], v[184:187], a[64:67]\n"
    "v_accvgpr_read_b32 v50, a50\n"
    "v_accvgpr_read_b32 v51, a51\n"
    "v_mfma_f32_16x16x32_bf16 a[68:71], v[176:179], v[124:127], a[68:71]\n"
    "ds_write_b128 v250, a[196:199] offset:22528\n"
    "v_accvgpr_read_b32 v52, a52\n"
    "v_accvgpr_read_b32 v53, a53\n"
    "v_mfma_f32_16x16x32_bf16 a[68:71], v[236:239], v[184:187], a[68:71]\n"
    "v_accvgpr_read_b32 v54, a54\n"
    "v_accvgpr_read_b32 v55, a55\n"
    "v_mfma_f32_16x16x32_bf16 a[72:75], v[144:147], v[128:131], a[72:75]\n"
    "v_accvgpr_read_b32 v56, a56\n"
    "v_accvgpr_read_b32 v57, a57\n"
    "v_mfma_f32_16x16x32_bf16 a[72:75], v[204:207], v[188:191], a[72:75]\n"
    "ds_write_b128 v250, a[200:203] offset:23552\n"
    "v_accvgpr_read_b32 v58, a58\n"
    "v_accvgpr_read_b32 v59, a59\n"
    "v_mfma_f32_16x16x32_bf16 a[76:79], v[148:151], v[128:131], a[76:79]\n"
    "v_accvgpr_read_b32 v60, a60\n"
    "v_accvgpr_read_b32 v61, a61\n"
    "v_mfma_f32_16x16x32_bf16 a[76:79], v[208:211], v[188:191], a[76:79]\n"
    "v_accvgpr_read_b32 v62, a62\n"
    "v_accvgpr_read_b32 v63, a63\n"
    "v_mfma_f32_16x16x32_bf16 a[80:83], v[152:155], v[128:131], a[80:83]\n"
    "ds_write_b128 v250, a[204:207] offset:24576\n"
    "v_accvgpr_read_b32 v64, a64\n"
    "v_accvgpr_read_b32 v65, a65\n"
    "v_mfma_f32_16x16x32_bf16 a[80:83], v[212:215], v[188:191], a[80:83]\n"
    "v_accvgpr_read_b32 v66, a66\n"
    "v_accvgpr_read_b32 v67, a67\n"
    "v_mfma_f32_16x16x32_bf16 a[84:87], v[156:159], v[128:131], a[84:87]\n"
    "v_accvgpr_read_b32 v68, a68\n"
    "v_accvgpr_read_b32 v69, a69\n"
    "v_mfma_f32_16x16x32_bf16 a[84:87], v[216:219], v[188:191], a[84:87]\n"
    "ds_write_b128 v250, a[208:211] offset:25600\n"
    "v_accvgpr_read_b32 v70, a70\n"
    "v_accvgpr_read_b32 v71, a71\n"
    "v_mfma_f32_16x16x32_bf16 a[88:91], v[160:163], v[128:131], a[88:91]\n"
    "v_accvgpr_read_b32 v72, a72\n"
    "v_accvgpr_read_b32 v73, a73\n"
    "v_mfma_f32_16x16x32_bf16 a[88:91], v[220:223], v[188:191], a[88:91]\n"
    "v_accvgpr_read_b32 v74, a74\n"
    "v_accvgpr_read_b32 v75, a75\n"
    "v_mfma_f32_16x16x32_bf16 a[92:95], v[164:167], v[128:131], a[92:95]\n"
    "ds_write_b128 v250, a[212:215] offset:26624\n"
    "v_accvgpr_read_b32 v76, a76\n"
    "v_accvgpr_read_b32 v77, a77\n"
    "v_mfma_f32_16x16x32_bf16 a[92:95], v[224:227], v[188:191], a[92:95]\n"
    "v_accvgpr_read_b32 v78, a78\n"
    "v_accvgpr_read_b32 v79, a79\n"
    "v_mfma_f32_16x16x32_bf16 a[96:99], v[168:171], v[128:131], a[96:99]\n"
    "v_accvgpr_read_b32 v80, a80\n"
    "v_accvgpr_read_b32 v81, a81\n"
    "v_mfma_f32_16x16x32_bf16 a[96:99], v[228:231], v[188:191], a[96:99]\n"
    "v_accvgpr_read_b32 v82, a82\n"
    "v_accvgpr_read_b32 v83, a83\n"
    "v_mfma_f32_16x16x32_bf16 a[100:103], v[172:175], v[128:131], a[100:103]\n"
    "v_accvgpr_read_b32 v84, a84\n"
    "v_accvgpr_read_b32 v85, a85\n"
    "v_mfma_f32_16x16x32_bf16 a[100:103], v[232:235], v[188:191], a[100:103]\n"
    "v_accvgpr_read_b32 v86, a86\n"
    "v_accvgpr_read_b32 v87, a87\n"
    "v_mfma_f32_16x16x32_bf16 a[104:107], v[176:179], v[128:131], a[104:107]\n"
    "v_mfma_f32_16x16x32_bf16 a[104:107], v[236:239], v[188:191], a[104:107]\n"
    "s_waitcnt lgkmcnt(0)\n"
    "s_barrier\n"
    "ds_read_b128 v[124:127], v251 offset:4096\n"
    "ds_read_b128 v[128:131], v251 offset:5120\n"
    "ds_read_b128 v[132:135], v251 offset:6144\n"
    "ds_read_b128 v[136:139], v251 offset:7168\n"
    "ds_read_b128 v[140:143], v251 offset:8192\n"
    "ds_read_b128 v[160:163], v251 offset:13312\n"
    "ds_read_b128 v[164:167], v251 offset:14336\n"
    "ds_read_b128 v[168:171], v251 offset:15360\n"
    "ds_read_b128 v[172:175], v251 offset:16384\n"
    "v_accvgpr_read_b32 v88, a88\n"
    "v_accvgpr_read_b32 v89, a89\n"
    "v_accvgpr_read_b32 v90, a90\n"
    "v_accvgpr_read_b32 v91, a91\n"
    "v_accvgpr_read_b32 v92, a92\n"
    "v_accvgpr_read_b32 v93, a93\n"
    "v_accvgpr_read_b32 v94, a94\n"
    "v_accvgpr_read_b32 v95, a95\n"
    "v_accvgpr_read_b32 v96, a96\n"
    "v_accvgpr_read_b32 v97, a97\n"
    "v_accvgpr_read_b32 v98, a98\n"
    "v_accvgpr_read_b32 v99, a99\n"
    "v_accvgpr_read_b32 v100, a100\n"
    "v_accvgpr_read_b32 v101, a101\n"
    "v_accvgpr_read_b32 v102, a102\n"
    "v_accvgpr_read_b32 v103, a103\n"
    "v_accvgpr_read_b32 v104, a104\n"
    "v_accvgpr_read_b32 v105, a105\n"
    "v_accvgpr_read_b32 v106, a106\n"
    "v_accvgpr_read_b32 v107, a107\n"
    "s_xor_b32 s71, %[wv], 3\n"
    "s_mul_i32 s71, s71, 27648\n"
    "v_add_u32 v245, s71, v242\n"
    "s_mul_i32 s70, %[wv], 27648\n"
    "s_and_b32 s71, %[wv], 1\n"
    "s_lshl_b32 s71, s71, 7\n"
    "s_add_u32 s70, s70, s71\n"
    "s_add_u32 s70, s70, 0x4000\n"
    "v_bfe_u32 v244, v255, 0, 4\n"
    "v_mul_u32_u24 v244, 160, v244\n"
    "v_bfe_u32 v243, v255, 4, 2\n"
    "v_lshl_add_u32 v244, v243, 3, v244\n"
    "v_add_u32 v244, s70, v244\n"
    "ds_read_b128 v[176:179], v251 offset:17408\n"
    "ds_read_b128 v[196:199], v251 offset:22528\n"
    "ds_read_b128 v[200:203], v251 offset:23552\n"
    "ds_read_b128 v[204:207], v251 offset:24576\n"
    "ds_read_b128 v[208:211], v251 offset:25600\n"
    "ds_read_b128 v[212:215], v251 offset:26624\n"
    "ds_read_b128 v[108:111], v251 offset:0\n"
    "ds_read_b128 v[112:115], v251 offset:1024\n"
    "ds_read_b128 v[116:119], v251 offset:2048\n"
    "s_waitcnt lgkmcnt(9)\n"
    "v_pk_add_f32 v[16:17], v[16:17], v[124:125]\n"
    "v_pk_add_f32 v[18:19], v[18:19], v[126:127]\n"
    "v_pk_add_f32 v[20:21], v[20:21], v[128:129]\n"
    "v_pk_add_f32 v[22:23], v[22:23], v[130:131]\n"
    "v_pk_add_f32 v[24:25], v[24:25], v[132:133]\n"
    "v_pk_add_f32 v[26:27], v[26:27], v[134:135]\n"
    "v_pk_add_f32 v[28:29], v[28:29], v[136:137]\n"
    "v_pk_add_f32 v[30:31], v[30:31], v[138:139]\n"
    "v_pk_add_f32 v[32:33], v[32:33], v[140:141]\n"
    "v_pk_add_f32 v[34:35], v[34:35], v[142:143]\n"
    "v_pk_add_f32 v[52:53], v[52:53], v[160:161]\n"
    "v_pk_add_f32 v[54:55], v[54:55], v[162:163]\n"
    "v_pk_add_f32 v[56:57], v[56:57], v[164:165]\n"
    "v_pk_add_f32 v[58:59], v[58:59], v[166:167]\n"
    "v_pk_add_f32 v[60:61], v[60:61], v[168:169]\n"
    "v_pk_add_f32 v[62:63], v[62:63], v[170:171]\n"
    "v_pk_add_f32 v[64:65], v[64:65], v[172:173]\n"
    "v_pk_add_f32 v[66:67], v[66:67], v[174:175]\n"
    "ds_read_b128 v[120:123], v251 offset:3072\n"
    "ds_read_b128 v[144:147], v251 offset:9216\n"
    "ds_read_b128 v[148:151], v251 offset:10240\n"
    "ds_read_b128 v[152:155], v251 offset:11264\n"
    "ds_read_b128 v[156:159], v251 offset:12288\n"
    "ds_read_b128 v[180:183], v251 offset:18432\n"
    "ds_read_b128 v[184:187], v251 offset:19456\n"
    "ds_read_b128 v[188:191], v251 offset:20480\n"
    "ds_read_b128 v[192:195], v251 offset:21504\n"
    "s_waitcnt lgkmcnt(9)\n"
    "v_pk_add_f32 v[68:69], v[68:69], v[176:177]\n"
    "v_pk_add_f32 v[70:71], v[70:71], v[178:179]\n"
    "v_pk_add_f32 v[88:89], v[88:89], v[196:197]\n"
    "v_pk_add_f32 v[90:91], v[90:91], v[198:199]\n"
    "v_pk_add_f32 v[92:93], v[92:93], v[200:201]\n"
    "v_pk_add_f32 v[94:95], v[94:95], v[202:203]\n"
    "v_pk_add_f32 v[96:97], v[96:97], v[204:205]\n"
    "v_pk_add_f32 v[98:99], v[98:99], v[206:207]\n"
    "v_pk_add_f32 v[100:101], v[100:101], v[208:209]\n"
    "v_pk_add_f32 v[102:103], v[102:103], v[210:211]\n"
    "v_pk_add_f32 v[104:105], v[104:105], v[212:213]\n"
    "v_pk_add_f32 v[106:107], v[106:107], v[214:215]\n"
    "v_pk_add_f32 v[0:1], v[0:1], v[108:109]\n"
    "v_pk_add_f32 v[2:3], v[2:3], v[110:111]\n"
    "v_pk_add_f32 v[4:5], v[4:5], v[112:113]\n"
    "v_pk_add_f32 v[6:7], v[6:7], v[114:115]\n"
    "v_pk_add_f32 v[8:9], v[8:9], v[116:117]\n"
    "v_pk_add_f32 v[10:11], v[10:11], v[118:119]\n"
    "s_waitcnt lgkmcnt(0)\n"
    "ds_write_b128 v251, v[16:19] offset:0\n"
    "ds_write_b128 v251, v[20:23] offset:1024\n"
    "ds_write_b128 v251, v[24:27] offset:2048\n"
    "ds_write_b128 v251, v[28:31] offset:3072\n"
    "ds_write_b128 v251, v[32:35] offset:4096\n"
    "ds_write_b128 v251, v[52:55] offset:5120\n"
    "ds_write_b128 v251, v[56:59] offset:6144\n"
    "ds_write_b128 v251, v[60:63] offset:7168\n"
    "ds_write_b128 v251, v[64:67] offset:8192\n"
    "ds_write_b128 v251, v[68:71] offset:9216\n"
    "ds_write_b128 v251, v[88:91] offset:10240\n"
    "ds_write_b128 v251, v[92:95] offset:11264\n"
    "ds_write_b128 v251, v[96:99] offset:12288\n"
    "ds_write_b128 v251, v[100:103] offset:13312\n"
    "ds_write_b128 v251, v[104:107] offset:14336\n"
    "v_pk_add_f32 v[12:13], v[12:13], v[120:121]\n"
    "v_pk_add_f32 v[14:15], v[14:15], v[122:123]\n"
    "v_pk_add_f32 v[36:37], v[36:37], v[144:145]\n"
    "v_pk_add_f32 v[38:39], v[38:39], v[146:147]\n"
    "v_pk_add_f32 v[40:41], v[40:41], v[148:149]\n"
    "v_pk_add_f32 v[42:43], v[42:43], v[150:151]\n"
    "v_pk_add_f32 v[44:45], v[44:45], v[152:153]\n"
    "v_pk_add_f32 v[46:47], v[46:47], v[154:155]\n"
    "v_pk_add_f32 v[48:49], v[48:49], v[156:157]\n"
    "v_pk_add_f32 v[50:51], v[50:51], v[158:159]\n"
    "v_pk_add_f32 v[72:73], v[72:73], v[180:181]\n"
    "v_pk_add_f32 v[74:75], v[74:75], v[182:183]\n"
    "v_pk_add_f32 v[76:77], v[76:77], v[184:185]\n"
    "v_pk_add_f32 v[78:79], v[78:79], v[186:187]\n"
    "v_pk_add_f32 v[80:81], v[80:81], v[188:189]\n"
    "v_pk_add_f32 v[82:83], v[82:83], v[190:191]\n"
    "v_pk_add_f32 v[84:85], v[84:85], v[192:193]\n"
    "v_pk_add_f32 v[86:87], v[86:87], v[194:195]\n"
    "s_bitcmp1_b32 %[wv], 0\n"
    "s_cselect_b32 s70, -32, 32\n"
    "v_mov_b32 v220, v244\n"
    "v_add_u32 v221, s70, v220\n"
    "v_add_u32 v222, s70, v221\n"
    "v_add_u32 v223, s70, v222\n"
    "v_add_u32 v224, s70, v223\n"
    "s_waitcnt lgkmcnt(0)\n"
    "s_barrier\n"
    "ds_read_b128 v[108:111], v245 offset:4096\n"
    "ds_read_b128 v[112:115], v245 offset:3072\n"
    "ds_read_b128 v[116:119], v245 offset:2048\n"
    "ds_read_b128 v[120:123], v245 offset:1024\n"
    "ds_read_b128 v[124:127], v245 offset:0\n"
    "ds_read_b128 v[128:131], v245 offset:9216\n"
    "ds_read_b128 v[132:135], v245 offset:8192\n"
    "ds_read_b128 v[136:139], v245 offset:7168\n"
    "ds_read_b128 v[140:143], v245 offset:6144\n"
    "ds_read_b128 v[144:147], v245 offset:5120\n"
    "ds_read_b128 v[148:151], v245 offset:14336\n"
    "ds_read_b128 v[152:155], v245 offset:13312\n"
    "ds_read_b128 v[156:159], v245 offset:12288\n"
    "ds_read_b128 v[160:163], v245 offset:11264\n"
    "ds_read_b128 v[164:167], v245 offset:10240\n"
    "s_waitcnt lgkmcnt(10)\n"
    "v_pk_add_f32 v[0:1], v[0:1], v[108:109]\n"
    "v_pk_add_f32 v[2:3], v[2:3], v[110:111]\n"
    "v_cvt_pk_bf16_f32 v180, v0, v1\n"
    "v_cvt_pk_bf16_f32 v181, v2, v3\n"
    "ds_write_b64 v220, v[180:181] offset:0\n"
    "v_pk_add_f32 v[4:5], v[4:5], v[112:113]\n"
    "v_pk_add_f32 v[6:7], v[6:7], v[114:115]\n"
    "v_cvt_pk_bf16_f32 v182, v4, v5\n"
    "v_cvt_pk_bf16_f32 v183, v6, v7\n"
    "ds_write_b64 v221, v[182:183] offset:0\n"
    "v_pk_add_f32 v[8:9], v[8:9], v[116:117]\n"
    "v_pk_add_f32 v[10:11], v[10:11], v[118:119]\n"
    "v_cvt_pk_bf16_f32 v184, v8, v9\n"
    "v_cvt_pk_bf16_f32 v185, v10, v11\n"
    "ds_write_b64 v222, v[184:185] offset:0\n"
    "v_pk_add_f32 v[12:13], v[12:13], v[120:121]\n"
    "v_pk_add_f32 v[14:15], v[14:15], v[122:123]\n"
    "v_cvt_pk_bf16_f32 v186, v12, v13\n"
    "v_cvt_pk_bf16_f32 v187, v14, v15\n"
    "ds_write_b64 v223, v[186:187] offset:0\n"
    "v_pk_add_f32 v[16:17], v[16:17], v[124:125]\n"
    "v_pk_add_f32 v[18:19], v[18:19], v[126:127]\n"
    "v_cvt_pk_bf16_f32 v188, v16, v17\n"
    "v_cvt_pk_bf16_f32 v189, v18, v19\n"
    "ds_write_b64 v224, v[188:189] offset:0\n"
    "s_waitcnt lgkmcnt(10)\n"
    "v_pk_add_f32 v[36:37], v[36:37], v[128:129]\n"
    "v_pk_add_f32 v[38:39], v[38:39], v[130:131]\n"
    "v_cvt_pk_bf16_f32 v190, v36, v37\n"
    "v_cvt_pk_bf16_f32 v191, v38, v39\n"
    "ds_write_b64 v220, v[190:191] offset:2560\n"
    "v_pk_add_f32 v[40:41], v[40:41], v[132:133]\n"
    "v_pk_add_f32 v[42:43], v[42:43], v[134:135]\n"
    "v_cvt_pk_bf16_f32 v192, v40, v41\n"
    "v_cvt_pk_bf16_f32 v193, v42, v43\n"
    "ds_write_b64 v221, v[192:193] offset:2560\n"
    "v_pk_add_f32 v[44:45], v[44:45], v[136:137]\n"
    "v_pk_add_f32 v[46:47], v[46:47], v[138:139]\n"
    "v_cvt_pk_bf16_f32 v194, v44, v45\n"
    "v_cvt_pk_bf16_f32 v195, v46, v47\n"
    "ds_write_b64 v222, v[194:195] offset:2560\n"
    "v_pk_add_f32 v[48:49], v[48:49], v[140:141]\n"
    "v_pk_add_f32 v[50:51], v[50:51], v[142:143]\n"
    "v_cvt_pk_bf16_f32 v196, v48, v49\n"
    "v_cvt_pk_bf16_f32 v197, v50, v51\n"
    "ds_write_b64 v223, v[196:197] offset:2560\n"
    "v_pk_add_f32 v[52:53], v[52:53], v[144:145]\n"
    "v_pk_add_f32 v[54:55], v[54:55], v[146:147]\n"
    "v_cvt_pk_bf16_f32 v198, v52, v53\n"
    "v_cvt_pk_bf16_f32 v199, v54, v55\n"
    "ds_write_b64 v224, v[198:199] offset:2560\n"
    "s_waitcnt lgkmcnt(10)\n"
    "v_pk_add_f32 v[72:73], v[72:73], v[148:149]\n"
    "v_pk_add_f32 v[74:75], v[74:75], v[150:151]\n"
    "v_cvt_pk_bf16_f32 v200, v72, v73\n"
    "v_cvt_pk_bf16_f32 v201, v74, v75\n"
    "ds_write_b64 v220, v[200:201] offset:5120\n"
    "v_pk_add_f32 v[76:77], v[76:77], v[152:153]\n"
    "v_pk_add_f32 v[78:79], v[78:79], v[154:155]\n"
    "v_cvt_pk_bf16_f32 v202, v76, v77\n"
    "v_cvt_pk_bf16_f32 v203, v78, v79\n"
    "ds_write_b64 v221, v[202:203] offset:5120\n"
    "v_pk_add_f32 v[80:81], v[80:81], v[156:157]\n"
    "v_pk_add_f32 v[82:83], v[82:83], v[158:159]\n"
    "v_cvt_pk_bf16_f32 v204, v80, v81\n"
    "v_cvt_pk_bf16_f32 v205, v82, v83\n"
    "ds_write_b64 v222, v[204:205] offset:5120\n"
    "v_pk_add_f32 v[84:85], v[84:85], v[160:161]\n"
    "v_pk_add_f32 v[86:87], v[86:87], v[162:163]\n"
    "v_cvt_pk_bf16_f32 v206, v84, v85\n"
    "v_cvt_pk_bf16_f32 v207, v86, v87\n"
    "ds_write_b64 v223, v[206:207] offset:5120\n"
    "v_pk_add_f32 v[88:89], v[88:89], v[164:165]\n"
    "v_pk_add_f32 v[90:91], v[90:91], v[166:167]\n"
    "v_cvt_pk_bf16_f32 v208, v88, v89\n"
    "v_cvt_pk_bf16_f32 v209, v90, v91\n"
    "ds_write_b64 v224, v[208:209] offset:5120\n"
    "s_waitcnt lgkmcnt(0)\n"
    "ds_read_b128 v[0:3], v250 offset:16384\n"
    "ds_read_b128 v[4:7], v250 offset:17344\n"
    "ds_read_b128 v[8:11], v250 offset:18304\n"
    "ds_read_b128 v[12:15], v250 offset:19264\n"
    "ds_read_b128 v[16:19], v250 offset:20224\n"
    "ds_read_b128 v[20:23], v250 offset:21184\n"
    "ds_read_b128 v[24:27], v250 offset:22144\n"
    "ds_read_b128 v[28:31], v250 offset:23104\n"
    "s_add_u32 s70, s67, 0\n"
    "s_waitcnt lgkmcnt(7)\n"
    "buffer_store_dwordx4 v[0:3], v253, s[44:47], s70 offen\n"
    "s_add_u32 s70, s67, 13824\n"
    "s_waitcnt lgkmcnt(6)\n"
    "buffer_store_dwordx4 v[4:7], v253, s[44:47], s70 offen\n"
    "s_add_u32 s70, s67, 27648\n"
    "s_waitcnt lgkmcnt(5)\n"
    "buffer_store_dwordx4 v[8:11], v253, s[44:47], s70 offen\n"
    "s_add_u32 s70, s67, 41472\n"
    "s_waitcnt lgkmcnt(4)\n"
    "buffer_store_dwordx4 v[12:15], v253, s[44:47], s70 offen\n"
    "s_add_u32 s70, s67, 55296\n"
    "s_waitcnt lgkmcnt(3)\n"
    "buffer_store_dwordx4 v[16:19], v253, s[44:47], s70 offen\n"
    "s_add_u32 s70, s67, 69120\n"
    "s_waitcnt lgkmcnt(2)\n"
    "buffer_store_dwordx4 v[20:23], v253, s[44:47], s70 offen\n"
    "s_add_u32 s70, s67, 82944\n"
    "s_waitcnt lgkmcnt(1)\n"
    "buffer_store_dwordx4 v[24:27], v253, s[44:47], s70 offen\n"
    "s_add_u32 s70, s67, 96768\n"
    "s_waitcnt lgkmcnt(0)\n"
    "buffer_store_dwordx4 v[28:31], v253, s[44:47], s70 offen\n"
    "s_waitcnt vmcnt(0)\n"
    :
    : [pa] "s"(pa), [pb] "s"(pb), [pc] "s"(pc), [pcnt] "s"(pcnt), [wv] "s"(w), [co] "s"(co), [cw] "s"(cw), [tlo] "s"(tlo), [thi] "s"(thi)
    :
      "v0", "v1", "v2", "v3", "v4", "v5", "v6", "v7", "v8", "v9", "v10", "v11", "v12", "v13", "v14", "v15", "v16", "v17", "v18", "v19", "v20", "v21", "v22", "v23",
      "v24", "v25", "v26", "v27", "v28", "v29", "v30", "v31", "v32", "v33", "v34", "v35", "v36", "v37", "v38", "v39", "v40", "v41", "v42", "v43", "v44", "v45", "v46", "v47",
      "v48", "v49", "v50", "v51", "v52", "v53", "v54", "v55", "v56", "v57", "v58", "v59", "v60", "v61", "v62", "v63", "v64", "v65", "v66", "v67", "v68", "v69", "v70", "v71",
      "v72", "v73", "v74", "v75", "v76", "v77", "v78", "v79", "v80", "v81", "v82", "v83", "v84", "v85", "v86", "v87", "v88", "v89", "v90", "v91", "v92", "v93", "v94", "v95",
      "v96", "v97", "v98", "v99", "v100", "v101", "v102", "v103", "v104", "v105", "v106", "v107", "v108", "v109", "v110", "v111", "v112", "v113", "v114", "v115", "v116", "v117", "v118", "v119",
      "v120", "v121", "v122", "v123", "v124", "v125", "v126", "v127", "v128", "v129", "v130", "v131", "v132", "v133", "v134", "v135", "v136", "v137", "v138", "v139", "v140", "v141", "v142", "v143",
      "v144", "v145", "v146", "v147", "v148", "v149", "v150", "v151", "v152", "v153", "v154", "v155", "v156", "v157", "v158", "v159", "v160", "v161", "v162", "v163", "v164", "v165", "v166", "v167",
      "v168", "v169", "v170", "v171", "v172", "v173", "v174", "v175", "v176", "v177", "v178", "v179", "v180", "v181", "v182", "v183", "v184", "v185", "v186", "v187", "v188", "v189", "v190", "v191",
      "v192", "v193", "v194", "v195", "v196", "v197", "v198", "v199", "v200", "v201", "v202", "v203", "v204", "v205", "v206", "v207", "v208", "v209", "v210", "v211", "v212", "v213", "v214", "v215",
      "v216", "v217", "v218", "v219", "v220", "v221", "v222", "v223", "v224", "v225", "v226", "v227", "v228", "v229", "v230", "v231", "v232", "v233", "v234", "v235", "v236", "v237", "v238", "v239",
      "v240", "v241", "v242", "v243", "v244", "v245", "v246", "v247", "v248", "v249", "v250", "v251", "v252", "v253", "v254", "v255", "a0", "a1", "a2", "a3", "a4", "a5", "a6", "a7",
      "a8", "a9", "a10", "a11", "a12", "a13", "a14", "a15", "a16", "a17", "a18", "a19", "a20", "a21", "a22", "a23", "a24", "a25", "a26", "a27", "a28", "a29", "a30", "a31",
      "a32", "a33", "a34", "a35", "a36", "a37", "a38", "a39", "a40", "a41", "a42", "a43", "a44", "a45", "a46", "a47", "a48", "a49", "a50", "a51", "a52", "a53", "a54", "a55",
      "a56", "a57", "a58", "a59", "a60", "a61", "a62", "a63", "a64", "a65", "a66", "a67", "a68", "a69", "a70", "a71", "a72", "a73", "a74", "a75", "a76", "a77", "a78", "a79",
      "a80", "a81", "a82", "a83", "a84", "a85", "a86", "a87", "a88", "a89", "a90", "a91", "a92", "a93", "a94", "a95", "a96", "a97", "a98", "a99", "a100", "a101", "a102", "a103",
      "a104", "a105", "a106", "a107", "a108", "a109", "a110", "a111", "a112", "a113", "a114", "a115", "a116", "a117", "a118", "a119", "a120", "a121", "a122", "a123", "a124", "a125", "a126", "a127",
      "a128", "a129", "a130", "a131", "a132", "a133", "a134", "a135", "a136", "a137", "a138", "a139", "a140", "a141", "a142", "a143", "a144", "a145", "a146", "a147", "a148", "a149", "a150", "a151",
      "a152", "a153", "a154", "a155", "a156", "a157", "a158", "a159", "a160", "a161", "a162", "a163", "a164", "a165", "a166", "a167", "a168", "a169", "a170", "a171", "a172", "a173", "a174", "a175",
      "a176", "a177", "a178", "a179", "a180", "a181", "a182", "a183", "a184", "a185", "a186", "a187", "a188", "a189", "a190", "a191", "a192", "a193", "a194", "a195", "a196", "a197", "a198", "a199",
      "a200", "a201", "a202", "a203", "a204", "a205", "a206", "a207", "a208", "a209", "a210", "a211", "a212", "a213", "a214", "a215", "a216", "a217", "a218", "a219", "a220", "a221", "a222", "a223",
      "a224", "a225", "a226", "a227", "a228", "a229", "a230", "a231", "a232", "a233", "a234", "a235", "a236", "a237", "a238", "a239", "a240", "a241", "a242", "a243", "a244", "a245", "a246", "a247",
      "a248", "a249", "a250", "a251", "a252", "a253", "a254", "a255", "s36", "s37", "s38", "s39", "s40", "s41", "s42", "s43", "s44", "s45", "s46", "s47", "s48", "s49", "s50", "s51",
      "s52", "s53", "s54", "s55", "s56", "s57", "s58", "s59", "s60", "s61", "s62", "s63", "s64", "s65", "s66", "s67", "s68", "s69", "s70", "s71", "s72", "s73", "s74", "s75",
      "s76", "s77", "s78", "s79", "s80", "s81", "s82", "s83", "s84", "s85", "s86", "s87", "s88", "s89", "s90", "s91", "s92", "s93", "vcc", "scc", "memory"
    );
  }
  if ((bid & 0xfb) == 0 && w == 0) {
    unsigned lane; char* fc = pcnt; unsigned fe = eo;
    asm volatile("v_mbcnt_lo_u32_b32 %0, -1, 0\n\tv_mbcnt_hi_u32_b32 %0, -1, %0" : "=v"(lane), "+s"(fc), "+s"(fe));
    const u64 tag = __hip_atomic_load((u64*)(fc + fe), __ATOMIC_RELAXED, __HIP_MEMORY_SCOPE_AGENT) + 1;
    const char* fl = fc + 512 + (256 + (fe >> 3) + (lane >> 5)) * 512 + 256 + (lane & 31) * 8;
    for (int spin = 0; spin < 0x40000; ++spin) {
      bool ok = true;
#pragma unroll
      for (int i = 0; i < 8; ++i) ok &= __hip_atomic_load((const u64*)(fl + 1024 * i), __ATOMIC_RELAXED, __HIP_MEMORY_SCOPE_AGENT) == tag;
      if (__all(ok)) break;
      __builtin_amdgcn_s_sleep(8);
    }
    if (lane == 0) __hip_atomic_store((u64*)(fc + fe), tag, __ATOMIC_RELAXED, __HIP_MEMORY_SCOPE_AGENT);
  }
}

extern "C" void kernel_launch(void* const* d_in, const int* in_sizes, int n_in, void* d_out, int out_size, void* d_ws, size_t ws_size, hipStream_t stream) {
  Ptrs P;
  for (int i = 0; i < 2; ++i) { P.x[i] = d_in[5 * i + 0]; P.k[i] = d_in[5 * i + 1]; P.v[i] = d_in[5 * i + 2]; P.wq[i] = d_in[5 * i + 3]; P.wo[i] = d_in[5 * i + 4]; }
  char* cnt = (char*)d_ws;
  char* ows = (char*)d_ws + 262144;
  char* wot = ows + (size_t)2 * NB * SQ * HD * 2;
  (void)hipFuncSetAttribute((const void*)k1_fused, hipFuncAttributeMaxDynamicSharedMemorySize, K_LDS);
  hipLaunchKernelGGL(k1_fused, dim3(256), dim3(256), K_LDS, stream, P, ows, wot, cnt, (char*)d_out);
}
```
